```python
import torch

def _softmax(x, axis=-1):
    return torch.softmax(x, dim=axis)

def _gelu(x):
    return torch.nn.functional.gelu(x, approximate='tanh')

def _silu(x):
    return torch.nn.functional.silu(x)

def _sigmoid(x):
    return torch.sigmoid(x)

def _layernorm(x, axis=-1, eps=1e-05):
    m = x.mean(dim=axis, keepdim=True)
    v = x.var(dim=axis, keepdim=True, unbiased=False)
    return (x - m) / torch.sqrt(v + eps)

def _scatter_add(tgt, idx, src, axis=0):
    return tgt.clone().index_add_(axis, idx, src)

def _segment_sum(src, seg, n):
    out = torch.zeros((n,) + src.shape[1:], dtype=src.dtype)
    return out.index_add_(0, seg, src)

def _pad(x, pw):
    flat = tuple((v for pair in reversed(pw) for v in pair))
    return torch.nn.functional.pad(x, flat)
kernel_name = 'standalone__conformer_family_branchformer__speech'

def setup_inputs():

    _w = lambda a: (lambda t: t.to(torch.bfloat16) if t.is_floating_point() else t)(torch.as_tensor(a))
    B, T, D, K = (2, 4000, 144, 31)
    s = float(D) ** (-0.5)
    return {'x': _w((lambda *s: torch.randn(*s, dtype=torch.bfloat16) * 0.05)(B, T, D)), 'w_q': _w((lambda *s: torch.randn(*s, dtype=torch.bfloat16) * 1.6)(D, D) * s), 'w_k': _w((lambda *s: torch.randn(*s, dtype=torch.bfloat16) * 1.6)(D, D) * s), 'w_v': _w((lambda *s: torch.randn(*s, dtype=torch.bfloat16) * 0.8)(D, D) * s), 'w_ao': _w((lambda *s: torch.randn(*s, dtype=torch.bfloat16) * 0.8)(D, D) * s), 'w_up': _w((lambda *s: torch.randn(*s, dtype=torch.bfloat16) * 1.6)(D, 2 * D) * s), 'dw': _w((lambda *s: torch.randn(*s, dtype=torch.bfloat16) * 1.6)(K, D) * (1.0 / K)), 'w_dn': _w((lambda *s: torch.randn(*s, dtype=torch.bfloat16) * 0.8)(D, D) * s), 'w_merge': _w((lambda *s: torch.randn(*s, dtype=torch.bfloat16) * 0.8)(2 * D, D) * s)}

def reference(x, w_q, w_k, w_v, w_ao, w_up, dw, w_dn, w_merge):
    B, T, D = x.shape
    K = dw.shape[0]
    xa = _layernorm(x, axis=-1)
    q = torch.einsum('btd,de->bte', xa, w_q)
    k = torch.einsum('btd,de->bte', xa, w_k)
    v = torch.einsum('btd,de->bte', xa, w_v)
    logits = torch.einsum('btd,bsd->bts', q, k) * float(D) ** (-0.5)
    attn = _softmax(logits, axis=-1)
    ya = torch.einsum('bts,bsd->btd', attn, v)
    ya = torch.einsum('btd,de->bte', ya, w_ao)
    xc = _layernorm(x, axis=-1)
    h = _gelu(torch.einsum('btd,de->bte', xc, w_up))
    u = h[..., :D].clone()
    g = _layernorm(h[..., D:], axis=-1)
    pad = K // 2
    gp = _pad(g, ((0, 0), (pad, pad), (0, 0)))
    conv = torch.zeros_like(g)
    for i in range(K):
        conv = conv + gp[:, i:i + T, :] * dw[i][None, None, :]
    yc = torch.einsum('btd,de->bte', u * conv, w_dn)
    merged = torch.concatenate([ya, yc], axis=-1)
    out = torch.einsum('btf,fd->btd', merged, w_merge)
    return x + out
```

```cpp
#include <hip/hip_runtime.h>
#include <hip/hip_bf16.h>
typedef __hip_bfloat16 bf16;
constexpr int B=2,T=4000,D=144,KW=31;
__device__ inline float b2f(bf16 x){return __bfloat162float(x);}
__global__ void k_ln(const bf16* x, float* xa){
  int r=blockIdx.x; __shared__ float s[D];
  int t=threadIdx.x; if(t<D) s[t]=b2f(x[r*D+t]); __syncthreads();
  float m=0;
#pragma unroll 4
 for(int i=0;i<D;i++) m+=s[i]; m/=D; float v=0;
#pragma unroll 4
 for(int i=0;i<D;i++){float d=s[i]-m; v+=d*d;} v/=D;
  if(t<D) xa[r*D+t]=(s[t]-m)/sqrtf(v+1e-5f);
}
__global__ void k_mm(const float* A,int lda,const bf16* W,int ldw,int K,int N,float* C,int ldc,int act){
  int r=blockIdx.x; __shared__ float s[288];
  for(int i=threadIdx.x;i<K;i+=blockDim.x) s[i]=A[r*lda+i]; __syncthreads();
  for(int n=threadIdx.x;n<N;n+=blockDim.x){ float a=0; for(int k=0;k<K;k++) a+=s[k]*b2f(W[k*ldw+n]);
    if(act){ float u=0.7978845608f*(a+0.044715f*a*a*a); a=0.5f*a*(1.f+tanhf(u)); }
    C[r*ldc+n]=a; }
}
__global__ void k_attn(const float* q,const float* k,const float* v,float* o){
  int r=blockIdx.x; int b=r/T; __shared__ float p[T]; __shared__ float sq[D]; __shared__ float red[256];
  int t=threadIdx.x; if(t<D) sq[t]=q[r*D+t]; __syncthreads();
  float mx=-1e30f;
  for(int s=t;s<T;s+=256){ const float* kk=k+(b*T+s)*D; float a=0; for(int d=0;d<D;d++) a+=sq[d]*kk[d]; a*=(1.f/12.f); p[s]=a; mx=fmaxf(mx,a);}
  red[t]=mx; __syncthreads(); for(int o2=128;o2>0;o2>>=1){ if(t<o2) red[t]=fmaxf(red[t],red[t+o2]); __syncthreads(); }
  mx=red[0]; __syncthreads(); float sm=0; for(int s=t;s<T;s+=256){ float e=expf(p[s]-mx); p[s]=e; sm+=e;} red[t]=sm; __syncthreads();
  for(int o2=128;o2>0;o2>>=1){ if(t<o2) red[t]+=red[t+o2]; __syncthreads(); }
  sm=red[0];
  if(t<D){ float a=0; for(int s=0;s<T;s++) a+=p[s]*v[(b*T+s)*D+t]; o[r*D+t]=a/sm; }
}
__global__ void k_lng(const float* h, float* g){
  int r=blockIdx.x; __shared__ float s[D];
  int t=threadIdx.x; if(t<D) s[t]=h[r*2*D+D+t]; __syncthreads();
  float m=0;
#pragma unroll 4
 for(int i=0;i<D;i++) m+=s[i]; m/=D; float v=0;
#pragma unroll 4
 for(int i=0;i<D;i++){float d=s[i]-m; v+=d*d;} v/=D;
  if(t<D) g[r*D+t]=(s[t]-m)/sqrtf(v+1e-5f);
}
__global__ void k_conv(const float* h,const float* g,const bf16* dw,float* uc){
  int r=blockIdx.x; int b=r/T, tt=r%T; int t=threadIdx.x; if(t>=D) return;
  float a=0; for(int i=0;i<KW;i++){ int s=tt+i-15; if(s>=0&&s<T) a+=g[(b*T+s)*D+t]*b2f(dw[i*D+t]); }
  uc[r*D+t]=a*h[r*2*D+t];
}
__global__ void k_fin(const bf16* x,const float* o,bf16* out,int n){ int i=blockIdx.x*256+threadIdx.x; if(i<n) out[i]=__float2bfloat16(b2f(x[i])+o[i]); }
extern "C" void kernel_launch(void* const* d_in, const int* in_sizes, int n_in, void* d_out, int out_size, void* d_ws, size_t ws_size, hipStream_t stream){
  const bf16 *x=(const bf16*)d_in[0],*wq=(const bf16*)d_in[1],*wk=(const bf16*)d_in[2],*wv=(const bf16*)d_in[3],*wao=(const bf16*)d_in[4],*wup=(const bf16*)d_in[5],*dw=(const bf16*)d_in[6],*wdn=(const bf16*)d_in[7],*wm=(const bf16*)d_in[8];
  float* ws=(float*)d_ws; int R=B*T; size_t S=(size_t)R*D;
  float *xa=ws,*q=ws+S,*k=ws+2*S,*v=ws+3*S,*ya=ws+4*S,*h=ws+5*S,*g=ws+7*S,*uc=ws+8*S,*mg=ws+9*S,*o=ws+11*S;
  k_ln<<<R,192,0,stream>>>(x,xa);
  k_mm<<<R,192,0,stream>>>(xa,D,wq,D,D,D,q,D,0);
  k_mm<<<R,192,0,stream>>>(xa,D,wk,D,D,D,k,D,0);
  k_mm<<<R,192,0,stream>>>(xa,D,wv,D,D,D,v,D,0);
  k_attn<<<R,256,0,stream>>>(q,k,v,ya);
  k_mm<<<R,192,0,stream>>>(ya,D,wao,D,D,D,mg,2*D,0);
  k_mm<<<R,320,0,stream>>>(xa,D,wup,2*D,D,2*D,h,2*D,1);
  k_lng<<<R,192,0,stream>>>(h,g);
  k_conv<<<R,192,0,stream>>>(h,g,dw,uc);
  k_mm<<<R,192,0,stream>>>(uc,D,wdn,D,D,D,mg+D,2*D,0);
  k_mm<<<R,192,0,stream>>>(mg,2*D,wm,D,2*D,D,o,D,0);
  k_fin<<<(R*D+255)/256,256,0,stream>>>(x,o,(bf16*)d_out,R*D);
}
```

```cpp
#include <hip/hip_runtime.h>
#include <stdint.h>

typedef unsigned short u16;
typedef unsigned long long u64;
typedef float f32x2 __attribute__((ext_vector_type(2)));
typedef float f32x4 __attribute__((ext_vector_type(4)));
typedef float f32x16 __attribute__((ext_vector_type(16)));
typedef short s16x4 __attribute__((ext_vector_type(4)));
typedef __bf16 bf16x8 __attribute__((ext_vector_type(8)));
typedef __bf16 bf16x2 __attribute__((ext_vector_type(2)));
typedef unsigned u32x2 __attribute__((ext_vector_type(2)));
typedef unsigned u32x4 __attribute__((ext_vector_type(4)));
typedef unsigned u32x16 __attribute__((ext_vector_type(16)));
typedef __amdgpu_buffer_rsrc_t rsrc_t;
#define DI __device__ __forceinline__
#define GAS __attribute__((address_space(1)))
#define LAS __attribute__((address_space(3)))
typedef LAS char* lptr;

constexpr int D = 144, NT = 125;
constexpr float BETA = 0.5f;
constexpr int TILEB = 9216;
constexpr int QSTR = 1216;
constexpr int MATB = 41472;
constexpr unsigned CODE_BYTES = 36864;
constexpr unsigned OOB = 0x40000000u, SENT = 0x7f8a5c3eu;
constexpr unsigned WS_EPOCH = 0;
constexpr unsigned WS_FA = 1024;
constexpr unsigned WS_FK = 4096;
constexpr unsigned WS_FS = 65536;
constexpr unsigned WS_KT = 262144;
constexpr unsigned WS_G2 = WS_KT + 256 * TILEB;
constexpr unsigned WS_WV = WS_G2 + 256 * 9728;
constexpr unsigned WS_WD = WS_WV + MATB;
constexpr unsigned WS_AW = WS_WD + MATB;
constexpr unsigned WS_XC = WS_AW + MATB + 8192;
constexpr unsigned WS_END = WS_XC + 256 * TILEB;
constexpr int L_SLOT = 36864, L_GW = 110592, L_SW = L_GW, L_U = 130048, L_T0 = L_U, L_T1 = 139776, L_T2 = 150016, L_ST = 159232, L_SM = L_T1, L_OI = L_GW;

DI unsigned cvtpk(float lo, float hi) { f32x2 v = {lo, hi}; return __builtin_bit_cast(unsigned, __builtin_convertvector(v, bf16x2)); }
DI float blo(unsigned x) { return __uint_as_float(x << 16); }
DI float bhi(unsigned x) { return __uint_as_float(x & 0xffff0000u); }
DI unsigned rfl(unsigned x) { return __builtin_amdgcn_readfirstlane(x); }
DI rsrc_t mkrs(const void* p, unsigned n) { return __builtin_amdgcn_make_buffer_rsrc((void*)p, (short)0, (int)n, 0x00020000); }
DI f32x16 mm32(u32x4 a, u32x4 b, f32x16 c) { return __builtin_amdgcn_mfma_f32_32x32x16_bf16(__builtin_bit_cast(bf16x8, a), __builtin_bit_cast(bf16x8, b), c, 0, 0, 0); }
DI f32x4 mm16(u32x4 a, u32x4 b, f32x4 c) { return __builtin_amdgcn_mfma_f32_16x16x32_bf16(__builtin_bit_cast(bf16x8, a), __builtin_bit_cast(bf16x8, b), c, 0, 0, 0); }
DI f32x4 mm4(u32x2 a, u32x2 b, f32x4 c) { return __builtin_amdgcn_mfma_f32_4x4x4bf16_1k(__builtin_bit_cast(s16x4, a), __builtin_bit_cast(s16x4, b), c, 0, 0, 0); }
DI u32x4 ld16(lptr p) { return *(LAS u32x4*)p; }
DI u32x2 ld8(lptr p) { return *(LAS u32x2*)p; }
DI u32x4 ld8z(lptr p) { u32x2 a = ld8(p); return u32x4{a[0], a[1], 0u, 0u}; }
DI void st16(lptr p, u32x4 v) { *(LAS u32x4*)p = v; }
DI void st8(lptr p, u32x2 v) { *(LAS u32x2*)p = v; }
DI u32x2 ldtr(lptr p) { return __builtin_bit_cast(u32x2, __builtin_amdgcn_ds_read_tr16_b64_v4i16((LAS s16x4*)p)); }
DI u32x4 ldtr2(lptr p, int o) { u32x2 a = ldtr(p), b = ldtr(p + o); return u32x4{a[0], a[1], b[0], b[1]}; }
DI u32x4 ldtrz(lptr p) { u32x2 a = ldtr(p); return u32x4{a[0], a[1], 0u, 0u}; }
DI int aoff(int row, int ch) { return 512 * ch + 16 * (row ^ ((ch & 3) << 2)); }
DI u32x2 pk4(float a, float b, float c, float d) { return u32x2{cvtpk(a, b), cvtpk(c, d)}; }
DI float gelu(float a) {
  float p = __builtin_fmaf(a * a, -2.f * 1.4426950408889634f * 0.7978845608028654f * 0.044715f, -2.f * 1.4426950408889634f * 0.7978845608028654f);
  return a * __builtin_amdgcn_rcpf(1.f + __builtin_amdgcn_exp2f(a * p));
}
DI float xhalf(float v) { auto r = __builtin_amdgcn_permlane32_swap(__float_as_uint(v), __float_as_uint(v), false, false); return __uint_as_float(r[0]) + __uint_as_float(r[1]); }
DI float xrow(float v) { auto r = __builtin_amdgcn_permlane16_swap(__float_as_uint(v), __float_as_uint(v), false, false); return __uint_as_float(r[0]) + __uint_as_float(r[1]); }
template <int CTRL> DI float dppadd(float v) { return v + __uint_as_float(__builtin_amdgcn_update_dpp(0, __float_as_uint(v), CTRL, 0xf, 0xf, true)); }
DI float red8(float v) { v = dppadd<0xB1>(v); v = dppadd<0x4E>(v); return dppadd<0x141>(v); }
#define VMW(n) asm volatile("s_waitcnt vmcnt(" #n ")" ::: "memory")
#define BAR() asm volatile("s_waitcnt lgkmcnt(0)\n\ts_barrier" ::: "memory")
#define SB() __builtin_amdgcn_sched_barrier(0)
DI void dma(rsrc_t r, unsigned voff, unsigned soff, unsigned lds) {
  asm volatile("s_mov_b32 m0, %0\n\ts_nop 0\n\tbuffer_load_dwordx4 %1, %2, %3 offen lds" :: "s"(lds), "v"(voff), "s"(r), "s"(soff) : "memory"); }
DI void dma_c(rsrc_t r, unsigned voff, unsigned soff, unsigned lds) {
  asm volatile("s_mov_b32 m0, %0\n\ts_nop 0\n\tbuffer_load_dwordx4 %1, %2, %3 offen sc1 lds" :: "s"(lds), "v"(voff), "s"(r), "s"(soff) : "memory"); }
DI void setflag(char* p, u64 tag) { __hip_atomic_store((GAS u64*)p, tag, __ATOMIC_RELAXED, __HIP_MEMORY_SCOPE_AGENT); }
DI void waitflag(const char* p, u64 tag) {
#pragma unroll 1
  for (unsigned s = 0; s < (1u << 16); ++s) {
    u64 v = __hip_atomic_load((GAS u64*)p, __ATOMIC_RELAXED, __HIP_MEMORY_SCOPE_AGENT);
    if (__all(v == tag)) break;
    __builtin_amdgcn_s_sleep(1);
  }
  __builtin_amdgcn_fence(__ATOMIC_ACQUIRE, "wavefront");
}
DI u32x4 bld16(rsrc_t r, unsigned off) { return __builtin_amdgcn_raw_buffer_load_b128(r, off, 0, 16); }
DI u32x2 bld8(rsrc_t r, unsigned off) { return __builtin_amdgcn_raw_buffer_load_b64(r, off, 0, 16); }
DI void bst16(rsrc_t r, unsigned off, u32x4 v) { __builtin_amdgcn_raw_buffer_store_b128(v, r, off, 0, 16); }
DI void bst8(rsrc_t r, unsigned off, u32x2 v) { __builtin_amdgcn_raw_buffer_store_b64(v, r, off, 0, 16); }
template <bool SWAP> DI void mmul(const u32x4 (&a)[9], const u32x4 (&b)[9], const u32x4 (&as)[5], const u32x4 (&bs)[5], f32x16& big, f32x4& sml) {
  big = f32x16{}; sml = f32x4{};
#pragma unroll
  for (int s = 0; s < 9; ++s) {
    big = SWAP ? mm32(b[s], a[s], big) : mm32(a[s], b[s], big);
    if (s < 5) sml = SWAP ? mm16(bs[s], as[s], sml) : mm16(as[s], bs[s], sml);
  }
}

DI void dma_mat(const void* src, unsigned n, unsigned lds, int w, int lane) {
  rsrc_t r = mkrs(src, n);
#pragma unroll 1
  for (unsigned i = w; 1024 * i < n; i += 4) dma(r, lane * 16, 1024 * i, lds + 1024 * i);
}
DI unsigned woff(int ch, int n) {
  return n < 128 ? ((n >> 5) * 9 + (ch >> 1)) * 1024 + ((ch & 1) * 32 + (n & 31)) * 16 : 36864 + (ch >> 2) * 1024 + ((ch & 3) * 16 + (n - 128)) * 16; }
DI unsigned woff8(int ch, int hf, int n) { return (n < 128 || ch < 16) ? woff(ch, n) + 8 * hf : 40960 + ((2 * (ch - 16) + hf) * 16 + (n - 128)) * 8; }
DI void aux_wg(int ai, const u16* wv, const u16* wao, const u16* wdn, const u16* wm, const u16* dw, char* ws, lptr L) {
  const int tid = threadIdx.x, lane = tid & 63, w = rfl(tid >> 6), r = lane & 31, h = lane >> 5, g = lane >> 4, q = (lane & 15) >> 2, p = lane & 3;
  constexpr int R1 = 0, R2 = 41984, R3 = 83968;
  const int nt = (ai & 1) * 4 + w, n = 32 * nt + r;
  dma_mat(ai < 2 ? wao : wdn, MATB, R1, w, lane); dma_mat(ai < 2 ? wm : wm + D * D, MATB, R2, w, lane);
  dma_mat(ai < 2 ? wv : dw, ai < 2 ? MATB : 31 * 288, R3, w, lane);
  VMW(0); BAR();
  if (ai == 3 && w > 0) {
    const rsrc_t ra = mkrs(ws + WS_AW, MATB);
#pragma unroll 1
    for (int G = w - 1; G < 9; G += 3) {
      unsigned v[18];
#pragma unroll
      for (int j = 0; j < 18; ++j) { unsigned t2[2];
#pragma unroll
        for (int y = 0; y < 2; ++y) { const int tap = 2 * j + y - p - 1; const unsigned e = *(LAS u16*)(L + R3 + 288 * (tap < 0 ? 0 : (tap > 30 ? 30 : tap)) + 2 * (16 * G + (lane >> 2))); t2[y] = (tap >= 0 && tap < 31) ? e : 0u; }
        v[j] = t2[0] | (t2[1] << 16); }
#pragma unroll
      for (int k = 0; k < 4; ++k) bst16(ra, 4608 * G + 1024 * k + lane * 16, u32x4{v[4 * k], v[4 * k + 1], v[4 * k + 2], v[4 * k + 3]});
      bst8(ra, 4608 * G + 4096 + lane * 8, u32x2{v[16], v[17]});
    }
  }
  if (nt < 5) {
    int col = 32 * nt + 16 * (g & 1) + 4 * p; if (col > 140) col = 140;
    u32x4 b[9];
#pragma unroll
    for (int s = 0; s < 9; ++s) b[s] = ldtr2(L + R2 + 288 * (16 * s + 8 * h + q) + 2 * col, 288 * 4);
    const unsigned big = n < D ? 0u : OOB;
    rsrc_t ro = mkrs(ws + (ai < 2 ? WS_WV : WS_WD), MATB);
    u32x4 b2[10];
#pragma unroll
    for (int c = 0; c < 5; ++c) {
      int row = 32 * c + r; if (row > 143) row = 143;
      f32x16 y = {};
#pragma unroll
      for (int s = 0; s < 9; ++s) y = mm32(ld16(L + R1 + 288 * row + 32 * s + 16 * h), b[s], y);
      if (ai >= 2) y *= 12.f / 1.4426950408889634f;
#pragma unroll
      for (int ks = 0; ks < 2; ++ks) { const int o = 8 * ks; b2[2 * c + ks] = u32x4{cvtpk(y[o], y[o + 1]), cvtpk(y[o + 2], y[o + 3]), cvtpk(y[o + 4], y[o + 5]), cvtpk(y[o + 6], y[o + 7])}; }
    }
    if (ai >= 2) {
#pragma unroll
      for (int ch = 0; ch < 18; ++ch) bst8(ro, big + woff8(ch, h, n), (ch & 1) ? u32x2{b2[ch >> 1][2], b2[ch >> 1][3]} : u32x2{b2[ch >> 1][0], b2[ch >> 1][1]});
    } else {
      f32x16 v[5]; float cs = 0.f;
#pragma unroll
      for (int c = 0; c < 5; ++c) {
        int row = 32 * c + r; if (row > 143) row = 143;
        v[c] = f32x16{};
#pragma unroll
        for (int s = 0; s < 9; ++s) { lptr pa = L + R3 + 288 * row + 32 * s + 8 * h; u32x2 a0 = ld8(pa), a1 = ld8(pa + 16); v[c] = mm32(u32x4{a0[0], a0[1], a1[0], a1[1]}, b2[s], v[c]); }
#pragma unroll
        for (int e = 0; e < (c < 4 ? 16 : 8); ++e) cs += v[c][e];
      }
      cs = xhalf(cs) * (1.f / D);
#pragma unroll
      for (int c = 0; c < 5; ++c) {
        const f32x16 y = v[c] - cs;
        u32x4 e0 = {cvtpk(y[0], y[1]), cvtpk(y[2], y[3]), cvtpk(y[4], y[5]), cvtpk(y[6], y[7])}, e1 = {cvtpk(y[8], y[9]), cvtpk(y[10], y[11]), cvtpk(y[12], y[13]), cvtpk(y[14], y[15])};
        if (c < 4) { bst16(ro, big + woff(4 * c + h, n), u32x4{e0[0], e0[1], e1[0], e1[1]}); bst16(ro, big + woff(4 * c + 2 + h, n), u32x4{e0[2], e0[3], e1[2], e1[3]}); }
        else { bst8(ro, big + woff8(16, h, n), u32x2{e0[0], e0[1]}); bst8(ro, big + woff8(17, h, n), u32x2{e0[2], e0[3]}); }
      }
    }
  }
  VMW(0); BAR();
}

DI int fresh_lane() { int l; asm volatile("v_mbcnt_lo_u32_b32 %0, -1, 0\n\tv_mbcnt_hi_u32_b32 %0, -1, %0" : "=v"(l)); return l; }
#define LANEVARS(l) const int lane = (l), tid = 64 * w + lane, r = lane & 31, h = lane >> 5, g = lane >> 4, q = (lane & 15) >> 2, p = lane & 3, i16 = lane & 15, tk = 16 * z + i16; (void)tid; (void)r; (void)h; (void)g; (void)q; (void)p; (void)tk;

__global__ __launch_bounds__(256, 1) void branchformer(const u16* __restrict__ x, const u16* __restrict__ wq, const u16* __restrict__ wk, const u16* __restrict__ wv,
    const u16* __restrict__ wao, const u16* __restrict__ wup, const u16* __restrict__ dw, const u16* __restrict__ wdn, const u16* __restrict__ wm, char* __restrict__ ws, u16* __restrict__ out) {
  extern __shared__ __attribute__((aligned(16))) char lds_[];
  lptr L = (lptr)lds_;
  const int w = rfl(threadIdx.x >> 6), z = w & 1;
  const int bid = blockIdx.x, xg = bid & 7, b = xg >> 2, half = xg & 1, pr = ((xg >> 1) & 1) * 32 + (bid >> 3);
  const int tile = 2 * pr + half;
  const int ct = tile < NT ? tile : NT - 1, pt = (tile ^ 1) < NT ? (tile ^ 1) : NT - 1;
  const int gt = b * NT + ct, fi = b * 128 + tile;
  const rsrc_t rw = mkrs(ws, WS_END);
  if (tile >= 126) {
    const int ai = 2 * b + half, tid = threadIdx.x;
    const u64 tag = __hip_atomic_load((GAS u64*)(ws + WS_EPOCH), __ATOMIC_RELAXED, __HIP_MEMORY_SCOPE_AGENT) + 1ull;
    aux_wg(ai, wv, wao, wdn, wm, dw, ws, L);
    if (tid == 0) { setflag(ws + WS_FA + 128 * ai, tag); setflag(ws + WS_FK + 128 * fi, tag); }
    if (ai == 0) {
      waitflag(ws + WS_FK + 128 * tid, tag);
      BAR();
      if (tid == 0) setflag(ws + WS_EPOCH, tag);
    }
    return;
  }
  f32x16 cac; f32x4 csc; u32x4 sr;
  {
    asm volatile(
    ".p2align 3\n\t"
    "s_load_dwordx4 s[68:71], s[6:7], 0x0\n\t"
    "s_load_dwordx2 s[28:29], s[6:7], 0x10\n\t"
    "s_load_dwordx2 s[84:85], s[6:7], 0x28\n\t"
    "s_load_dwordx4 s[72:75], s[6:7], 0x48\n\t"
    "s_getpc_b64 s[92:93]\n\t"
    "v_mbcnt_lo_u32_b32 v0, -1, 0\n\t"
    "s_lshr_b32 s19, s8, 3\n\t"
    "v_mbcnt_hi_u32_b32 v0, -1, v0\n\t"
    "s_lshl_b32 s19, s19, 2\n\t"
    "v_mov_b32 v21, 1073741824\n\t"
    "s_add_u32 s19, s19, s60\n\t"
    "v_lshlrev_b32_e64 v20, 7, v0\n\t"
    "s_mul_i32 s19, s19, 512\n\t"
    "v_cmp_gt_u32 vcc, 4, v0\n\t"
    "s_and_b32 s93, s93, 0xffff\n\t"
    "s_mov_b32 s94, 40960\n\t"
    "s_mov_b32 s95, 0x20000\n\t"
    "v_cndmask_b32 v22, v21, v20, vcc\n\t"
    "s_mov_b32 m0, 150016\n\t"
    "s_nop 0\n\t"
    "buffer_load_dword v22, s[92:95], s19 offen lds\n\t"
    "s_lshr_b32 s13, s8, 3\n\t"
    "s_lshl_b32 s19, s8, 5\n\t"
    "s_and_b32 s19, s19, 64\n\t"
    "s_lshl_b32 s9, s13, 1\n\t"
    "s_add_u32 s9, s9, s19\n\t"
    "s_and_b32 s34, s8, 1\n\t"
    "s_add_u32 s9, s9, s34\n\t"
    "s_min_u32 s10, s9, 124\n\t"
    "s_bfe_u32 s19, s8, 0x10002\n\t"
    "s_lshl_b32 s12, s19, 7\n\t"
    "s_mul_i32 s19, s19, 125\n\t"
    "s_add_u32 s11, s10, s19\n\t"
    "s_mul_i32 s14, s11, 9216\n\t"
    "s_mul_i32 s61, s60, 9216\n\t"
    "s_lshr_b32 s20, s61, 2\n\t"
    "s_add_u32 s15, s14, s20\n\t"
    "s_lshr_b32 s17, s61, 1\n\t"
    "s_add_u32 s17, s17, 79872\n\t"
    "v_lshlrev_b32_e64 v117, 4, v0\n\t"
    "s_mov_b32 m0, s17\n\t"
    "s_xor_b32 s16, s9, 1\n\t"
    "s_min_u32 s16, s16, 124\n\t"
    "s_add_u32 s16, s16, s19\n\t"
    "s_mul_i32 s16, s16, 9216\n\t"
    "s_add_u32 s16, s16, s20\n\t"
    "s_lshl_b32 s13, s13, 2\n\t"
    "s_add_u32 s13, s13, s60\n\t"
    "s_cmp_lt_u32 s13, 21\n\t"
    "s_cselect_b32 s21, 0, 21\n\t"
    "s_cselect_b32 s23, 0, 1\n\t"
    "s_cmp_lt_u32 s13, 42\n\t"
    "s_cselect_b32 s21, s21, 42\n\t"
    "s_cselect_b32 s23, s23, 2\n\t"
    "s_cselect_b32 s22, 1, 2\n\t"
    "s_mul_i32 s22, s22, 41472\n\t"
    "s_sub_u32 s21, s13, s21\n\t"
    "s_lshl_b32 s21, s21, 11\n\t"
    "v_cmp_gt_u32 vcc, 16, v0\n\t"
    "s_mov_b32 s94, s22\n\t"
    "s_nop 0\n\t"
    "v_cndmask_b32 v20, v21, v20, vcc\n\t"
    "s_sub_u32 s18, 64, s60\n\t"
    "s_mul_i32 s18, s18, s34\n\t"
    "s_lshl_b32 s20, s60, 4\n\t"
    "s_add_u32 s18, s18, s20\n\t"
    "s_add_u32 s19, s19, s18\n\t"
    "s_mul_i32 s64, s19, 9216\n\t"
    "s_add_u32 s64, s64, 260096\n\t"
    "s_sub_u32 s19, 124, s18\n\t"
    "s_lshl_b32 s35, s19, 7\n\t"
    "s_mul_i32 s19, s19, 9216\n\t"
    "s_add_u32 s66, s19, 11264\n\t"
    "s_cmp_eq_u32 s60, 0\n\t"
    "s_cselect_b32 s33, s19, 1073741824\n\t"
    "s_cmp_eq_u32 s34, 0\n\t"
    "s_cselect_b32 s79, 138240, s33\n\t"
    "s_mul_i32 s55, s60, 10368\n\t"
    "s_lshr_b32 s56, s61, 3\n\t"
    "s_mul_i32 s77, s60, 9472\n\t"
    "s_mov_b32 s27, 0x20000\n\t"
    "s_mov_b32 s31, 0x20000\n\t"
    "s_mov_b32 s67, 0x20000\n\t"
    "s_mov_b32 s83, 0x20000\n\t"
    "s_mov_b32 s87, 0x20000\n\t"
    "s_mov_b32 s91, 0x20000\n\t"
    "s_mov_b32 s26, 41472\n\t"
    "s_mov_b32 s30, 41472\n\t"
    "s_mov_b32 s86, 82944\n\t"
    "s_mov_b32 s82, 7603712\n\t"
    "s_add_u32 s19, s12, s9\n\t"
    "s_lshl_b32 s36, s19, 7\n\t"
    "s_add_u32 s36, s36, 4096\n\t"
    "s_lshl_b32 s19, s19, 2\n\t"
    "s_add_u32 s19, s19, s60\n\t"
    "s_lshl_b32 s37, s19, 7\n\t"
    "s_add_u32 s37, s37, 65536\n\t"
    "s_add_u32 s19, s12, s18\n\t"
    "s_lshl_b32 s38, s19, 7\n\t"
    "s_add_u32 s38, s38, 4096\n\t"
    "s_max_u32 s19, s10, 1\n\t"
    "s_add_u32 s19, s19, s12\n\t"
    "s_lshl_b32 s39, s19, 9\n\t"
    "s_add_u32 s39, s39, 65024\n\t"
    "s_mul_i32 s19, s11, 9728\n\t"
    "s_add_u32 s41, s19, 2616576\n\t"
    "s_add_u32 s43, s19, 2631168\n\t"
    "s_add_u32 s52, s19, 2505984\n\t"
    "s_cmp_eq_u32 s10, 0\n\t"
    "s_cselect_b32 s41, 1073741824, s41\n\t"
    "s_cmp_eq_u32 s10, 124\n\t"
    "s_cselect_b32 s43, 1073741824, s43\n\t"
    "s_bitcmp1_b32 s60, 0\n\t"
    "s_cselect_b32 s45, s43, s41\n\t"
    "s_cselect_b32 s44, 14592, 0\n\t"
    "s_add_u32 s45, s45, 3840\n\t"
    "s_add_u32 s44, s44, 114432\n\t"
    "s_lshl_b32 s19, s60, 10\n\t"
    "s_add_u32 s41, s41, s19\n\t"
    "s_add_u32 s43, s43, s19\n\t"
    "s_add_u32 s40, s19, 110592\n\t"
    "s_add_u32 s42, s19, 125184\n\t"
    "s_waitcnt lgkmcnt(0)\n\t"
    "s_mov_b64 s[24:25], s[70:71]\n\t"
    "s_and_b32 s69, s69, 0xffff\n\t"
    "s_mov_b32 s70, 2304000\n\t"
    "s_mov_b32 s71, 0x20000\n\t"
    "buffer_load_dwordx4 v117, s[68:71], s15 offen offset:0 lds\n\t"
    "buffer_load_dwordx4 v117, s[68:71], s15 offen offset:1024 lds\n\t"
    "buffer_load_dwordx4 v117, s[68:71], s15 offen offset:1280 lds\n\t"
    "s_cmp_eq_u32 s23, 0\n\t"
    "s_cselect_b64 s[92:93], s[24:25], s[28:29]\n\t"
    "s_cmp_eq_u32 s23, 2\n\t"
    "s_cselect_b64 s[92:93], s[84:85], s[92:93]\n\t"
    "s_and_b32 s93, s93, 0xffff\n\t"
    "s_mov_b32 m0, 150016\n\t"
    "s_mov_b32 s80, s72\n\t"
    "buffer_load_dword v20, s[92:95], s21 offen lds\n\t"
    "s_and_b32 s81, s73, 0xffff\n\t"
    "s_add_u32 m0, s17, 2304\n\t"
    "s_and_b32 s25, s25, 0xffff\n\t"
    "buffer_load_dwordx4 v117, s[68:71], s16 offen offset:0 lds\n\t"
    "buffer_load_dwordx4 v117, s[68:71], s16 offen offset:1024 lds\n\t"
    "buffer_load_dwordx4 v117, s[68:71], s16 offen offset:1280 lds\n\t"
    "s_and_b32 s29, s29, 0xffff\n\t"
    "s_and_b32 s85, s85, 0xffff\n\t"
    "s_add_u32 s64, s64, s72\n\t"
    "s_addc_u32 s65, s81, 0\n\t"
    "s_add_u32 s88, s74, s14\n\t"
    "s_addc_u32 s89, s75, 0\n\t"
    "s_and_b32 s89, s89, 0xffff\n\t"
    "s_add_u32 s68, s68, s14\n\t"
    "s_addc_u32 s69, s69, 0\n\t"
    "s_movk_i32 s70, 9216\n\t"
    "buffer_load_dwordx2 v[234:235], off, s[80:83], 0 sc1\n\t"
    "s_add_u32 s48, s14, 132096\n\t"
    "s_cmp_lt_u32 s9, 125\n\t"
    "s_cselect_b32 s48, s48, 1073741824\n\t"
    "s_cselect_b32 s52, s52, 1073741824\n\t"
    "s_cselect_b32 s90, 9216, 0\n\t"
    "s_lshl_b32 s49, s13, 10\n\t"
    "s_add_u32 s49, s49, 5111808\n\t"
    "s_mul_i32 s51, s8, 9216\n\t"
    "s_add_u32 s51, s51, 5244416\n\t"
    "s_xor_b32 s53, s8, 1\n\t"
    "s_mul_i32 s53, s53, 9216\n\t"
    "s_add_u32 s53, s53, 5244416\n\t"
    "s_lshr_b32 s54, s61, 1\n\t"
    "s_add_u32 s54, s54, 5194752\n\t"
    "s_add_u32 s57, s61, 5153280\n\t"
    "s_add_u32 s58, s61, 5111808\n\t"
    "s_mov_b32 s59, 5148672\n\t"
    "s_cmp_lt_u32 s60, 2\n\t"
    "s_cselect_b32 s59, s59, 1073741824\n\t"
    "s_mov_b32 s62, 0xc26cf62a\n\t"
    "s_mov_b32 s63, 0xc199319f\n\t"
    "v_and_b32 v4, 3, v0\n\t"
    "v_lshrrev_b32 v9, 2, v0\n\t"
    "v_and_b32 v11, 1, v0\n\t"
    "v_lshrrev_b32 v12, 3, v0\n\t"
    "v_and_b32 v13, 7, v0\n\t"
    "v_mul_u32_u24 v14, 288, v13\n\t"
    "s_lshr_b32 s46, s61, 1\n\t"
    "s_add_u32 s46, s46, 79872\n\t"
    "v_add_u32_e64 v14, s46, v14\n\t"
    "v_lshl_add_u32 v64, v12, 4, v14\n\t"
    "v_lshl_add_u32 v65, v12, 2, v14\n\t"
    "s_lshl_b32 s95, s60, 7\n\t"
    "v_lshl_add_u32 v13, v13, 4, s95\n\t"
    "v_and_b32 v15, 3, v12\n\t"
    "v_lshlrev_b32 v16, 6, v15\n\t"
    "v_xor_b32 v16, v16, v13\n\t"
    "s_add_u32 s92, s55, 0\n\t"
    "s_add_u32 m0, s55, 37888\n\t"
    "s_nop 0\n\t"
    "buffer_load_dwordx4 v117, s[24:27], s92 offen offset:0 lds\n\t"
    "v_lshl_add_u32 v16, v12, 9, v16\n\t"
    "v_add_u32 v66, 130048, v16\n\t"
    "v_lshrrev_b32 v2, 5, v0\n\t"
    "v_mul_u32_u24 v17, 576, v2\n\t"
    "v_xor_b32 v16, v17, v13\n\t"
    "v_lshl_add_u32 v16, v15, 2, v16\n\t"
    "v_add_u32 v67, 138240, v16\n\t"
    "v_and_b32 v1, 0x1f0, v117\n\t"
    "v_bfe_u32 v3, v0, 4, 1\n\t"
    "v_and_b32 v5, 3, v9\n\t"
    "v_lshlrev_b32 v116, 3, v0\n\t"
    "v_and_b32 v6, 0x78, v116\n\t"
    "v_lshrrev_b32 v8, 4, v0\n\t"
    "v_lshlrev_b32 v16, 1, v8\n\t"
    "s_movk_i32 s46, 0x9c\n\t"
    "v_bfe_u32 v10, s46, v16, 2\n\t"
    "v_lshlrev_b32 v16, 1, v5\n\t"
    "v_bfe_u32 v12, s46, v16, 2\n\t"
    "v_lshl_add_u32 v16, v2, 3, v5\n\t"
    "v_mul_u32_u24 v16, 288, v16\n\t"
    "s_lshl_b32 s46, s60, 6\n\t"
    "v_lshl_add_u32 v18, v3, 5, s46\n\t"
    "v_lshl_add_u32 v18, v4, 3, v18\n\t"
    "v_add_u32 v74, v16, v18\n\t"
    "v_add_u32_e64 v16, v16, v74\n\t"
    "v_add_u32 v50, 37888, v16\n\t"
    "v_add_u32 v51, 102400, v16\n\t"
    "v_lshl_add_u32 v16, v10, 3, v5\n\t"
    "v_mul_u32_u24 v16, 288, v16\n\t"
    "v_lshlrev_b32 v18, 3, v4\n\t"
    "v_add_u32 v75, v16, v18\n\t"
    "v_add_u32_e64 v52, v16, v75\n\t"
    "v_lshl_add_u32 v16, v8, 2, v5\n\t"
    "buffer_load_dwordx4 v117, s[24:27], s92 offen offset:1024 lds\n\t"
    "v_mul_u32_u24 v16, 288, v16\n\t"
    "v_add_u32 v76, v16, v18\n\t"
    "v_add_u32 v53, v16, v76\n\t"
    "v_add_u32 v74, 37888, v74\n\t"
    "v_add_u32 v75, 37888, v75\n\t"
    "v_add_u32 v76, 37888, v76\n\t"
    "v_mul_u32_u24_e64 v18, 18, v1\n\t"
    "v_lshl_add_u32 v16, v12, 3, v4\n\t"
    "v_mul_u32_u24 v16, 288, v16\n\t"
    "v_lshrrev_b32_e64 v19, 1, v12\n\t"
    "v_lshl_add_u32 v16, v19, 7, v16\n\t"
    "v_lshl_add_u32 v71, v10, 4, v16\n\t"
    "v_lshl_add_u32 v48, v8, 3, v16\n\t"
    "s_lshl_b32 s46, s60, 6\n\t"
    "v_add_u32 v16, s46, v18\n\t"
    "v_lshl_add_u32 v121, v2, 3, v16\n\t"
    "s_and_b32 s94, s60, 1\n\t"
    "s_lshl_b32 s95, s94, 8\n\t"
    "v_lshl_add_u32 v7, v6, 1, s95\n\t"
    "v_xor_b32_e64 v16, v17, v1\n\t"
    "v_add_u32 v119, 101376, v16\n\t"
    "v_xor_b32 v68, 0x80, v119\n\t"
    "v_add_u32_e64 v96, s61, v16\n\t"
    "v_xor_b32 v97, 0x80, v96\n\t"
    "v_mul_u32_u24 v16, 576, v10\n\t"
    "v_xor_b32 v16, v16, v7\n\t"
    "v_add_u32 v54, 101376, v16\n\t"
    "v_xor_b32 v16, v17, v7\n\t"
    "v_lshl_add_u32 v16, v3, 3, v16\n\t"
    "v_add_u32 v120, 109568, v16\n\t"
    "s_cmp_lt_u32 s60, 2\n\t"
    "s_cselect_b32 s46, 0, 9728\n\t"
    "s_cselect_b32 s47, 29184, 0\n\t"
    "s_cselect_b32 s95, 0, 0x80\n\t"
    "s_cselect_b32 s73, 288, 0\n\t"
    "s_add_u32 s73, s73, 38144\n\t"
    "v_add_u32 v52, s73, v52\n\t"
    "buffer_load_dwordx4 v117, s[24:27], s92 offen offset:2048 lds\n\t"
    "s_add_u32 s73, s73, 73728\n\t"
    "v_add_u32 v53, s73, v53\n\t"
    "s_add_u32 s47, s47, 19456\n\t"
    "v_add_u32 v69, s46, v54\n\t"
    "v_add_u32 v70, s46, v120\n\t"
    "v_add_u32 v77, s47, v54\n\t"
    "v_add_u32_e64 v78, s47, v120\n\t"
    "s_add_u32 s47, s47, 101376\n\t"
    "v_lshl_add_u32 v16, v10, 9, v7\n\t"
    "v_add_u32 v49, s47, v16\n\t"
    "v_xor_b32 v16, s95, v120\n\t"
    "v_lshl_add_u32 v216, s95, 3, v16\n\t"
    "v_add_u32 v244, 159232, v7\n\t"
    "s_lshr_b32 s46, s95, 5\n\t"
    "s_add_u32 s46, s46, 148992\n\t"
    "v_add_u32 v79, s46, v7\n\t"
    "v_add_u32 v47, 148992, v1\n\t"
    "v_mul_u32_u24_e64 v18, 36, v6\n\t"
    "v_add_u32 v16, 159232, v18\n\t"
    "v_lshl_add_u32 v72, v10, 4, v16\n\t"
    "v_lshl_add_u32 v73, v8, 3, v16\n\t"
    "s_mul_i32 s46, s94, 4608\n\t"
    "s_add_u32 s46, s46, 256\n\t"
    "v_lshl_add_u32 v16, v8, 3, v18\n\t"
    "v_add_u32 v122, s46, v16\n\t"
    "s_lshl_b32 s46, s60, 11\n\t"
    "v_add_u32_e64 v113, s46, v117\n\t"
    "s_add_u32 s46, s46, 101376\n\t"
    "v_lshl_add_u32 v16, v2, 3, v1\n\t"
    "v_add_u32 v212, s46, v16\n\t"
    "v_xor_b32 v213, 64, v212\n\t"
    "v_xor_b32 v214, 0x80, v212\n\t"
    "v_xor_b32 v215, 0xc0, v212\n\t"
    "buffer_load_dwordx4 v117, s[24:27], s92 offen offset:3072 lds\n\t"
    "v_lshlrev_b32 v16, 1, v6\n\t"
    "v_lshl_add_u32 v18, v8, 9, v16\n\t"
    "v_add_u32 v240, s46, v18\n\t"
    "s_lshl_b32 s46, s60, 9\n\t"
    "s_add_u32 s46, s46, 159232\n\t"
    "v_add_u32 v100, s46, v16\n\t"
    "v_add_u32 v243, 159232, v1\n\t"
    "s_lshl_b32 s46, s60, 9\n\t"
    "v_add_u32 v242, s46, v243\n\t"
    "s_lshl_b32 s46, s95, 2\n\t"
    "v_add_u32 v251, s46, v244\n\t"
    "v_lshrrev_b32 v16, 1, v4\n\t"
    "v_mul_u32_u24 v16, 576, v16\n\t"
    "v_lshlrev_b32 v18, 6, v2\n\t"
    "v_lshl_add_u32 v18, v5, 4, v18\n\t"
    "v_lshl_add_u32 v18, v3, 7, v18\n\t"
    "v_xor_b32 v16, v16, v18\n\t"
    "v_lshl_add_u32 v16, v11, 3, v16\n\t"
    "v_add_u32 v98, s61, v16\n\t"
    "v_xor_b32 v99, 0x80, v98\n\t"
    "v_lshl_add_u32 v16, v3, 10, v16\n\t"
    "s_lshl_b32 s46, s60, 7\n\t"
    "v_xor_b32 v16, s46, v16\n\t"
    "v_add_u32 v217, 139776, v16\n\t"
    "v_add_u32 v105, 10240, v117\n\t"
    "s_add_u32 s46, s60, 0\n\t"
    "s_and_b32 s46, s46, 3\n\t"
    "s_lshl_b32 s46, s46, 11\n\t"
    "s_add_u32 s46, s46, 2048\n\t"
    "v_add_u32 v101, s46, v117\n\t"
    "v_add_u32 v56, 99328, v101\n\t"
    "s_add_u32 s46, s60, 1\n\t"
    "s_and_b32 s46, s46, 3\n\t"
    "s_add_u32 s92, s55, 4096\n\t"
    "s_add_u32 m0, s55, 41984\n\t"
    "s_nop 0\n\t"
    "buffer_load_dwordx4 v117, s[24:27], s92 offen offset:0 lds\n\t"
    "s_lshl_b32 s46, s46, 11\n\t"
    "v_add_u32 v102, s46, v117\n\t"
    "v_add_u32 v57, 101376, v102\n\t"
    "s_add_u32 s46, s60, 2\n\t"
    "s_and_b32 s46, s46, 3\n\t"
    "s_lshl_b32 s46, s46, 11\n\t"
    "s_add_u32 s46, s46, 2048\n\t"
    "v_add_u32 v103, s46, v117\n\t"
    "v_add_u32 v58, 99328, v103\n\t"
    "s_add_u32 s46, s60, 3\n\t"
    "s_and_b32 s46, s46, 3\n\t"
    "s_lshl_b32 s46, s46, 11\n\t"
    "v_add_u32_e64 v104, s46, v117\n\t"
    "v_add_u32 v59, 101376, v104\n\t"
    "v_add_u32 v246, 73728, v96\n\t"
    "v_add_u32 v247, 73728, v97\n\t"
    "v_add_u32 v248, 73728, v98\n\t"
    "v_add_u32 v249, 73728, v99\n\t"
    "v_mul_u32_u24 v18, 1216, v2\n\t"
    "s_mul_i32 s47, s60, 2432\n\t"
    "s_add_u32 s47, s47, 115456\n\t"
    "v_add_u32_e64 v118, s47, v117\n\t"
    "v_add3_u32 v16, v18, v6, s47\n\t"
    "v_lshl_add_u32 v218, v3, 7, v16\n\t"
    "v_mul_u32_u24 v19, 14592, v3\n\t"
    "v_add_u32 v219, v19, v16\n\t"
    "v_lshrrev_b32_e64 v16, 1, v1\n\t"
    "s_lshl_b32 s47, s60, 8\n\t"
    "s_add_u32 s47, s47, 130048\n\t"
    "v_add3_u32 v220, v18, v16, s47\n\t"
    "s_lshl_b32 s46, s94, 9\n\t"
    "s_add_u32 s46, s46, 8192\n\t"
    "v_add_u32 v114, s46, v116\n\t"
    "buffer_load_dwordx4 v117, s[24:27], s92 offen offset:1024 lds\n\t"
    "v_lshlrev_b32 v252, 4, v6\n\t"
    "v_add_u32 v16, s39, v252\n\t"
    "v_lshlrev_b32 v18, 7, v4\n\t"
    "v_add_u32 v18, 1024, v18\n\t"
    "v_cmp_gt_u32 vcc, 12, v0\n\t"
    "s_nop 1\n\t"
    "v_cndmask_b32 v115, v18, v16, vcc\n\t"
    "v_mov_b32 v16, s35\n\t"
    "v_cmp_gt_u32 vcc, 16, v0\n\t"
    "s_nop 1\n\t"
    "v_cndmask_b32 v252, v16, v252, vcc\n\t"
    "s_lshl_b32 s46, s60, 12\n\t"
    "s_add_u32 s46, s46, 139776\n\t"
    "v_add_u32 v108, s46, v117\n\t"
    "s_lshl_b32 s46, s94, 10\n\t"
    "s_add_u32 s46, s46, 139776\n\t"
    "v_add_u32 v112, s46, v117\n\t"
    "v_mul_u32_u24 v18, 1216, v4\n\t"
    "v_lshl_add_u32 v16, v9, 3, v18\n\t"
    "s_lshl_b32 s46, s60, 7\n\t"
    "s_add_u32 s46, s46, 110592\n\t"
    "v_add_u32 v123, s46, v16\n\t"
    "s_mul_i32 s47, s94, 4864\n\t"
    "s_add_u32 s46, s47, 111616\n\t"
    "v_add_u32_e64 v124, s46, v16\n\t"
    "v_lshl_add_u32 v18, v5, 3, v18\n\t"
    "s_add_u32 s46, s47, 130048\n\t"
    "v_add_u32_e64 v16, s46, v18\n\t"
    "v_lshl_add_u32 v126, v8, 6, v16\n\t"
    "v_lshl_add_u32 v127, v8, 5, v16\n\t"
    "v_mul_u32_u24 v16, 4864, v3\n\t"
    "v_lshl_add_u32 v16, v2, 6, v16\n\t"
    "v_add_u32 v18, 130048, v18\n\t"
    "v_add_u32_e64 v125, v16, v18\n\t"
    "v_lshl_add_u32 v16, v8, 9, v7\n\t"
    "buffer_load_dwordx4 v117, s[24:27], s92 offen offset:2048 lds\n\t"
    "v_add_u32 v241, 110592, v16\n\t"
    "v_add_u32 v106, s61, v117\n\t"
    "v_add_u32 v107, 73728, v106\n\t"
    "s_add_u32 s46, s60, 3\n\t"
    "s_and_b32 s46, s46, 3\n\t"
    "s_mul_i32 s46, s46, 9216\n\t"
    "s_add_u32 s46, s46, 0\n\t"
    "v_add_u32_e64 v109, s46, v117\n\t"
    "s_add_u32 s46, s60, 2\n\t"
    "s_and_b32 s46, s46, 3\n\t"
    "s_mul_i32 s46, s46, 9216\n\t"
    "s_add_u32 s46, s46, 36864\n\t"
    "v_add_u32_e64 v110, s46, v117\n\t"
    "s_add_u32 s46, s60, 1\n\t"
    "s_and_b32 s46, s46, 3\n\t"
    "s_mul_i32 s46, s46, 9216\n\t"
    "s_add_u32 s46, s46, 73728\n\t"
    "v_add_u32_e64 v111, s46, v117\n\t"
    "v_mov_b32 v63, 0xc199319f\n\t"
    "v_mov_b32 v62, 0x3f803f80\n\t"
    "v_mov_b32 v140, 0\n\t"
    "v_mov_b32 v141, 0\n\t"
    "v_mov_b32 v204, 0\n\t"
    "v_mov_b32 v205, 0\n\t"
    "v_accvgpr_write_b32 a162, 0\n\t"
    "v_accvgpr_write_b32 a163, 0\n\t"
    "v_accvgpr_write_b32 a198, 0\n\t"
    "v_accvgpr_write_b32 a199, 0\n\t"
    "v_accvgpr_write_b32 a222, 0\n\t"
    "v_accvgpr_write_b32 a223, 0\n\t"
    "v_accvgpr_write_b32 a242, 0\n\t"
    "v_accvgpr_write_b32 a243, 0\n\t"
    "v_mov_b32 v228, 0x7f817f81\n\t"
    "v_mov_b32 v229, 0x7f817f81\n\t"
    "v_mov_b32 v230, 0x7f817f81\n\t"
    "v_mov_b32 v231, 0x7f817f81\n\t"
    "s_add_u32 s76, s48, 4096\n\t"
    "s_add_u32 s78, s48, 8192\n\t"
    "s_movk_i32 s72, 9216\n\t"
    "s_cmp_lt_u32 s60, 2\n\t"
    "s_cselect_b64 s[74:75], -1, 0\n\t"
    "s_waitcnt vmcnt(8)\n\t"
    "ds_read_b128 v[128:131], v64 offset:0\n\t"
    "ds_read_b128 v[132:135], v64 offset:128\n\t"
    "ds_read_b32 v136, v65 offset:256\n\t"
    "ds_read_b128 v[192:195], v64 offset:2304\n\t"
    "ds_read_b128 v[196:199], v64 offset:2432\n\t"
    "ds_read_b32 v200, v65 offset:2560\n\t"
    "buffer_load_dwordx4 v117, s[24:27], s92 offen offset:3072 lds\n\t"
    "s_add_u32 s92, s55, 8192\n\t"
    "s_add_u32 m0, s55, 46080\n\t"
    "s_nop 0\n\t"
    "buffer_load_dwordx4 v117, s[24:27], s92 offen offset:0 lds\n\t"
    "buffer_load_dwordx4 v117, s[24:27], s92 offen offset:1024 lds\n\t"
    "s_waitcnt lgkmcnt(5)\n\t"
    "v_dot2c_f32_bf16 v140, v128, v62\n\t"
    "s_waitcnt lgkmcnt(2)\n\t"
    "v_dot2c_f32_bf16 v204, v192, v62\n\t"
    "v_dot2c_f32_bf16 v141, v128, v128\n\t"
    "v_dot2c_f32_bf16 v205, v192, v192\n\t"
    "buffer_load_dwordx4 v117, s[24:27], s92 offen offset:1152 lds\n\t"
    "v_dot2c_f32_bf16 v140, v129, v62\n\t"
    "v_dot2c_f32_bf16 v204, v193, v62\n\t"
    "v_dot2c_f32_bf16 v141, v129, v129\n\t"
    "v_dot2c_f32_bf16 v205, v193, v193\n\t"
    "v_dot2c_f32_bf16 v140, v130, v62\n\t"
    "v_dot2c_f32_bf16 v204, v194, v62\n\t"
    "v_dot2c_f32_bf16 v141, v130, v130\n\t"
    "v_dot2c_f32_bf16 v205, v194, v194\n\t"
    "s_add_u32 s92, s56, 36864\n\t"
    "s_add_u32 m0, s56, 159232\n\t"
    "s_nop 0\n\t"
    "buffer_load_dwordx4 v117, s[28:31], s92 offen offset:0 lds\n\t"
    "v_dot2c_f32_bf16 v140, v131, v62\n\t"
    "v_dot2c_f32_bf16 v204, v195, v62\n\t"
    "v_dot2c_f32_bf16 v141, v131, v131\n\t"
    "v_dot2c_f32_bf16 v205, v195, v195\n\t"
    "v_dot2c_f32_bf16 v140, v132, v62\n\t"
    "s_waitcnt lgkmcnt(1)\n\t"
    "v_dot2c_f32_bf16 v204, v196, v62\n\t"
    "v_dot2c_f32_bf16 v141, v132, v132\n\t"
    "v_dot2c_f32_bf16 v205, v196, v196\n\t"
    "buffer_load_dwordx4 v117, s[28:31], s92 offen offset:128 lds\n\t"
    "v_dot2c_f32_bf16 v140, v133, v62\n\t"
    "v_dot2c_f32_bf16 v204, v197, v62\n\t"
    "v_dot2c_f32_bf16 v141, v133, v133\n\t"
    "v_dot2c_f32_bf16 v205, v197, v197\n\t"
    "v_dot2c_f32_bf16 v140, v134, v62\n\t"
    "v_dot2c_f32_bf16 v204, v198, v62\n\t"
    "v_dot2c_f32_bf16 v141, v134, v134\n\t"
    "v_dot2c_f32_bf16 v205, v198, v198\n\t"
    "s_add_u32 s92, s61, 0\n\t"
    "s_add_u32 m0, s77, 0\n\t"
    "s_nop 0\n\t"
    "buffer_load_dwordx4 v117, s[28:31], s92 offen offset:0 lds\n\t"
    "v_dot2c_f32_bf16 v140, v135, v62\n\t"
    "v_dot2c_f32_bf16 v204, v199, v62\n\t"
    "v_dot2c_f32_bf16 v141, v135, v135\n\t"
    "v_dot2c_f32_bf16 v205, v199, v199\n\t"
    "v_dot2c_f32_bf16 v140, v136, v62\n\t"
    "s_waitcnt lgkmcnt(0)\n\t"
    "v_dot2c_f32_bf16 v204, v200, v62\n\t"
    "v_dot2c_f32_bf16 v141, v136, v136\n\t"
    "v_dot2c_f32_bf16 v205, v200, v200\n\t"
    "buffer_load_dwordx4 v117, s[28:31], s92 offen offset:1024 lds\n\t"
    "v_lshlrev_b32 v148, 16, v128\n\t"
    "v_and_b32 v149, 0xffff0000, v128\n\t"
    "v_lshlrev_b32_e64 v150, 16, v129\n\t"
    "v_add_f32_dpp v140, v140, v140 row_ror:8 row_mask:0xf bank_mask:0xf\n\t"
    "v_add_f32_dpp v204, v204, v204 row_ror:8 row_mask:0xf bank_mask:0xf\n\t"
    "v_and_b32 v151, 0xffff0000, v129\n\t"
    "v_add_f32_dpp v141, v141, v141 row_ror:8 row_mask:0xf bank_mask:0xf\n\t"
    "v_add_f32_dpp v205, v205, v205 row_ror:8 row_mask:0xf bank_mask:0xf\n\t"
    "buffer_load_dwordx4 v117, s[28:31], s92 offen offset:2048 lds\n\t"
    "v_lshlrev_b32 v152, 16, v130\n\t"
    "v_mov_b32 v142, v140\n\t"
    "v_mov_b32 v206, v204\n\t"
    "v_and_b32 v153, 0xffff0000, v130\n\t"
    "v_mov_b32 v143, v141\n\t"
    "v_mov_b32 v207, v205\n\t"
    "v_lshlrev_b32 v154, 16, v131\n\t"
    "v_permlane16_swap_b32 v142, v140\n\t"
    "buffer_load_dwordx4 v117, s[28:31], s92 offen offset:3072 lds\n\t"
    "v_permlane16_swap_b32 v206, v204\n\t"
    "v_and_b32 v155, 0xffff0000, v131\n\t"
    "v_permlane16_swap_b32 v143, v141\n\t"
    "v_permlane16_swap_b32 v207, v205\n\t"
    "v_lshlrev_b32 v156, 16, v132\n\t"
    "v_add_f32 v140, v140, v142\n\t"
    "v_add_f32_e64 v204, v204, v206\n\t"
    "v_and_b32 v157, 0xffff0000, v132\n\t"
    "buffer_load_dwordx4 v117, s[28:31], s92 offen offset:3584 lds\n\t"
    "v_add_f32 v141, v141, v143\n\t"
    "v_add_f32 v205, v205, v207\n\t"
    "v_lshlrev_b32 v158, 16, v133\n\t"
    "v_mov_b32 v142, v140\n\t"
    "v_mov_b32 v206, v204\n\t"
    "v_and_b32 v159, 0xffff0000, v133\n\t"
    "v_mov_b32 v143, v141\n\t"
    "v_mov_b32_e64 v207, v205\n\t"
    "s_add_u32 s92, s61, 4608\n\t"
    "s_add_u32 m0, s77, 4736\n\t"
    "s_nop 0\n\t"
    "buffer_load_dwordx4 v117, s[28:31], s92 offen offset:0 lds\n\t"
    "v_lshlrev_b32 v160, 16, v134\n\t"
    "v_permlane32_swap_b32 v142, v140\n\t"
    "v_permlane32_swap_b32 v206, v204\n\t"
    "v_and_b32 v161, 0xffff0000, v134\n\t"
    "v_permlane32_swap_b32 v143, v141\n\t"
    "v_permlane32_swap_b32 v207, v205\n\t"
    "v_lshlrev_b32 v162, 16, v135\n\t"
    "v_add_f32 v140, v140, v142\n\t"
    "buffer_load_dwordx4 v117, s[28:31], s92 offen offset:1024 lds\n\t"
    "v_add_f32 v204, v204, v206\n\t"
    "v_and_b32 v163, 0xffff0000, v135\n\t"
    "v_add_f32 v141, v141, v143\n\t"
    "v_add_f32 v205, v205, v207\n\t"
    "v_lshlrev_b32 v164, 16, v136\n\t"
    "v_mul_f32 v140, 0x3be38e39, v140\n\t"
    "v_mul_f32 v204, 0x3be38e39, v204\n\t"
    "v_and_b32 v165, 0xffff0000, v136\n\t"
    "buffer_load_dwordx4 v117, s[28:31], s92 offen offset:2048 lds\n\t"
    "v_mul_f32 v141, 0x3be38e39, v141\n\t"
    "v_mul_f32 v205, 0x3be38e39, v205\n\t"
    "v_fma_f32 v141, -v140, v140, v141\n\t"
    "v_fma_f32 v205, -v204, v204, v205\n\t"
    "v_max_f32 v141, 0, v141\n\t"
    "v_max_f32 v205, 0, v205\n\t"
    "v_add_f32 v141, 0x3727c5ac, v141\n\t"
    "v_add_f32 v205, 0x3727c5ac, v205\n\t"
    "buffer_load_dwordx4 v117, s[28:31], s92 offen offset:3072 lds\n\t"
    "v_rsq_f32 v142, v141\n\t"
    "v_rsq_f32 v206, v205\n\t"
    "v_mul_f32 v144, -v140, v142\n\t"
    "v_mul_f32 v208, -v204, v206\n\t"
    "v_add_f32_e64 v146, 0x3f000000, v144\n\t"
    "v_pk_fma_f32 v[18:19], v[148:149], v[142:143], v[146:147] op_sel_hi:[1,0,0]\n\t"
    "v_pk_fma_f32 v[20:21], v[150:151], v[142:143], v[146:147] op_sel_hi:[1,0,0]\n\t"
    "v_cvt_pk_bf16_f32 v128, v18, v19\n\t"
    "buffer_load_dwordx4 v117, s[28:31], s92 offen offset:3584 lds\n\t"
    "v_pk_fma_f32 v[22:23], v[152:153], v[142:143], v[146:147] op_sel_hi:[1,0,0]\n\t"
    "v_cvt_pk_bf16_f32 v129, v20, v21\n\t"
    "v_pk_fma_f32 v[24:25], v[154:155], v[142:143], v[146:147] op_sel_hi:[1,0,0]\n\t"
    "v_cvt_pk_bf16_f32 v130, v22, v23\n\t"
    "v_pk_fma_f32 v[26:27], v[156:157], v[142:143], v[146:147] op_sel_hi:[1,0,0]\n\t"
    "v_cvt_pk_bf16_f32 v131, v24, v25\n\t"
    "v_pk_fma_f32 v[28:29], v[158:159], v[142:143], v[146:147] op_sel_hi:[1,0,0]\n\t"
    "v_cvt_pk_bf16_f32 v132, v26, v27\n\t"
    "v_pk_fma_f32 v[30:31], v[160:161], v[142:143], v[146:147] op_sel_hi:[1,0,0]\n\t"
    "v_cvt_pk_bf16_f32 v133, v28, v29\n\t"
    "v_pk_fma_f32 v[32:33], v[162:163], v[142:143], v[146:147] op_sel_hi:[1,0,0]\n\t"
    "v_cvt_pk_bf16_f32 v134, v30, v31\n\t"
    "v_pk_fma_f32 v[34:35], v[164:165], v[142:143], v[146:147] op_sel_hi:[1,0,0]\n\t"
    "v_cvt_pk_bf16_f32 v135, v32, v33\n\t"
    "v_cvt_pk_bf16_f32 v136, v34, v35\n\t"
    "ds_write_b32 v67, v136 offset:19968\n\t"
    "ds_read_b128 v[176:179], v66 offset:28160\n\t"
    "v_mul_f32 v140, 0x3df6384f, v142\n\t"
    "v_mul_f32 v144, 0x3df6384f, v144\n\t"
    "v_mul_f32 v206, 0x3df6384f, v206\n\t"
    "v_mul_f32 v208, 0x3df6384f, v208\n\t"
    "v_pk_fma_f32 v[148:149], v[148:149], v[140:141], v[144:145] op_sel_hi:[1,0,0]\n\t"
    "v_pk_fma_f32 v[150:151], v[150:151], v[140:141], v[144:145] op_sel_hi:[1,0,0]\n\t"
    "v_cvt_pk_bf16_f32 v180, v148, v149\n\t"
    "v_pk_fma_f32 v[152:153], v[152:153], v[140:141], v[144:145] op_sel_hi:[1,0,0]\n\t"
    "v_cvt_pk_bf16_f32 v181, v150, v151\n\t"
    "buffer_store_dwordx4 v[128:131], v66, s[80:83], s48 offen sc1\n\t"
    "buffer_store_dwordx4 v[132:135], v66, s[80:83], s76 offen sc1\n\t"
    "s_mov_b64 exec, 0xffff\n\t"
    "s_waitcnt lgkmcnt(0)\n\t"
    "buffer_store_dwordx4 v[176:179], v66, s[80:83], s78 offen sc1\n\t"
    "s_mov_b64 exec, -1\n\t"
    "v_pk_fma_f32 v[154:155], v[154:155], v[140:141], v[144:145] op_sel_hi:[1,0,0]\n\t"
    "v_cvt_pk_bf16_f32 v182, v152, v153\n\t"
    "v_pk_fma_f32 v[156:157], v[156:157], v[140:141], v[144:145] op_sel_hi:[1,0,0]\n\t"
    "v_cvt_pk_bf16_f32 v183, v154, v155\n\t"
    "v_pk_fma_f32 v[158:159], v[158:159], v[140:141], v[144:145] op_sel_hi:[1,0,0]\n\t"
    "v_cvt_pk_bf16_f32 v184, v156, v157\n\t"
    "v_pk_fma_f32 v[160:161], v[160:161], v[140:141], v[144:145] op_sel_hi:[1,0,0]\n\t"
    "v_cvt_pk_bf16_f32 v185, v158, v159\n\t"
    "v_pk_fma_f32 v[162:163], v[162:163], v[140:141], v[144:145] op_sel_hi:[1,0,0]\n\t"
    "v_cvt_pk_bf16_f32 v186, v160, v161\n\t"
    "v_pk_fma_f32 v[164:165], v[164:165], v[140:141], v[144:145] op_sel_hi:[1,0,0]\n\t"
    "v_cvt_pk_bf16_f32 v187, v162, v163\n\t"
    "v_cvt_pk_bf16_f32 v188, v164, v165\n\t"
    "ds_write_b128 v66, v[180:183] offset:0\n\t"
    "ds_write_b128 v66, v[184:187] offset:4096\n\t"
    "ds_write_b32 v67, v188 offset:0\n\t"
    "v_lshlrev_b32 v0, 16, v192\n\t"
    "v_and_b32 v1, 0xffff0000, v192\n\t"
    "v_lshlrev_b32 v2, 16, v193\n\t"
    "v_and_b32 v3, 0xffff0000, v193\n\t"
    "v_lshlrev_b32 v4, 16, v194\n\t"
    "v_and_b32 v5, 0xffff0000, v194\n\t"
    "v_lshlrev_b32 v6, 16, v195\n\t"
    "v_and_b32 v7, 0xffff0000, v195\n\t"
    "v_lshlrev_b32 v8, 16, v196\n\t"
    "v_and_b32 v9, 0xffff0000, v196\n\t"
    "v_lshlrev_b32 v10, 16, v197\n\t"
    "v_and_b32 v11, 0xffff0000, v197\n\t"
    "v_lshlrev_b32 v12, 16, v198\n\t"
    "v_and_b32 v13, 0xffff0000, v198\n\t"
    "v_lshlrev_b32 v14, 16, v199\n\t"
    "v_and_b32 v15, 0xffff0000, v199\n\t"
    "v_lshlrev_b32_e64 v16, 16, v200\n\t"
    "v_and_b32 v17, 0xffff0000, v200\n\t"
    "v_pk_fma_f32 v[0:1], v[0:1], v[206:207], v[208:209] op_sel_hi:[1,0,0]\n\t"
    "v_pk_fma_f32 v[2:3], v[2:3], v[206:207], v[208:209] op_sel_hi:[1,0,0]\n\t"
    "v_cvt_pk_bf16_f32 v166, v0, v1\n\t"
    "v_pk_fma_f32 v[4:5], v[4:5], v[206:207], v[208:209] op_sel_hi:[1,0,0]\n\t"
    "v_cvt_pk_bf16_f32 v167, v2, v3\n\t"
    "v_pk_fma_f32 v[6:7], v[6:7], v[206:207], v[208:209] op_sel_hi:[1,0,0]\n\t"
    "v_cvt_pk_bf16_f32 v168, v4, v5\n\t"
    "v_pk_fma_f32 v[8:9], v[8:9], v[206:207], v[208:209] op_sel_hi:[1,0,0]\n\t"
    "v_cvt_pk_bf16_f32 v169, v6, v7\n\t"
    "v_pk_fma_f32 v[10:11], v[10:11], v[206:207], v[208:209] op_sel_hi:[1,0,0]\n\t"
    "v_cvt_pk_bf16_f32 v170, v8, v9\n\t"
    "v_pk_fma_f32 v[12:13], v[12:13], v[206:207], v[208:209] op_sel_hi:[1,0,0]\n\t"
    "v_cvt_pk_bf16_f32 v171, v10, v11\n\t"
    "v_pk_fma_f32 v[14:15], v[14:15], v[206:207], v[208:209] op_sel_hi:[1,0,0]\n\t"
    "v_cvt_pk_bf16_f32 v172, v12, v13\n\t"
    "v_pk_fma_f32 v[16:17], v[16:17], v[206:207], v[208:209] op_sel_hi:[1,0,0]\n\t"
    "v_cvt_pk_bf16_f32 v173, v14, v15\n\t"
    "v_cvt_pk_bf16_f32 v174, v16, v17\n\t"
    "ds_write_b128 v66, v[166:169] offset:9728\n\t"
    "ds_write_b128 v66, v[170:173] offset:13824\n\t"
    "ds_write_b32 v67, v174 offset:9728\n\t"
    "s_waitcnt vmcnt(26)\n\t"
    "v_add_co_u32 v234, vcc, 1, v234\n\t"
    "s_nop 1\n\t"
    "v_addc_co_u32 v235, vcc, 0, v235, vcc\n\t"
    "s_waitcnt vmcnt(15)\n\t"
    "s_waitcnt lgkmcnt(0)\n\t"
    "s_barrier\n\t"
    "ds_read_b64_tr_b16 a[0:1], v74 offset:0\n\t"
    "ds_read_b64_tr_b16 a[2:3], v74 offset:1152\n\t"
    "ds_read_b128 a[72:75], v119 offset:28672\n\t"
    "ds_read_b128 a[108:111], v119 offset:38400\n\t"
    "ds_read_b64_tr_b16 a[4:5], v74 offset:4608\n\t"
    "ds_read_b64_tr_b16 a[6:7], v74 offset:5760\n\t"
    "ds_read_b128 a[76:79], v68 offset:29696\n\t"
    "ds_read_b128 a[112:115], v68 offset:39424\n\t"
    "ds_read_b64_tr_b16 a[8:9], v74 offset:9216\n\t"
    "ds_read_b64_tr_b16 a[10:11], v74 offset:10368\n\t"
    "ds_read_b128 a[80:83], v119 offset:30720\n\t"
    "ds_read_b128 a[116:119], v119 offset:40448\n\t"
    "v_mov_b32 v204, 0\n\t"
    "v_mov_b32 v205, 0\n\t"
    "v_mov_b32 v206, 0\n\t"
    "v_mov_b32 v207, 0\n\t"
    "s_waitcnt lgkmcnt(9)\n\t"
    "v_mfma_f32_32x32x16_bf16 v[128:143], a[0:3], a[72:75], 0\n\t"
    "ds_read_b64_tr_b16 a[12:13], v74 offset:13824\n\t"
    "ds_read_b64_tr_b16 a[14:15], v74 offset:14976\n\t"
    "s_waitcnt lgkmcnt(10)\n\t"
    "v_mfma_f32_32x32x16_bf16 v[144:159], a[0:3], a[108:111], 0\n\t"
    "s_add_u32 s92, s55, 41472\n\t"
    "s_add_u32 m0, s55, 79360\n\t"
    "s_nop 0\n\t"
    "buffer_load_dwordx4 v117, s[84:87], s92 offen offset:0 lds\n\t"
    "ds_read_b128 a[84:87], v68 offset:31744\n\t"
    "ds_read_b128 a[120:123], v68 offset:41472\n\t"
    "s_waitcnt lgkmcnt(9)\n\t"
    "v_mfma_f32_32x32x16_bf16 v[128:143], a[4:7], a[76:79], v[128:143]\n\t"
    "ds_read_b64_tr_b16 a[16:17], v74 offset:18432\n\t"
    "ds_read_b64_tr_b16 a[18:19], v74 offset:19584\n\t"
    "s_waitcnt lgkmcnt(10)\n\t"
    "v_mfma_f32_32x32x16_bf16 v[144:159], a[4:7], a[112:115], v[144:159]\n\t"
    "buffer_load_dwordx4 v117, s[84:87], s92 offen offset:1024 lds\n\t"
    "ds_read_b128 a[88:91], v119 offset:32768\n\t"
    "ds_read_b128 a[124:127], v119 offset:42496\n\t"
    "s_waitcnt lgkmcnt(9)\n\t"
    "v_mfma_f32_32x32x16_bf16 v[128:143], a[8:11], a[80:83], v[128:143]\n\t"
    "ds_read_b64_tr_b16 a[20:21], v74 offset:23040\n\t"
    "ds_read_b64_tr_b16 a[22:23], v74 offset:24192\n\t"
    "s_waitcnt lgkmcnt(10)\n\t"
    "v_mfma_f32_32x32x16_bf16 v[144:159], a[8:11], a[116:119], v[144:159]\n\t"
    "buffer_load_dwordx4 v117, s[84:87], s92 offen offset:2048 lds\n\t"
    "ds_read_b128 a[92:95], v68 offset:33792\n\t"
    "ds_read_b128 a[128:131], v68 offset:43520\n\t"
    "s_waitcnt lgkmcnt(9)\n\t"
    "v_mfma_f32_32x32x16_bf16 v[128:143], a[12:15], a[84:87], v[128:143]\n\t"
    "ds_read_b64_tr_b16 a[24:25], v74 offset:27648\n\t"
    "ds_read_b64_tr_b16 a[26:27], v74 offset:28800\n\t"
    "s_waitcnt lgkmcnt(10)\n\t"
    "v_mfma_f32_32x32x16_bf16 v[144:159], a[12:15], a[120:123], v[144:159]\n\t"
    "buffer_load_dwordx4 v117, s[84:87], s92 offen offset:3072 lds\n\t"
    "ds_read_b128 a[96:99], v119 offset:34816\n\t"
    "ds_read_b128 a[132:135], v119 offset:44544\n\t"
    "s_waitcnt lgkmcnt(9)\n\t"
    "v_mfma_f32_32x32x16_bf16 v[128:143], a[16:19], a[88:91], v[128:143]\n\t"
    "ds_read_b64_tr_b16 a[28:29], v74 offset:32256\n\t"
    "ds_read_b64_tr_b16 a[30:31], v74 offset:33408\n\t"
    "s_waitcnt lgkmcnt(10)\n\t"
    "v_mfma_f32_32x32x16_bf16 v[144:159], a[16:19], a[124:127], v[144:159]\n\t"
    "s_add_u32 s92, s55, 45568\n\t"
    "s_add_u32 m0, s55, 83456\n\t"
    "s_nop 0\n\t"
    "buffer_load_dwordx4 v117, s[84:87], s92 offen offset:0 lds\n\t"
    "ds_read_b128 a[100:103], v68 offset:35840\n\t"
    "ds_read_b128 a[136:139], v68 offset:45568\n\t"
    "s_waitcnt lgkmcnt(9)\n\t"
    "v_mfma_f32_32x32x16_bf16 v[128:143], a[20:23], a[92:95], v[128:143]\n\t"
    "ds_read_b64_tr_b16 a[32:33], v74 offset:36864\n\t"
    "ds_read_b64_tr_b16 a[34:35], v74 offset:38016\n\t"
    "s_waitcnt lgkmcnt(10)\n\t"
    "v_mfma_f32_32x32x16_bf16 v[144:159], a[20:23], a[128:131], v[144:159]\n\t"
    "buffer_load_dwordx4 v117, s[84:87], s92 offen offset:1024 lds\n\t"
    "ds_read_b128 a[104:107], v119 offset:36864\n\t"
    "ds_read_b128 a[140:143], v119 offset:46592\n\t"
    "s_waitcnt lgkmcnt(9)\n\t"
    "v_mfma_f32_32x32x16_bf16 v[128:143], a[24:27], a[96:99], v[128:143]\n\t"
    "ds_read_b64_tr_b16 a[144:145], v75 offset:256\n\t"
    "ds_read_b64_tr_b16 a[146:147], v75 offset:1408\n\t"
    "s_waitcnt lgkmcnt(10)\n\t"
    "v_mfma_f32_32x32x16_bf16 v[144:159], a[24:27], a[132:135], v[144:159]\n\t"
    "buffer_load_dwordx4 v117, s[84:87], s92 offen offset:2048 lds\n\t"
    "ds_read_b64_tr_b16 a[148:149], v75 offset:9472\n\t"
    "ds_read_b64_tr_b16 a[150:151], v75 offset:10624\n\t"
    "s_waitcnt lgkmcnt(9)\n\t"
    "v_mfma_f32_32x32x16_bf16 v[128:143], a[28:31], a[100:103], v[128:143]\n\t"
    "ds_read_b64_tr_b16 a[152:153], v75 offset:18688\n\t"
    "ds_read_b64_tr_b16 a[154:155], v75 offset:19840\n\t"
    "s_waitcnt lgkmcnt(10)\n\t"
    "v_mfma_f32_32x32x16_bf16 v[144:159], a[28:31], a[136:139], v[144:159]\n\t"
    "buffer_load_dwordx4 v117, s[84:87], s92 offen offset:3072 lds\n\t"
    "ds_read_b64_tr_b16 a[156:157], v75 offset:27904\n\t"
    "ds_read_b64_tr_b16 a[158:159], v75 offset:29056\n\t"
    "s_waitcnt lgkmcnt(9)\n\t"
    "v_mfma_f32_32x32x16_bf16 v[128:143], a[32:35], a[104:107], v[128:143]\n\t"
    "ds_read_b64_tr_b16 a[160:161], v76 offset:37120\n\t"
    "ds_read_b128 v[186:189], v69 offset:28672\n\t"
    "s_waitcnt lgkmcnt(10)\n\t"
    "v_mfma_f32_32x32x16_bf16 v[144:159], a[32:35], a[140:143], v[144:159]\n\t"
    "ds_read_b128 v[190:193], v69 offset:30720\n\t"
    "ds_read_b128 v[194:197], v69 offset:32768\n\t"
    "ds_read_b128 v[198:201], v69 offset:34816\n\t"
    "ds_read_b64 v[202:203], v70 offset:28672\n\t"
    "s_waitcnt lgkmcnt(4)\n\t"
    "v_mfma_f32_16x16x32_bf16 v[160:163], a[144:147], v[186:189], 0\n\t"
    "s_waitcnt lgkmcnt(3)\n\t"
    "v_mfma_f32_16x16x32_bf16 v[160:163], a[148:151], v[190:193], v[160:163]\n\t"
    "s_waitcnt lgkmcnt(2)\n\t"
    "v_mfma_f32_16x16x32_bf16 v[160:163], a[152:155], v[194:197], v[160:163]\n\t"
    "s_waitcnt lgkmcnt(1)\n\t"
    "v_mfma_f32_16x16x32_bf16 v[160:163], a[156:159], v[198:201], v[160:163]\n\t"
    "s_waitcnt lgkmcnt(0)\n\t"
    "v_mfma_f32_16x16x32_bf16 v[160:163], a[160:163], v[202:205], v[160:163]\n\t"
    "v_cvt_pk_bf16_f32 v168, v128, v129\n\t"
    "v_cvt_pk_bf16_f32 v169, v130, v131\n\t"
    "v_cvt_pk_bf16_f32 v170, v132, v133\n\t"
    "v_cvt_pk_bf16_f32 v171, v134, v135\n\t"
    "v_cvt_pk_bf16_f32 v172, v136, v137\n\t"
    "v_cvt_pk_bf16_f32 v173, v138, v139\n\t"
    "v_cvt_pk_bf16_f32 v174, v140, v141\n\t"
    "v_cvt_pk_bf16_f32 v175, v142, v143\n\t"
    "ds_write_b64 v212, v[168:169] offset:48640\n\t"
    "ds_write_b64 v213, v[170:171] offset:49152\n\t"
    "ds_write_b64 v214, v[172:173] offset:49664\n\t"
    "ds_write_b64 v215, v[174:175] offset:50176\n\t"
    "v_cvt_pk_bf16_f32 v176, v144, v145\n\t"
    "v_cvt_pk_bf16_f32 v177, v146, v147\n\t"
    "v_cvt_pk_bf16_f32 v178, v148, v149\n\t"
    "v_cvt_pk_bf16_f32 v179, v150, v151\n\t"
    "v_cvt_pk_bf16_f32 v180, v152, v153\n\t"
    "v_cvt_pk_bf16_f32 v181, v154, v155\n\t"
    "v_cvt_pk_bf16_f32 v182, v156, v157\n\t"
    "v_cvt_pk_bf16_f32 v183, v158, v159\n\t"
    "ds_write_b64 v212, v[176:177] offset:19456\n\t"
    "ds_write_b64 v213, v[178:179] offset:19968\n\t"
    "ds_write_b64 v214, v[180:181] offset:20480\n\t"
    "ds_write_b64 v215, v[182:183] offset:20992\n\t"
    "v_cvt_pk_bf16_f32 v184, v160, v161\n\t"
    "v_cvt_pk_bf16_f32 v185, v162, v163\n\t"
    "ds_write_b64 v78, v[184:185] offset:0\n\t"
    "s_waitcnt vmcnt(8)\n\t"
    "s_waitcnt lgkmcnt(0)\n\t"
    "s_barrier\n\t"
    "s_cmp_lg_u32 s60, 3\n\t"
    "s_cbranch_scc1 KF_%=\n\t"
    "s_mov_b64 exec, 1\n\t"
    "buffer_store_dwordx2 v[234:235], off, s[80:83], s36 sc1\n\t"
    "s_mov_b64 exec, -1\n\t"
    "KF_%=:\n\t"
    "ds_read_b128 a[204:207], v77 offset:0\n\t"
    "ds_read_b128 a[0:3], v71 offset:0\n\t"
    "ds_read_b128 a[208:211], v77 offset:2048\n\t"
    "ds_read_b128 a[4:7], v71 offset:64\n\t"
    "ds_read_b128 a[212:215], v77 offset:4096\n\t"
    "ds_read_b128 a[8:11], v71 offset:128\n\t"
    "ds_read_b128 a[216:219], v77 offset:6144\n\t"
    "ds_read_b128 a[12:15], v71 offset:192\n\t"
    "ds_read_b64 a[220:221], v78 offset:0\n\t"
    "ds_read_b64 a[180:181], v48 offset:256\n\t"
    "ds_read_b64 a[182:183], v48 offset:1408\n\t"
    "ds_read_b128 a[16:19], v71 offset:1152\n\t"
    "ds_read_b128 a[20:23], v71 offset:1216\n\t"
    "ds_read_b128 a[24:27], v71 offset:1280\n\t"
    "ds_read_b128 a[28:31], v71 offset:1344\n\t"
    "ds_read_b64 a[184:185], v48 offset:9728\n\t"
    "ds_read_b128 a[32:35], v71 offset:9472\n\t"
    "s_add_u32 s92, s55, 49664\n\t"
    "s_add_u32 m0, s55, 87552\n\t"
    "s_nop 0\n\t"
    "buffer_load_dwordx4 v117, s[84:87], s92 offen offset:0 lds\n\t"
    "buffer_load_dwordx4 v117, s[84:87], s92 offen offset:1024 lds\n\t"
    "s_waitcnt lgkmcnt(14)\n\t"
    "v_mfma_f32_16x16x32_bf16 v[128:131], a[0:3], a[204:207], 0\n\t"
    "ds_read_b128 a[36:39], v71 offset:9536\n\t"
    "s_waitcnt lgkmcnt(14)\n\t"
    "v_mfma_f32_16x16x32_bf16 v[128:131], a[4:7], a[208:211], v[128:131]\n\t"
    "ds_read_b128 a[40:43], v71 offset:9600\n\t"
    "s_waitcnt lgkmcnt(13)\n\t"
    "v_mfma_f32_16x16x32_bf16 v[128:131], a[8:11], a[212:215], v[128:131]\n\t"
    "s_waitcnt lgkmcnt(7)\n\t"
    "ds_read_b128 a[44:47], v71 offset:9664\n\t"
    "v_mfma_f32_16x16x32_bf16 v[128:131], a[12:15], a[216:219], v[128:131]\n\t"
    "ds_read_b64 a[186:187], v48 offset:10880\n\t"
    "buffer_load_dwordx4 v117, s[84:87], s92 offen offset:1152 lds\n\t"
    "v_mfma_f32_16x16x32_bf16 v[128:131], a[180:183], a[220:223], v[128:131]\n\t"
    "ds_read_b128 a[48:51], v71 offset:10624\n\t"
    "v_mfma_f32_16x16x32_bf16 v[132:135], a[16:19], a[204:207], 0\n\t"
    "s_waitcnt lgkmcnt(7)\n\t"
    "ds_read_b128 a[52:55], v71 offset:10688\n\t"
    "v_mfma_f32_16x16x32_bf16 v[132:135], a[20:23], a[208:211], v[132:135]\n\t"
    "ds_read_b128 a[56:59], v71 offset:10752\n\t"
    "v_mfma_f32_16x16x32_bf16 v[132:135], a[24:27], a[212:215], v[132:135]\n\t"
    "ds_read_b128 a[60:63], v71 offset:10816\n\t"
    "s_add_u32 s92, s55, 0\n\t"
    "s_add_u32 m0, s55, 37888\n\t"
    "s_nop 0\n\t"
    "buffer_load_dwordx4 v117, s[84:87], s92 offen offset:0 lds\n\t"
    "v_mfma_f32_16x16x32_bf16 v[132:135], a[28:31], a[216:219], v[132:135]\n\t"
    "s_waitcnt lgkmcnt(7)\n\t"
    "ds_read_b64 a[188:189], v48 offset:19200\n\t"
    "v_mfma_f32_16x16x32_bf16 v[132:135], a[182:185], a[220:223], v[132:135]\n\t"
    "ds_read_b128 a[64:67], v71 offset:18944\n\t"
    "v_cvt_pk_bf16_f32 v168, v128, v129\n\t"
    "v_mfma_f32_16x16x32_bf16 v[136:139], a[32:35], a[204:207], 0\n\t"
    "ds_read_b128 a[68:71], v71 offset:19008\n\t"
    "v_cvt_pk_bf16_f32 v169, v130, v131\n\t"
    "v_mfma_f32_16x16x32_bf16 v[136:139], a[36:39], a[208:211], v[136:139]\n\t"
    "s_waitcnt lgkmcnt(7)\n\t"
    "ds_read_b128 a[108:111], v71 offset:19072\n\t"
    "buffer_load_dwordx4 v117, s[84:87], s92 offen offset:1024 lds\n\t"
    "v_mfma_f32_16x16x32_bf16 v[136:139], a[40:43], a[212:215], v[136:139]\n\t"
    "ds_read_b128 a[112:115], v71 offset:19136\n\t"
    "v_mfma_f32_16x16x32_bf16 v[136:139], a[44:47], a[216:219], v[136:139]\n\t"
    "ds_read_b64 a[190:191], v48 offset:20352\n\t"
    "v_mfma_f32_16x16x32_bf16 v[136:139], a[184:187], a[220:223], v[136:139]\n\t"
    "s_waitcnt lgkmcnt(7)\n\t"
    "ds_read_b128 a[116:119], v71 offset:20096\n\t"
    "v_cvt_pk_bf16_f32 v170, v132, v133\n\t"
    "v_mfma_f32_16x16x32_bf16 v[140:143], a[48:51], a[204:207], 0\n\t"
    "ds_read_b128 a[120:123], v71 offset:20160\n\t"
    "buffer_load_dwordx4 v117, s[84:87], s92 offen offset:2048 lds\n\t"
    "v_mfma_f32_16x16x32_bf16 v[140:143], a[52:55], a[208:211], v[140:143]\n\t"
    "ds_read_b128 a[124:127], v71 offset:20224\n\t"
    "v_cvt_pk_bf16_f32 v171, v134, v135\n\t"
    "v_mfma_f32_16x16x32_bf16 v[140:143], a[56:59], a[212:215], v[140:143]\n\t"
    "s_waitcnt lgkmcnt(7)\n\t"
    "ds_read_b128 a[128:131], v71 offset:20288\n\t"
    "v_mfma_f32_16x16x32_bf16 v[140:143], a[60:63], a[216:219], v[140:143]\n\t"
    "ds_read_b64 a[192:193], v48 offset:28672\n\t"
    "v_mfma_f32_16x16x32_bf16 v[140:143], a[186:189], a[220:223], v[140:143]\n\t"
    "ds_read_b128 a[132:135], v71 offset:28416\n\t"
    "buffer_load_dwordx4 v117, s[84:87], s92 offen offset:3072 lds\n\t"
    "v_mfma_f32_16x16x32_bf16 v[144:147], a[64:67], a[204:207], 0\n\t"
    "s_waitcnt lgkmcnt(7)\n\t"
    "ds_read_b128 a[136:139], v71 offset:28480\n\t"
    "v_cvt_pk_bf16_f32 v172, v136, v137\n\t"
    "v_mfma_f32_16x16x32_bf16 v[144:147], a[68:71], a[208:211], v[144:147]\n\t"
    "ds_read_b128 a[140:143], v71 offset:28544\n\t"
    "v_cvt_pk_bf16_f32 v173, v138, v139\n\t"
    "v_mfma_f32_16x16x32_bf16 v[144:147], a[108:111], a[212:215], v[144:147]\n\t"
    "ds_read_b128 a[144:147], v71 offset:28608\n\t"
    "v_mfma_f32_16x16x32_bf16 v[144:147], a[112:115], a[216:219], v[144:147]\n\t"
    "s_waitcnt lgkmcnt(7)\n\t"
    "ds_read_b64 a[194:195], v48 offset:29824\n\t"
    "s_add_u32 s92, s55, 4096\n\t"
    "s_add_u32 m0, s55, 41984\n\t"
    "s_nop 0\n\t"
    "buffer_load_dwordx4 v117, s[84:87], s92 offen offset:0 lds\n\t"
    "v_mfma_f32_16x16x32_bf16 v[144:147], a[188:191], a[220:223], v[144:147]\n\t"
    "ds_read_b128 a[148:151], v71 offset:29568\n\t"
    "v_cvt_pk_bf16_f32 v174, v140, v141\n\t"
    "v_mfma_f32_16x16x32_bf16 v[148:151], a[116:119], a[204:207], 0\n\t"
    "ds_read_b128 a[152:155], v71 offset:29632\n\t"
    "v_cvt_pk_bf16_f32 v175, v142, v143\n\t"
    "v_mfma_f32_16x16x32_bf16 v[148:151], a[120:123], a[208:211], v[148:151]\n\t"
    "s_waitcnt lgkmcnt(7)\n\t"
    "ds_read_b128 a[156:159], v71 offset:29696\n\t"
    "v_mfma_f32_16x16x32_bf16 v[148:151], a[124:127], a[212:215], v[148:151]\n\t"
    "ds_read_b128 a[160:163], v71 offset:29760\n\t"
    "buffer_load_dwordx4 v117, s[84:87], s92 offen offset:1024 lds\n\t"
    "v_mfma_f32_16x16x32_bf16 v[148:151], a[128:131], a[216:219], v[148:151]\n\t"
    "ds_read_b64 a[196:197], v73 offset:256\n\t"
    "v_mfma_f32_16x16x32_bf16 v[148:151], a[190:193], a[220:223], v[148:151]\n\t"
    "s_waitcnt lgkmcnt(7)\n\t"
    "ds_read_b128 a[164:167], v72 offset:0\n\t"
    "v_cvt_pk_bf16_f32 v176, v144, v145\n\t"
    "v_mfma_f32_16x16x32_bf16 v[152:155], a[132:135], a[204:207], 0\n\t"
    "ds_read_b128 a[168:171], v72 offset:64\n\t"
    "v_cvt_pk_bf16_f32 v177, v146, v147\n\t"
    "v_mfma_f32_16x16x32_bf16 v[152:155], a[136:139], a[208:211], v[152:155]\n\t"
    "ds_read_b128 a[172:175], v72 offset:128\n\t"
    "buffer_load_dwordx4 v117, s[84:87], s92 offen offset:2048 lds\n\t"
    "v_mfma_f32_16x16x32_bf16 v[152:155], a[140:143], a[212:215], v[152:155]\n\t"
    "s_waitcnt lgkmcnt(7)\n\t"
    "ds_read_b128 a[176:179], v72 offset:192\n\t"
    "v_mfma_f32_16x16x32_bf16 v[152:155], a[144:147], a[216:219], v[152:155]\n\t"
    "v_mfma_f32_16x16x32_bf16 v[152:155], a[192:195], a[220:223], v[152:155]\n\t"
    "v_cvt_pk_bf16_f32 v178, v148, v149\n\t"
    "v_mfma_f32_16x16x32_bf16 v[156:159], a[148:151], a[204:207], 0\n\t"
    "s_waitcnt lgkmcnt(5)\n\t"
    "buffer_load_dwordx4 v117, s[84:87], s92 offen offset:3072 lds\n\t"
    "v_mfma_f32_16x16x32_bf16 v[156:159], a[152:155], a[208:211], v[156:159]\n\t"
    "v_cvt_pk_bf16_f32 v179, v150, v151\n\t"
    "v_mfma_f32_16x16x32_bf16 v[156:159], a[156:159], a[212:215], v[156:159]\n\t"
    "v_mfma_f32_16x16x32_bf16 v[156:159], a[160:163], a[216:219], v[156:159]\n\t"
    "s_waitcnt lgkmcnt(2)\n\t"
    "v_mfma_f32_16x16x32_bf16 v[156:159], a[194:197], a[220:223], v[156:159]\n\t"
    "s_add_u32 s92, s55, 8192\n\t"
    "s_add_u32 m0, s55, 46080\n\t"
    "s_nop 0\n\t"
    "buffer_load_dwordx4 v117, s[84:87], s92 offen offset:0 lds\n\t"
    "v_mfma_f32_16x16x32_bf16 v[160:163], a[164:167], a[204:207], 0\n\t"
    "v_cvt_pk_bf16_f32 v180, v152, v153\n\t"
    "v_mfma_f32_16x16x32_bf16 v[160:163], a[168:171], a[208:211], v[160:163]\n\t"
    "s_waitcnt lgkmcnt(0)\n\t"
    "v_cvt_pk_bf16_f32 v181, v154, v155\n\t"
    "v_mfma_f32_16x16x32_bf16 v[160:163], a[172:175], a[212:215], v[160:163]\n\t"
    "v_mfma_f32_16x16x32_bf16 v[160:163], a[176:179], a[216:219], v[160:163]\n\t"
    "buffer_load_dwordx4 v117, s[84:87], s92 offen offset:1024 lds\n\t"
    "v_mfma_f32_16x16x32_bf16 v[160:163], a[196:199], a[220:223], v[160:163]\n\t"
    "v_cvt_pk_bf16_f32 v182, v156, v157\n\t"
    "buffer_load_dwordx4 v117, s[84:87], s92 offen offset:1152 lds\n\t"
    "buffer_load_dwordx2 v[232:233], v252, s[80:83], s38 offen sc1\n\t"
    "ds_read_b128 a[224:227], v54 offset:28672\n\t"
    "ds_read_b128 a[228:231], v54 offset:30720\n\t"
    "ds_read_b128 a[232:235], v54 offset:32768\n\t"
    "ds_read_b128 a[236:239], v54 offset:34816\n\t"
    "ds_read_b64 a[240:241], v120 offset:28672\n\t"
    "v_cvt_pk_bf16_f32 v183, v158, v159\n\t"
    "v_cvt_pk_bf16_f32 v184, v160, v161\n\t"
    "v_cvt_pk_bf16_f32 v185, v162, v163\n\t"
    "ds_write_b128 v49, v[168:171] offset:0\n\t"
    "ds_write_b128 v49, v[172:175] offset:2048\n\t"
    "v_dot2c_f32_bf16 v206, v168, v186\n\t"
    "v_dot2c_f32_bf16 v207, v168, v62\n\t"
    "v_dot2c_f32_bf16 v206, v169, v187\n\t"
    "v_dot2c_f32_bf16 v207, v169, v62\n\t"
    "v_dot2c_f32_bf16 v206, v170, v188\n\t"
    "v_dot2c_f32_bf16 v207, v170, v62\n\t"
    "v_dot2c_f32_bf16 v206, v171, v189\n\t"
    "v_dot2c_f32_bf16 v207, v171, v62\n\t"
    "v_dot2c_f32_bf16 v206, v172, v190\n\t"
    "v_dot2c_f32_bf16 v207, v172, v62\n\t"
    "v_dot2c_f32_bf16 v206, v173, v191\n\t"
    "v_dot2c_f32_bf16 v207, v173, v62\n\t"
    "v_dot2c_f32_bf16 v206, v174, v192\n\t"
    "v_dot2c_f32_bf16 v207, v174, v62\n\t"
    "v_dot2c_f32_bf16 v206, v175, v193\n\t"
    "v_dot2c_f32_bf16 v207, v175, v62\n\t"
    "v_dot2c_f32_bf16 v206, v176, v194\n\t"
    "v_dot2c_f32_bf16 v207, v176, v62\n\t"
    "ds_write_b128 v49, v[176:179] offset:4096\n\t"
    "ds_write_b128 v49, v[180:183] offset:6144\n\t"
    "v_dot2c_f32_bf16 v206, v177, v195\n\t"
    "v_dot2c_f32_bf16 v207, v177, v62\n\t"
    "v_dot2c_f32_bf16 v206, v178, v196\n\t"
    "v_dot2c_f32_bf16 v207, v178, v62\n\t"
    "v_dot2c_f32_bf16 v206, v179, v197\n\t"
    "v_dot2c_f32_bf16 v207, v179, v62\n\t"
    "v_dot2c_f32_bf16 v206, v180, v198\n\t"
    "v_dot2c_f32_bf16 v207, v180, v62\n\t"
    "v_dot2c_f32_bf16 v206, v181, v199\n\t"
    "v_dot2c_f32_bf16 v207, v181, v62\n\t"
    "v_dot2c_f32_bf16 v206, v182, v200\n\t"
    "v_dot2c_f32_bf16 v207, v182, v62\n\t"
    "v_dot2c_f32_bf16 v206, v183, v201\n\t"
    "v_dot2c_f32_bf16 v207, v183, v62\n\t"
    "v_dot2c_f32_bf16 v206, v184, v202\n\t"
    "v_dot2c_f32_bf16 v207, v184, v62\n\t"
    "v_dot2c_f32_bf16 v206, v185, v203\n\t"
    "v_dot2c_f32_bf16 v207, v185, v62\n\t"
    "ds_write_b64 v78, v[184:185] offset:0\n\t"
    "s_nop 0\n\t"
    "v_mul_f32 v206, 0x41051592, v206\n\t"
    "v_fmac_f32 v206, 0x3f000000, v207\n\t"
    "v_mov_b32 v36, v206\n\t"
    "s_nop 1\n\t"
    "v_permlane16_swap_b32 v36, v206\n\t"
    "v_add_f32 v206, v206, v36\n\t"
    "v_mov_b32 v36, v206\n\t"
    "s_nop 1\n\t"
    "v_permlane32_swap_b32 v36, v206\n\t"
    "v_add_f32 v206, v206, v36\n\t"
    "ds_write_b32 v79, v206 offset:0\n\t"
    "s_waitcnt vmcnt(0)\n\t"
    "s_nop 0\n\t"
    "v_cmp_ne_u64 vcc, v[232:233], v[234:235]\n\t"
    "s_nop 4\n\t"
    "s_cbranch_vccz PKD_%=\n\t"
    "s_mov_b32 s95, 0\n\t"
    "PKL_%=:\n\t"
    "s_sleep 1\n\t"
    "buffer_load_dwordx2 v[232:233], v252, s[80:83], s38 offen sc1\n\t"
    "s_waitcnt vmcnt(0)\n\t"
    "v_cmp_ne_u64 vcc, v[232:233], v[234:235]\n\t"
    "s_nop 1\n\t"
    "s_add_u32 s95, s95, 1\n\t"
    "s_cmp_lt_u32 s95, 0x10000\n\t"
    "s_cselect_b64 vcc, vcc, 0\n\t"
    "s_nop 4\n\t"
    "s_cbranch_vccnz PKL_%=\n\t"
    "PKD_%=:\n\t"
    "s_nop 1\n\t"
    "s_waitcnt lgkmcnt(0)\n\t"
    "s_barrier\n\t"
    "ds_read_b64_tr_b16 a[36:37], v50 offset:288\n\t"
    "ds_read_b64_tr_b16 a[38:39], v50 offset:2592\n\t"
    "ds_read_b64_tr_b16 a[40:41], v50 offset:9504\n\t"
    "ds_read_b64_tr_b16 a[42:43], v50 offset:11808\n\t"
    "s_add_u32 m0, s61, 0\n\t"
    "s_nop 0\n\t"
    "buffer_load_dwordx4 v101, s[64:67], 0 offen offset:0 sc1 lds\n\t"
    "buffer_load_dwordx4 v101, s[64:67], 0 offen offset:1024 sc1 lds\n\t"
    "ds_read_b64_tr_b16 a[44:45], v50 offset:18720\n\t"
    "ds_read_b64_tr_b16 a[46:47], v50 offset:21024\n\t"
    "ds_read_b64_tr_b16 a[48:49], v50 offset:27936\n\t"
    "ds_read_b64_tr_b16 a[50:51], v50 offset:30240\n\t"
    "ds_read_b64_tr_b16 a[52:53], v50 offset:37152\n\t"
    "ds_read_b64_tr_b16 a[54:55], v50 offset:39456\n\t"
    "buffer_load_dwordx4 v102, s[64:67], 0 offen offset:2048 sc1 lds\n\t"
    "buffer_load_dwordx4 v102, s[64:67], 0 offen offset:3072 sc1 lds\n\t"
    "ds_read_b64 v[186:187], v47 offset:0\n\t"
    "v_accvgpr_write_b32 a126, 0\n\t"
    "v_accvgpr_write_b32 a127, 0\n\t"
    "s_waitcnt lgkmcnt(9)\n\t"
    "v_mfma_f32_32x32x16_bf16 v[0:15], a[36:39], a[72:75], 0\n\t"
    "s_add_u32 m0, s61, 4096\n\t"
    "s_nop 0\n\t"
    "buffer_load_dwordx4 v103, s[64:67], 0 offen offset:0 sc1 lds\n\t"
    "ds_read_b64_tr_b16 a[56:57], v50 offset:46368\n\t"
    "ds_read_b64_tr_b16 a[58:59], v50 offset:48672\n\t"
    "ds_read_b64_tr_b16 a[60:61], v50 offset:55584\n\t"
    "ds_read_b64_tr_b16 a[62:63], v50 offset:57888\n\t"
    "s_waitcnt lgkmcnt(11)\n\t"
    "v_mfma_f32_32x32x16_bf16 v[0:15], a[40:43], a[76:79], v[0:15]\n\t"
    "buffer_load_dwordx4 v103, s[64:67], 0 offen offset:1024 sc1 lds\n\t"
    "ds_read_b64_tr_b16 a[64:65], v51 offset:288\n\t"
    "ds_read_b64_tr_b16 a[66:67], v51 offset:2592\n\t"
    "ds_read_b64_tr_b16 a[68:69], v51 offset:9504\n\t"
    "ds_read_b64_tr_b16 a[70:71], v51 offset:11808\n\t"
    "s_waitcnt lgkmcnt(13)\n\t"
    "v_mfma_f32_32x32x16_bf16 v[0:15], a[44:47], a[80:83], v[0:15]\n\t"
    "buffer_load_dwordx4 v104, s[64:67], 0 offen offset:2048 sc1 lds\n\t"
    "ds_read_b64_tr_b16 a[0:1], v50 offset:0\n\t"
    "ds_read_b64_tr_b16 a[2:3], v50 offset:2304\n\t"
    "ds_read_b64_tr_b16 a[4:5], v50 offset:9216\n\t"
    "ds_read_b64_tr_b16 a[6:7], v50 offset:11520\n\t"
    "s_waitcnt lgkmcnt(14)\n\t"
    "v_mfma_f32_32x32x16_bf16 v[0:15], a[48:51], a[84:87], v[0:15]\n\t"
    "buffer_load_dwordx4 v104, s[64:67], 0 offen offset:3072 sc1 lds\n\t"
    "ds_read_b64_tr_b16 a[8:9], v50 offset:18432\n\t"
    "ds_read_b64_tr_b16 a[10:11], v50 offset:20736\n\t"
    "ds_read_b64_tr_b16 a[12:13], v50 offset:27648\n\t"
    "ds_read_b64_tr_b16 a[14:15], v50 offset:29952\n\t"
    "s_waitcnt lgkmcnt(14)\n\t"
    "v_mfma_f32_32x32x16_bf16 v[0:15], a[52:55], a[88:91], v[0:15]\n\t"
    "s_add_u32 m0, s61, 8192\n\t"
    "s_nop 0\n\t"
    "buffer_load_dwordx4 v105, s[64:67], 0 offen offset:0 sc1 lds\n\t"
    "ds_read_b64_tr_b16 a[16:17], v50 offset:36864\n\t"
    "ds_read_b64_tr_b16 a[18:19], v50 offset:39168\n\t"
    "ds_read_b64_tr_b16 a[20:21], v50 offset:46080\n\t"
    "ds_read_b64_tr_b16 a[22:23], v50 offset:48384\n\t"
    "v_mfma_f32_32x32x16_bf16 v[0:15], a[56:59], a[92:95], v[0:15]\n\t"
    "ds_read_b64_tr_b16 a[24:25], v50 offset:55296\n\t"
    "ds_read_b64_tr_b16 a[26:27], v50 offset:57600\n\t"
    "ds_read_b64_tr_b16 a[28:29], v51 offset:0\n\t"
    "ds_read_b64_tr_b16 a[30:31], v51 offset:2304\n\t"
    "v_sub_f32 v64, 0, v187\n\t"
    "v_sub_f32 v60, 0, v186\n\t"
    "s_waitcnt lgkmcnt(14)\n\t"
    "v_mfma_f32_32x32x16_bf16 v[0:15], a[60:63], a[96:99], v[0:15]\n\t"
    "ds_read_b64_tr_b16 a[32:33], v51 offset:9216\n\t"
    "ds_read_b64_tr_b16 a[34:35], v51 offset:11520\n\t"
    "ds_read_b64_tr_b16 a[108:109], v52 offset:0\n\t"
    "ds_read_b64_tr_b16 a[110:111], v52 offset:2304\n\t"
    "v_mov_b32 v65, v64\n\t"
    "v_mov_b32 v66, v64\n\t"
    "v_mfma_f32_32x32x16_bf16 v[0:15], a[64:67], a[100:103], v[0:15]\n\t"
    "ds_read_b64_tr_b16 a[112:113], v52 offset:18432\n\t"
    "ds_read_b64_tr_b16 a[114:115], v52 offset:20736\n\t"
    "ds_read_b64_tr_b16 a[116:117], v52 offset:36864\n\t"
    "ds_read_b64_tr_b16 a[118:119], v52 offset:39168\n\t"
    "v_mov_b32 v67, v64\n\t"
    "v_mov_b32 v68, v64\n\t"
    "v_mfma_f32_32x32x16_bf16 v[0:15], a[68:71], a[104:107], v[0:15]\n\t"
    "ds_read_b64_tr_b16 a[120:121], v52 offset:55296\n\t"
    "ds_read_b64_tr_b16 a[122:123], v52 offset:57600\n\t"
    "ds_read_b64_tr_b16 a[124:125], v53 offset:0\n\t"
    "v_mov_b32 v69, v64\n\t"
    "v_mov_b32 v70, v64\n\t"
    "v_mfma_f32_32x32x16_bf16 v[16:31], a[72:75], a[0:3], 0\n\t"
    "ds_read_b128 a[144:147], v56 offset:19456\n\t"
    "ds_read_b128 a[148:151], v56 offset:20480\n\t"
    "v_mov_b32 v71, v64\n\t"
    "v_mov_b32 v72, v64\n\t"
    "v_mov_b32 v73, v64\n\t"
    "v_mov_b32_e64 v74, v64\n\t"
    "s_waitcnt lgkmcnt(14)\n\t"
    "v_mfma_f32_32x32x16_bf16 v[16:31], a[76:79], a[4:7], v[16:31]\n\t"
    "ds_read_b128 a[152:155], v57 offset:19456\n\t"
    "ds_read_b128 a[156:159], v57 offset:20480\n\t"
    "v_mov_b32 v75, v64\n\t"
    "v_mov_b32 v76, v64\n\t"
    "v_mov_b32 v77, v64\n\t"
    "v_mov_b32 v78, v64\n\t"
    "v_mfma_f32_32x32x16_bf16 v[16:31], a[80:83], a[8:11], v[16:31]\n\t"
    "ds_read_b128 a[160:163], v58 offset:19456\n\t"
    "ds_read_b128 a[164:167], v58 offset:20480\n\t"
    "v_mov_b32 v79, v64\n\t"
    "v_mov_b32 v80, v60\n\t"
    "v_mov_b32 v81, v60\n\t"
    "v_mov_b32 v82, v60\n\t"
    "s_waitcnt lgkmcnt(6)\n\t"
    "s_barrier\n\t"
    "v_mfma_f32_16x16x32_bf16 v[32:35], a[108:111], a[224:227], 0\n\t"
    "s_add_u32 m0, s61, 36864\n\t"
    "s_nop 0\n\t"
    "buffer_load_dwordx4 v101, s[64:67], s72 offen offset:0 sc1 lds\n\t"
    "v_mfma_f32_16x16x32_bf16 v[32:35], a[112:115], a[228:231], v[32:35]\n\t"
    "ds_read_b128 a[168:171], v59 offset:19456\n\t"
    "v_mfma_f32_16x16x32_bf16 v[32:35], a[116:119], a[232:235], v[32:35]\n\t"
    "buffer_load_dwordx4 v101, s[64:67], s72 offen offset:1024 sc1 lds\n\t"
    "v_mfma_f32_16x16x32_bf16 v[32:35], a[120:123], a[236:239], v[32:35]\n\t"
    "ds_read_b128 a[172:175], v59 offset:20480\n\t"
    "v_mfma_f32_16x16x32_bf16 v[32:35], a[124:127], a[240:243], v[32:35]\n\t"
    "buffer_load_dwordx4 v102, s[64:67], s72 offen offset:2048 sc1 lds\n\t"
    "v_mfma_f32_32x32x16_bf16 v[16:31], a[84:87], a[12:15], v[16:31]\n\t"
    "buffer_load_dwordx4 v102, s[64:67], s72 offen offset:3072 sc1 lds\n\t"
    "ds_read_b128 a[176:179], v119 offset:27648\n\t"
    "ds_read_b128 a[180:183], v56 offset:48640\n\t"
    "v_mov_b32 v83, v60\n\t"
    "v_mov_b32 v84, v60\n\t"
    "v_mfma_f32_32x32x16_bf16 v[16:31], a[88:91], a[16:19], v[16:31]\n\t"
    "s_add_u32 m0, s61, 40960\n\t"
    "s_nop 0\n\t"
    "buffer_load_dwordx4 v103, s[64:67], s72 offen offset:0 sc1 lds\n\t"
    "ds_read_b128 a[184:187], v56 offset:49664\n\t"
    "ds_read_b128 a[188:191], v57 offset:48640\n\t"
    "v_mov_b32 v85, v60\n\t"
    "v_mov_b32 v86, v60\n\t"
    "v_mfma_f32_32x32x16_bf16 v[16:31], a[92:95], a[20:23], v[16:31]\n\t"
    "buffer_load_dwordx4 v103, s[64:67], s72 offen offset:1024 sc1 lds\n\t"
    "ds_read_b128 a[192:195], v57 offset:49664\n\t"
    "ds_read_b128 a[196:199], v58 offset:48640\n\t"
    "v_mov_b32 v87, v60\n\t"
    "v_mov_b32 v88, v60\n\t"
    "v_mfma_f32_32x32x16_bf16 v[16:31], a[96:99], a[24:27], v[16:31]\n\t"
    "buffer_load_dwordx4 v104, s[64:67], s72 offen offset:2048 sc1 lds\n\t"
    "ds_read_b128 a[200:203], v58 offset:49664\n\t"
    "ds_read_b128 a[204:207], v59 offset:48640\n\t"
    "v_mov_b32 v89, v60\n\t"
    "v_mov_b32 v90, v60\n\t"
    "v_mfma_f32_32x32x16_bf16 v[16:31], a[100:103], a[28:31], v[16:31]\n\t"
    "buffer_load_dwordx4 v104, s[64:67], s72 offen offset:3072 sc1 lds\n\t"
    "ds_read_b128 a[208:211], v59 offset:49664\n\t"
    "ds_read_b128 a[212:215], v119 offset:56832\n\t"
    "v_mov_b32 v91, v60\n\t"
    "v_mov_b32 v92, v60\n\t"
    "v_mfma_f32_32x32x16_bf16 v[16:31], a[104:107], a[32:35], v[16:31]\n\t"
    "s_add_u32 m0, s61, 45056\n\t"
    "s_nop 0\n\t"
    "buffer_load_dwordx4 v105, s[64:67], s72 offen offset:0 sc1 lds\n\t"
    "v_mov_b32 v93, v60\n\t"
    "v_mov_b32 v94, v60\n\t"
    "v_mov_b32_e64 v95, v60\n\t"
    "s_waitcnt vmcnt(9)\n\t"
    "ds_read_b128 a[216:219], v96 offset:0\n\t"
    "ds_read_b128 a[220:223], v97 offset:1024\n\t"
    "ds_read_b128 a[224:227], v96 offset:2048\n\t"
    "ds_read_b128 a[228:231], v97 offset:3072\n\t"
    "ds_read_b128 a[232:235], v96 offset:4096\n\t"
    "ds_read_b128 a[236:239], v97 offset:5120\n\t"
    "ds_read_b128 a[240:243], v96 offset:6144\n\t"
    "ds_read_b128 a[244:247], v97 offset:7168\n\t"
    "ds_read_b128 a[248:251], v96 offset:8192\n\t"
    "s_mov_b32 s72, 18432\n\t"
    "s_add_u32 m0, s61, 73728\n\t"
    "s_waitcnt lgkmcnt(8)\n\t"
    "v_mfma_f32_32x32x16_bf16 v[128:143], a[216:219], a[144:147], v[64:79]\n\t"
    "buffer_load_dwordx4 v101, s[64:67], s72 offen offset:0 sc1 lds\n\t"
    "ds_read_b64_tr_b16 v[176:177], v98 offset:0\n\t"
    "ds_read_b64_tr_b16 v[178:179], v98 offset:256\n\t"
    "v_mul_f32 v36, v0, v0\n\t"
    "v_mul_f32 v37, v1, v1\n\t"
    "v_fma_f32 v36, v36, s62, v63\n\t"
    "s_waitcnt lgkmcnt(9)\n\t"
    "v_mfma_f32_32x32x16_bf16 v[128:143], a[220:223], a[148:151], v[128:143]\n\t"
    "buffer_load_dwordx4 v101, s[64:67], s72 offen offset:1024 sc1 lds\n\t"
    "ds_read_b64_tr_b16 v[180:181], v99 offset:1024\n\t"
    "ds_read_b64_tr_b16 v[182:183], v99 offset:1280\n\t"
    "v_fma_f32 v37, v37, s62, v63\n\t"
    "v_mul_f32 v36, v36, v0\n\t"
    "v_mul_f32_e64 v37, v37, v1\n\t"
    "s_waitcnt lgkmcnt(10)\n\t"
    "v_mfma_f32_32x32x16_bf16 v[128:143], a[224:227], a[152:155], v[128:143]\n\t"
    "buffer_load_dwordx4 v102, s[64:67], s72 offen offset:2048 sc1 lds\n\t"
    "ds_read_b64_tr_b16 v[184:185], v98 offset:2048\n\t"
    "ds_read_b64_tr_b16 v[186:187], v98 offset:2304\n\t"
    "v_exp_f32 v36, v36\n\t"
    "s_waitcnt lgkmcnt(11)\n\t"
    "v_mfma_f32_32x32x16_bf16 v[128:143], a[228:231], a[156:159], v[128:143]\n\t"
    "buffer_load_dwordx4 v102, s[64:67], s72 offen offset:3072 sc1 lds\n\t"
    "ds_read_b64_tr_b16 v[188:189], v99 offset:3072\n\t"
    "ds_read_b64_tr_b16 v[190:191], v99 offset:3328\n\t"
    "s_add_u32 m0, s61, 77824\n\t"
    "v_exp_f32 v37, v37\n\t"
    "s_waitcnt lgkmcnt(12)\n\t"
    "v_mfma_f32_32x32x16_bf16 v[128:143], a[232:235], a[160:163], v[128:143]\n\t"
    "buffer_load_dwordx4 v103, s[64:67], s72 offen offset:0 sc1 lds\n\t"
    "ds_read_b64_tr_b16 v[192:193], v98 offset:4096\n\t"
    "ds_read_b64_tr_b16 v[194:195], v98 offset:4352\n\t"
    "v_add_f32 v36, 1.0, v36\n\t"
    "v_add_f32_e64 v37, 1.0, v37\n\t"
    "s_waitcnt lgkmcnt(13)\n\t"
    "v_mfma_f32_32x32x16_bf16 v[128:143], a[236:239], a[164:167], v[128:143]\n\t"
    "buffer_load_dwordx4 v103, s[64:67], s72 offen offset:1024 sc1 lds\n\t"
    "ds_read_b64_tr_b16 v[196:197], v99 offset:5120\n\t"
    "ds_read_b64_tr_b16 v[198:199], v99 offset:5376\n\t"
    "v_rcp_f32 v36, v36\n\t"
    "s_waitcnt lgkmcnt(14)\n\t"
    "v_mfma_f32_32x32x16_bf16 v[128:143], a[240:243], a[168:171], v[128:143]\n\t"
    "buffer_load_dwordx4 v104, s[64:67], s72 offen offset:2048 sc1 lds\n\t"
    "ds_read_b64_tr_b16 v[200:201], v98 offset:6144\n\t"
    "ds_read_b64_tr_b16 v[202:203], v98 offset:6400\n\t"
    "v_rcp_f32 v37, v37\n\t"
    "v_mul_f32_e64 v0, v0, v36\n\t"
    "s_waitcnt lgkmcnt(14)\n\t"
    "v_mfma_f32_32x32x16_bf16 v[128:143], a[244:247], a[172:175], v[128:143]\n\t"
    "buffer_load_dwordx4 v104, s[64:67], s72 offen offset:3072 sc1 lds\n\t"
    "ds_read_b64_tr_b16 v[204:205], v99 offset:7168\n\t"
    "ds_read_b64_tr_b16 v[206:207], v99 offset:7424\n\t"
    "s_add_u32 m0, s61, 81920\n\t"
    "v_mul_f32 v1, v1, v37\n\t"
    "v_mul_f32 v36, v2, v2\n\t"
    "v_mfma_f32_32x32x16_bf16 v[128:143], a[248:251], a[176:179], v[128:143]\n\t"
    "buffer_load_dwordx4 v105, s[64:67], s72 offen offset:0 sc1 lds\n\t"
    "ds_read_b64_tr_b16 v[208:209], v98 offset:8192\n\t"
    "ds_read_b64_tr_b16 v[210:211], v98 offset:8448\n\t"
    "v_mul_f32_e64 v37, v3, v3\n\t"
    "v_fma_f32 v36, v36, s62, v63\n\t"
    "v_fma_f32 v37, v37, s62, v63\n\t"
    "v_mfma_f32_32x32x16_bf16 v[144:159], a[216:219], a[180:183], v[80:95]\n\t"
    "v_mul_f32 v36, v36, v2\n\t"
    "v_mul_f32 v37, v37, v3\n\t"
    "v_exp_f32 v36, v36\n\t"
    "v_exp_f32 v37, v37\n\t"
    "v_add_f32 v36, 1.0, v36\n\t"
    "v_mfma_f32_32x32x16_bf16 v[144:159], a[220:223], a[184:187], v[144:159]\n\t"
    "v_add_f32 v37, 1.0, v37\n\t"
    "v_rcp_f32 v36, v36\n\t"
    "v_rcp_f32 v37, v37\n\t"
    "v_mul_f32 v2, v2, v36\n\t"
    "v_mul_f32 v3, v3, v37\n\t"
    "v_mfma_f32_32x32x16_bf16 v[144:159], a[224:227], a[188:191], v[144:159]\n\t"
    "v_exp_f32 v128, v128\n\t"
    "v_exp_f32 v129, v129\n\t"
    "v_mul_f32 v36, v4, v4\n\t"
    "v_mul_f32 v37, v5, v5\n\t"
    "v_fma_f32 v36, v36, s62, v63\n\t"
    "v_mfma_f32_32x32x16_bf16 v[144:159], a[228:231], a[192:195], v[144:159]\n\t"
    "v_exp_f32 v132, v132\n\t"
    "v_exp_f32 v133, v133\n\t"
    "v_exp_f32 v130, v130\n\t"
    "v_fma_f32 v37, v37, s62, v63\n\t"
    "v_mfma_f32_32x32x16_bf16 v[144:159], a[232:235], a[196:199], v[144:159]\n\t"
    "v_exp_f32 v131, v131\n\t"
    "v_exp_f32 v134, v134\n\t"
    "v_exp_f32 v135, v135\n\t"
    "v_mul_f32 v36, v36, v4\n\t"
    "v_mfma_f32_32x32x16_bf16 v[144:159], a[236:239], a[200:203], v[144:159]\n\t"
    "v_exp_f32 v136, v136\n\t"
    "v_exp_f32 v137, v137\n\t"
    "v_exp_f32 v140, v140\n\t"
    "v_mul_f32 v37, v37, v5\n\t"
    "v_mfma_f32_32x32x16_bf16 v[144:159], a[240:243], a[204:207], v[144:159]\n\t"
    "v_exp_f32 v141, v141\n\t"
    "v_exp_f32 v138, v138\n\t"
    "v_exp_f32 v139, v139\n\t"
    "v_cvt_pk_bf16_f32 v160, v128, v129\n\t"
    "v_mfma_f32_32x32x16_bf16 v[144:159], a[244:247], a[208:211], v[144:159]\n\t"
    "v_cvt_pk_bf16_f32 v164, v132, v133\n\t"
    "v_exp_f32 v142, v142\n\t"
    "v_exp_f32_e64 v143, v143\n\t"
    "v_cvt_pk_bf16_f32 v161, v130, v131\n\t"
    "v_permlane16_swap_b32 v160, v164\n\t"
    "v_mfma_f32_32x32x16_bf16 v[144:159], a[248:251], a[212:215], v[144:159]\n\t"
    "s_waitcnt vmcnt(9)\n\t"
    "s_waitcnt lgkmcnt(0)\n\t"
    "v_cvt_pk_bf16_f32 v165, v134, v135\n\t"
    "v_cvt_pk_bf16_f32 v162, v136, v137\n\t"
    "v_cvt_pk_bf16_f32 v166, v140, v141\n\t"
    "v_permlane16_swap_b32 v161, v165\n\t"
    "v_cvt_pk_bf16_f32 v163, v138, v139\n\t"
    "v_cvt_pk_bf16_f32 v167, v142, v143\n\t"
    "v_permlane16_swap_b32 v162, v166\n\t"
    "s_nop 0\n\t"
    "v_permlane16_swap_b32 v163, v167\n\t"
    "s_nop 1\n\t"
    "v_mfma_f32_16x16x32_bf16 a[0:3], v[176:179], v[160:163], 0\n\t"
    "ds_read_b128 a[216:219], v96 offset:36864\n\t"
    "ds_read_b128 a[220:223], v97 offset:37888\n\t"
    "v_mfma_f32_16x16x32_bf16 a[8:11], v[176:179], v[164:167], 0\n\t"
    "ds_read_b128 a[224:227], v96 offset:38912\n\t"
    "ds_read_b128 a[228:231], v97 offset:39936\n\t"
    "v_mfma_f32_16x16x32_bf16 a[4:7], v[180:183], v[160:163], 0\n\t"
    "ds_read_b128 a[232:235], v96 offset:40960\n\t"
    "ds_read_b128 a[236:239], v97 offset:41984\n\t"
    "v_mfma_f32_16x16x32_bf16 a[12:15], v[180:183], v[164:167], 0\n\t"
    "ds_read_b128 a[240:243], v96 offset:43008\n\t"
    "ds_read_b128 a[244:247], v97 offset:44032\n\t"
    "v_mfma_f32_16x16x32_bf16 a[16:19], v[184:187], v[160:163], 0\n\t"
    "ds_read_b128 a[248:251], v96 offset:45056\n\t"
    "v_exp_f32 v36, v36\n\t"
    "v_mfma_f32_16x16x32_bf16 a[24:27], v[184:187], v[164:167], 0\n\t"
    "v_exp_f32 v37, v37\n\t"
    "v_add_f32 v36, 1.0, v36\n\t"
    "v_mfma_f32_16x16x32_bf16 a[20:23], v[188:191], v[160:163], 0\n\t"
    "v_exp_f32 v144, v144\n\t"
    "v_add_f32 v37, 1.0, v37\n\t"
    "v_mfma_f32_16x16x32_bf16 a[28:31], v[188:191], v[164:167], 0\n\t"
    "v_exp_f32 v145, v145\n\t"
    "v_mfma_f32_16x16x32_bf16 a[32:35], v[192:195], v[160:163], 0\n\t"
    "v_exp_f32 v148, v148\n\t"
    "v_mfma_f32_16x16x32_bf16 a[40:43], v[192:195], v[164:167], 0\n\t"
    "v_exp_f32 v149, v149\n\t"
    "v_mfma_f32_16x16x32_bf16 a[36:39], v[196:199], v[160:163], 0\n\t"
    "v_exp_f32 v146, v146\n\t"
    "v_mfma_f32_16x16x32_bf16 a[44:47], v[196:199], v[164:167], 0\n\t"
    "v_exp_f32 v147, v147\n\t"
    "v_mfma_f32_16x16x32_bf16 a[48:51], v[200:203], v[160:163], 0\n\t"
    "v_exp_f32 v150, v150\n\t"
    "v_mfma_f32_16x16x32_bf16 a[56:59], v[200:203], v[164:167], 0\n\t"
    "v_exp_f32 v151, v151\n\t"
    "v_mfma_f32_16x16x32_bf16 a[52:55], v[204:207], v[160:163], 0\n\t"
    "v_exp_f32 v152, v152\n\t"
    "v_mfma_f32_16x16x32_bf16 a[60:63], v[204:207], v[164:167], 0\n\t"
    "s_mov_b32 s72, 27648\n\t"
    "v_exp_f32 v153, v153\n\t"
    "v_mfma_f32_16x16x32_bf16 a[128:131], v[208:211], v[160:163], 0\n\t"
    "s_add_u32 m0, s61, 0\n\t"
    "v_exp_f32 v156, v156\n\t"
    "v_mfma_f32_16x16x32_bf16 a[132:135], v[208:211], v[164:167], 0\n\t"
    "s_waitcnt lgkmcnt(0)\n\t"
    "v_exp_f32 v157, v157\n\t"
    "v_mfma_f32_32x32x16_bf16 v[128:143], a[216:219], a[144:147], v[64:79]\n\t"
    "buffer_load_dwordx4 v101, s[64:67], s72 offen offset:0 sc1 lds\n\t"
    "v_exp_f32 v154, v154\n\t"
    "v_exp_f32_e64 v155, v155\n\t"
    "v_mfma_f32_32x32x16_bf16 v[128:143], a[220:223], a[148:151], v[128:143]\n\t"
    "buffer_load_dwordx4 v101, s[64:67], s72 offen offset:1024 sc1 lds\n\t"
    "v_cvt_pk_bf16_f32 v168, v144, v145\n\t"
    "v_cvt_pk_bf16_f32 v172, v148, v149\n\t"
    "v_exp_f32 v158, v158\n\t"
    "v_mfma_f32_32x32x16_bf16 v[128:143], a[224:227], a[152:155], v[128:143]\n\t"
    "buffer_load_dwordx4 v102, s[64:67], s72 offen offset:2048 sc1 lds\n\t"
    "v_exp_f32 v159, v159\n\t"
    "v_cvt_pk_bf16_f32 v169, v146, v147\n\t"
    "v_rcp_f32_e64 v36, v36\n\t"
    "v_mfma_f32_32x32x16_bf16 v[128:143], a[228:231], a[156:159], v[128:143]\n\t"
    "buffer_load_dwordx4 v102, s[64:67], s72 offen offset:3072 sc1 lds\n\t"
    "s_add_u32 m0, s61, 4096\n\t"
    "v_permlane16_swap_b32 v168, v172\n\t"
    "v_cvt_pk_bf16_f32 v173, v150, v151\n\t"
    "v_cvt_pk_bf16_f32 v170, v152, v153\n\t"
    "v_mfma_f32_32x32x16_bf16 v[128:143], a[232:235], a[160:163], v[128:143]\n\t"
    "buffer_load_dwordx4 v103, s[64:67], s72 offen offset:0 sc1 lds\n\t"
    "v_cvt_pk_bf16_f32 v174, v156, v157\n\t"
    "v_permlane16_swap_b32 v169, v173\n\t"
    "v_rcp_f32_e64 v37, v37\n\t"
    "v_mfma_f32_32x32x16_bf16 v[128:143], a[236:239], a[164:167], v[128:143]\n\t"
    "buffer_load_dwordx4 v103, s[64:67], s72 offen offset:1024 sc1 lds\n\t"
    "v_cvt_pk_bf16_f32 v171, v154, v155\n\t"
    "v_cvt_pk_bf16_f32 v175, v158, v159\n\t"
    "v_permlane16_swap_b32 v170, v174\n\t"
    "v_mul_f32 v4, v4, v36\n\t"
    "v_mfma_f32_32x32x16_bf16 v[128:143], a[240:243], a[168:171], v[128:143]\n\t"
    "buffer_load_dwordx4 v104, s[64:67], s72 offen offset:2048 sc1 lds\n\t"
    "v_permlane16_swap_b32 v171, v175\n\t"
    "v_mul_f32 v5, v5, v37\n\t"
    "v_mul_f32 v36, v6, v6\n\t"
    "v_mul_f32 v37, v7, v7\n\t"
    "v_mfma_f32_32x32x16_bf16 v[128:143], a[244:247], a[172:175], v[128:143]\n\t"
    "buffer_load_dwordx4 v104, s[64:67], s72 offen offset:3072 sc1 lds\n\t"
    "s_add_u32 m0, s61, 8192\n\t"
    "v_fma_f32 v36, v36, s62, v63\n\t"
    "v_fma_f32 v37, v37, s62, v63\n\t"
    "v_mul_f32 v36, v36, v6\n\t"
    "v_mul_f32 v37, v37, v7\n\t"
    "v_mfma_f32_32x32x16_bf16 v[128:143], a[248:251], a[176:179], v[128:143]\n\t"
    "buffer_load_dwordx4 v105, s[64:67], s72 offen offset:0 sc1 lds\n\t"
    "v_exp_f32 v36, v36\n\t"
    "v_exp_f32 v37, v37\n\t"
    "v_add_f32 v36, 1.0, v36\n\t"
    "v_mfma_f32_16x16x32_bf16 a[64:67], v[176:179], v[168:171], 0\n\t"
    "v_add_f32 v37, 1.0, v37\n\t"
    "v_rcp_f32_e64 v36, v36\n\t"
    "v_mfma_f32_16x16x32_bf16 a[72:75], v[176:179], v[172:175], 0\n\t"
    "buffer_store_dwordx4 v[228:231], v113, s[80:83], s51 offen offset:0 sc1\n\t"
    "v_mfma_f32_16x16x32_bf16 a[68:71], v[180:183], v[168:171], 0\n\t"
    "ds_read_b64_tr_b16 v[176:177], v98 offset:36864\n\t"
    "ds_read_b64_tr_b16 v[178:179], v98 offset:37120\n\t"
    "v_mfma_f32_16x16x32_bf16 a[76:79], v[180:183], v[172:175], 0\n\t"
    "v_rcp_f32 v37, v37\n\t"
    "v_mul_f32 v6, v6, v36\n\t"
    "v_mfma_f32_16x16x32_bf16 a[80:83], v[184:187], v[168:171], 0\n\t"
    "ds_read_b64_tr_b16 v[180:181], v99 offset:37888\n\t"
    "ds_read_b64_tr_b16 v[182:183], v99 offset:38144\n\t"
    "v_mul_f32 v7, v7, v37\n\t"
    "v_mfma_f32_16x16x32_bf16 a[88:91], v[184:187], v[172:175], 0\n\t"
    "v_mul_f32 v36, v8, v8\n\t"
    "v_mul_f32_e64 v37, v9, v9\n\t"
    "v_fma_f32 v36, v36, s62, v63\n\t"
    "v_mfma_f32_16x16x32_bf16 a[84:87], v[188:191], v[168:171], 0\n\t"
    "v_exp_f32_e64 v128, v128\n\t"
    "v_fma_f32 v37, v37, s62, v63\n\t"
    "v_mfma_f32_16x16x32_bf16 a[92:95], v[188:191], v[172:175], 0\n\t"
    "v_exp_f32 v129, v129\n\t"
    "v_mul_f32 v36, v36, v8\n\t"
    "v_mfma_f32_16x16x32_bf16 a[96:99], v[192:195], v[168:171], 0\n\t"
    "v_exp_f32 v132, v132\n\t"
    "v_mul_f32 v37, v37, v9\n\t"
    "v_mfma_f32_16x16x32_bf16 a[104:107], v[192:195], v[172:175], 0\n\t"
    "v_exp_f32 v133, v133\n\t"
    "v_mfma_f32_16x16x32_bf16 a[100:103], v[196:199], v[168:171], 0\n\t"
    "v_exp_f32 v130, v130\n\t"
    "v_mfma_f32_16x16x32_bf16 a[108:111], v[196:199], v[172:175], 0\n\t"
    "v_exp_f32 v131, v131\n\t"
    "v_mfma_f32_16x16x32_bf16 a[112:115], v[200:203], v[168:171], 0\n\t"
    "v_exp_f32 v134, v134\n\t"
    "v_mfma_f32_16x16x32_bf16 a[120:123], v[200:203], v[172:175], 0\n\t"
    "v_exp_f32 v135, v135\n\t"
    "v_mfma_f32_16x16x32_bf16 a[116:119], v[204:207], v[168:171], 0\n\t"
    "v_exp_f32 v136, v136\n\t"
    "v_mfma_f32_16x16x32_bf16 a[124:127], v[204:207], v[172:175], 0\n\t"
    "v_exp_f32 v137, v137\n\t"
    "v_mfma_f32_16x16x32_bf16 a[136:139], v[208:211], v[168:171], 0\n\t"
    "v_exp_f32 v140, v140\n\t"
    "v_mfma_f32_16x16x32_bf16 a[140:143], v[208:211], v[172:175], 0\n\t"
    "v_exp_f32_e64 v141, v141\n\t"
    "v_mfma_f32_32x32x16_bf16 v[144:159], a[216:219], a[180:183], v[80:95]\n\t"
    "ds_read_b64_tr_b16 v[184:185], v98 offset:38912\n\t"
    "ds_read_b64_tr_b16 v[186:187], v98 offset:39168\n\t"
    "v_exp_f32 v138, v138\n\t"
    "v_exp_f32 v139, v139\n\t"
    "v_mfma_f32_32x32x16_bf16 v[144:159], a[220:223], a[184:187], v[144:159]\n\t"
    "ds_read_b64_tr_b16 v[188:189], v99 offset:39936\n\t"
    "ds_read_b64_tr_b16 v[190:191], v99 offset:40192\n\t"
    "v_cvt_pk_bf16_f32 v160, v128, v129\n\t"
    "v_cvt_pk_bf16_f32 v164, v132, v133\n\t"
    "v_exp_f32_e64 v142, v142\n\t"
    "v_mfma_f32_32x32x16_bf16 v[144:159], a[224:227], a[188:191], v[144:159]\n\t"
    "ds_read_b64_tr_b16 v[192:193], v98 offset:40960\n\t"
    "ds_read_b64_tr_b16 v[194:195], v98 offset:41216\n\t"
    "v_exp_f32 v143, v143\n\t"
    "v_cvt_pk_bf16_f32 v161, v130, v131\n\t"
    "v_exp_f32 v36, v36\n\t"
    "v_mfma_f32_32x32x16_bf16 v[144:159], a[228:231], a[192:195], v[144:159]\n\t"
    "ds_read_b64_tr_b16 v[196:197], v99 offset:41984\n\t"
    "ds_read_b64_tr_b16 v[198:199], v99 offset:42240\n\t"
    "v_permlane16_swap_b32 v160, v164\n\t"
    "v_cvt_pk_bf16_f32 v165, v134, v135\n\t"
    "v_cvt_pk_bf16_f32 v162, v136, v137\n\t"
    "v_mfma_f32_32x32x16_bf16 v[144:159], a[232:235], a[196:199], v[144:159]\n\t"
    "ds_read_b64_tr_b16 v[200:201], v98 offset:43008\n\t"
    "ds_read_b64_tr_b16 v[202:203], v98 offset:43264\n\t"
    "v_cvt_pk_bf16_f32 v166, v140, v141\n\t"
    "v_permlane16_swap_b32 v161, v165\n\t"
    "v_exp_f32_e64 v37, v37\n\t"
    "v_mfma_f32_32x32x16_bf16 v[144:159], a[236:239], a[200:203], v[144:159]\n\t"
    "ds_read_b64_tr_b16 v[204:205], v99 offset:44032\n\t"
    "ds_read_b64_tr_b16 v[206:207], v99 offset:44288\n\t"
    "v_cvt_pk_bf16_f32 v163, v138, v139\n\t"
    "v_cvt_pk_bf16_f32 v167, v142, v143\n\t"
    "v_permlane16_swap_b32 v162, v166\n\t"
    "v_add_f32 v36, 1.0, v36\n\t"
    "v_mfma_f32_32x32x16_bf16 v[144:159], a[240:243], a[204:207], v[144:159]\n\t"
    "ds_read_b64_tr_b16 v[208:209], v98 offset:45056\n\t"
    "ds_read_b64_tr_b16 v[210:211], v98 offset:45312\n\t"
    "v_permlane16_swap_b32 v163, v167\n\t"
    "v_add_f32 v37, 1.0, v37\n\t"
    "v_rcp_f32 v36, v36\n\t"
    "v_mfma_f32_32x32x16_bf16 v[144:159], a[244:247], a[208:211], v[144:159]\n\t"
    "v_rcp_f32 v37, v37\n\t"
    "v_mul_f32 v8, v8, v36\n\t"
    "v_mul_f32 v9, v9, v37\n\t"
    "v_mul_f32 v36, v10, v10\n\t"
    "v_mul_f32 v37, v11, v11\n\t"
    "v_fma_f32 v36, v36, s62, v63\n\t"
    "v_mfma_f32_32x32x16_bf16 v[144:159], a[248:251], a[212:215], v[144:159]\n\t"
    "s_waitcnt vmcnt(10)\n\t"
    "s_waitcnt lgkmcnt(0)\n\t"
    "v_fma_f32 v37, v37, s62, v63\n\t"
    "v_mul_f32 v36, v36, v10\n\t"
    "v_mul_f32 v37, v37, v11\n\t"
    "v_exp_f32_e64 v36, v36\n\t"
    "v_mfma_f32_16x16x32_bf16 a[0:3], v[176:179], v[160:163], a[0:3]\n\t"
    "ds_read_b128 a[216:219], v246 offset:0\n\t"
    "ds_read_b128 a[220:223], v247 offset:1024\n\t"
    "v_mfma_f32_16x16x32_bf16 a[8:11], v[176:179], v[164:167], a[8:11]\n\t"
    "ds_read_b128 a[224:227], v246 offset:2048\n\t"
    "ds_read_b128 a[228:231], v247 offset:3072\n\t"
    "v_mfma_f32_16x16x32_bf16 a[4:7], v[180:183], v[160:163], a[4:7]\n\t"
    "ds_read_b128 a[232:235], v246 offset:4096\n\t"
    "ds_read_b128 a[236:239], v247 offset:5120\n\t"
    "v_mfma_f32_16x16x32_bf16 a[12:15], v[180:183], v[164:167], a[12:15]\n\t"
    "ds_read_b128 a[240:243], v246 offset:6144\n\t"
    "ds_read_b128 a[244:247], v247 offset:7168\n\t"
    "v_mfma_f32_16x16x32_bf16 a[16:19], v[184:187], v[160:163], a[16:19]\n\t"
    "ds_read_b128 a[248:251], v246 offset:8192\n\t"
    "v_exp_f32 v37, v37\n\t"
    "v_mfma_f32_16x16x32_bf16 a[24:27], v[184:187], v[164:167], a[24:27]\n\t"
    "v_add_f32 v36, 1.0, v36\n\t"
    "v_add_f32 v37, 1.0, v37\n\t"
    "v_mfma_f32_16x16x32_bf16 a[20:23], v[188:191], v[160:163], a[20:23]\n\t"
    "v_exp_f32 v144, v144\n\t"
    "v_mfma_f32_16x16x32_bf16 a[28:31], v[188:191], v[164:167], a[28:31]\n\t"
    "v_exp_f32 v145, v145\n\t"
    "v_mfma_f32_16x16x32_bf16 a[32:35], v[192:195], v[160:163], a[32:35]\n\t"
    "v_exp_f32 v148, v148\n\t"
    "v_mfma_f32_16x16x32_bf16 a[40:43], v[192:195], v[164:167], a[40:43]\n\t"
    "v_exp_f32 v149, v149\n\t"
    "v_mfma_f32_16x16x32_bf16 a[36:39], v[196:199], v[160:163], a[36:39]\n\t"
    "v_exp_f32 v146, v146\n\t"
    "v_mfma_f32_16x16x32_bf16 a[44:47], v[196:199], v[164:167], a[44:47]\n\t"
    "v_exp_f32 v147, v147\n\t"
    "v_mfma_f32_16x16x32_bf16 a[48:51], v[200:203], v[160:163], a[48:51]\n\t"
    "v_exp_f32 v150, v150\n\t"
    "v_mfma_f32_16x16x32_bf16 a[56:59], v[200:203], v[164:167], a[56:59]\n\t"
    "v_exp_f32 v151, v151\n\t"
    "v_mfma_f32_16x16x32_bf16 a[52:55], v[204:207], v[160:163], a[52:55]\n\t"
    "v_exp_f32 v152, v152\n\t"
    "v_mfma_f32_16x16x32_bf16 a[60:63], v[204:207], v[164:167], a[60:63]\n\t"
    "s_mov_b32 s72, 36864\n\t"
    "v_exp_f32_e64 v153, v153\n\t"
    "v_mfma_f32_16x16x32_bf16 a[128:131], v[208:211], v[160:163], a[128:131]\n\t"
    "s_add_u32 m0, s61, 36864\n\t"
    "v_exp_f32 v156, v156\n\t"
    "v_mfma_f32_16x16x32_bf16 a[132:135], v[208:211], v[164:167], a[132:135]\n\t"
    "s_waitcnt lgkmcnt(0)\n\t"
    "v_exp_f32 v157, v157\n\t"
    "v_mfma_f32_32x32x16_bf16 v[128:143], a[216:219], a[144:147], v[64:79]\n\t"
    "buffer_load_dwordx4 v101, s[64:67], s72 offen offset:0 sc1 lds\n\t"
    "v_exp_f32 v154, v154\n\t"
    "v_exp_f32_e64 v155, v155\n\t"
    "v_mfma_f32_32x32x16_bf16 v[128:143], a[220:223], a[148:151], v[128:143]\n\t"
    "buffer_load_dwordx4 v101, s[64:67], s72 offen offset:1024 sc1 lds\n\t"
    "v_cvt_pk_bf16_f32 v168, v144, v145\n\t"
    "v_cvt_pk_bf16_f32 v172, v148, v149\n\t"
    "v_exp_f32 v158, v158\n\t"
    "v_mfma_f32_32x32x16_bf16 v[128:143], a[224:227], a[152:155], v[128:143]\n\t"
    "buffer_load_dwordx4 v102, s[64:67], s72 offen offset:2048 sc1 lds\n\t"
    "v_exp_f32 v159, v159\n\t"
    "v_cvt_pk_bf16_f32 v169, v146, v147\n\t"
    "v_rcp_f32_e64 v36, v36\n\t"
    "v_mfma_f32_32x32x16_bf16 v[128:143], a[228:231], a[156:159], v[128:143]\n\t"
    "buffer_load_dwordx4 v102, s[64:67], s72 offen offset:3072 sc1 lds\n\t"
    "s_add_u32 m0, s61, 40960\n\t"
    "v_permlane16_swap_b32 v168, v172\n\t"
    "v_cvt_pk_bf16_f32 v173, v150, v151\n\t"
    "v_cvt_pk_bf16_f32 v170, v152, v153\n\t"
    "v_mfma_f32_32x32x16_bf16 v[128:143], a[232:235], a[160:163], v[128:143]\n\t"
    "buffer_load_dwordx4 v103, s[64:67], s72 offen offset:0 sc1 lds\n\t"
    "v_cvt_pk_bf16_f32 v174, v156, v157\n\t"
    "v_permlane16_swap_b32 v169, v173\n\t"
    "v_rcp_f32_e64 v37, v37\n\t"
    "v_mfma_f32_32x32x16_bf16 v[128:143], a[236:239], a[164:167], v[128:143]\n\t"
    "buffer_load_dwordx4 v103, s[64:67], s72 offen offset:1024 sc1 lds\n\t"
    "v_cvt_pk_bf16_f32 v171, v154, v155\n\t"
    "v_cvt_pk_bf16_f32 v175, v158, v159\n\t"
    "v_permlane16_swap_b32 v170, v174\n\t"
    "v_mul_f32 v10, v10, v36\n\t"
    "v_mfma_f32_32x32x16_bf16 v[128:143], a[240:243], a[168:171], v[128:143]\n\t"
    "buffer_load_dwordx4 v104, s[64:67], s72 offen offset:2048 sc1 lds\n\t"
    "v_permlane16_swap_b32 v171, v175\n\t"
    "v_mul_f32 v11, v11, v37\n\t"
    "v_mul_f32 v36, v12, v12\n\t"
    "v_mul_f32 v37, v13, v13\n\t"
    "v_mfma_f32_32x32x16_bf16 v[128:143], a[244:247], a[172:175], v[128:143]\n\t"
    "buffer_load_dwordx4 v104, s[64:67], s72 offen offset:3072 sc1 lds\n\t"
    "s_add_u32 m0, s61, 45056\n\t"
    "v_fma_f32 v36, v36, s62, v63\n\t"
    "v_fma_f32 v37, v37, s62, v63\n\t"
    "v_mul_f32 v36, v36, v12\n\t"
    "v_mul_f32 v37, v37, v13\n\t"
    "v_mfma_f32_32x32x16_bf16 v[128:143], a[248:251], a[176:179], v[128:143]\n\t"
    "buffer_load_dwordx4 v105, s[64:67], s72 offen offset:0 sc1 lds\n\t"
    "v_exp_f32 v36, v36\n\t"
    "v_exp_f32 v37, v37\n\t"
    "v_add_f32 v36, 1.0, v36\n\t"
    "v_mfma_f32_16x16x32_bf16 a[64:67], v[176:179], v[168:171], a[64:67]\n\t"
    "v_add_f32 v37, 1.0, v37\n\t"
    "v_rcp_f32_e64 v36, v36\n\t"
    "v_mfma_f32_16x16x32_bf16 a[72:75], v[176:179], v[172:175], a[72:75]\n\t"
    "buffer_store_dwordx4 v[228:231], v113, s[80:83], s51 offen offset:1024 sc1\n\t"
    "v_mfma_f32_16x16x32_bf16 a[68:71], v[180:183], v[168:171], a[68:71]\n\t"
    "ds_read_b64_tr_b16 v[176:177], v248 offset:0\n\t"
    "ds_read_b64_tr_b16 v[178:179], v248 offset:256\n\t"
    "v_mfma_f32_16x16x32_bf16 a[76:79], v[180:183], v[172:175], a[76:79]\n\t"
    "v_rcp_f32 v37, v37\n\t"
    "v_mul_f32 v12, v12, v36\n\t"
    "v_mfma_f32_16x16x32_bf16 a[80:83], v[184:187], v[168:171], a[80:83]\n\t"
    "ds_read_b64_tr_b16 v[180:181], v249 offset:1024\n\t"
    "ds_read_b64_tr_b16 v[182:183], v249 offset:1280\n\t"
    "v_mul_f32 v13, v13, v37\n\t"
    "v_mfma_f32_16x16x32_bf16 a[88:91], v[184:187], v[172:175], a[88:91]\n\t"
    "v_mul_f32 v36, v14, v14\n\t"
    "v_mul_f32_e64 v37, v15, v15\n\t"
    "v_fma_f32 v36, v36, s62, v63\n\t"
    "v_mfma_f32_16x16x32_bf16 a[84:87], v[188:191], v[168:171], a[84:87]\n\t"
    "v_exp_f32_e64 v128, v128\n\t"
    "v_fma_f32 v37, v37, s62, v63\n\t"
    "v_mfma_f32_16x16x32_bf16 a[92:95], v[188:191], v[172:175], a[92:95]\n\t"
    "v_exp_f32 v129, v129\n\t"
    "v_mul_f32 v36, v36, v14\n\t"
    "v_mfma_f32_16x16x32_bf16 a[96:99], v[192:195], v[168:171], a[96:99]\n\t"
    "v_exp_f32 v132, v132\n\t"
    "v_mul_f32 v37, v37, v15\n\t"
    "v_mfma_f32_16x16x32_bf16 a[104:107], v[192:195], v[172:175], a[104:107]\n\t"
    "v_exp_f32 v133, v133\n\t"
    "v_mfma_f32_16x16x32_bf16 a[100:103], v[196:199], v[168:171], a[100:103]\n\t"
    "v_exp_f32 v130, v130\n\t"
    "v_mfma_f32_16x16x32_bf16 a[108:111], v[196:199], v[172:175], a[108:111]\n\t"
    "v_exp_f32 v131, v131\n\t"
    "v_mfma_f32_16x16x32_bf16 a[112:115], v[200:203], v[168:171], a[112:115]\n\t"
    "v_exp_f32 v134, v134\n\t"
    "v_mfma_f32_16x16x32_bf16 a[120:123], v[200:203], v[172:175], a[120:123]\n\t"
    "v_exp_f32 v135, v135\n\t"
    "v_mfma_f32_16x16x32_bf16 a[116:119], v[204:207], v[168:171], a[116:119]\n\t"
    "v_exp_f32 v136, v136\n\t"
    "v_mfma_f32_16x16x32_bf16 a[124:127], v[204:207], v[172:175], a[124:127]\n\t"
    "v_exp_f32 v137, v137\n\t"
    "v_mfma_f32_16x16x32_bf16 a[136:139], v[208:211], v[168:171], a[136:139]\n\t"
    "v_exp_f32 v140, v140\n\t"
    "v_mfma_f32_16x16x32_bf16 a[140:143], v[208:211], v[172:175], a[140:143]\n\t"
    "v_exp_f32_e64 v141, v141\n\t"
    "v_mfma_f32_32x32x16_bf16 v[144:159], a[216:219], a[180:183], v[80:95]\n\t"
    "ds_read_b64_tr_b16 v[184:185], v248 offset:2048\n\t"
    "ds_read_b64_tr_b16 v[186:187], v248 offset:2304\n\t"
    "v_exp_f32 v138, v138\n\t"
    "v_exp_f32 v139, v139\n\t"
    "v_mfma_f32_32x32x16_bf16 v[144:159], a[220:223], a[184:187], v[144:159]\n\t"
    "ds_read_b64_tr_b16 v[188:189], v249 offset:3072\n\t"
    "ds_read_b64_tr_b16 v[190:191], v249 offset:3328\n\t"
    "v_cvt_pk_bf16_f32 v160, v128, v129\n\t"
    "v_cvt_pk_bf16_f32 v164, v132, v133\n\t"
    "v_exp_f32_e64 v142, v142\n\t"
    "v_mfma_f32_32x32x16_bf16 v[144:159], a[224:227], a[188:191], v[144:159]\n\t"
    "ds_read_b64_tr_b16 v[192:193], v248 offset:4096\n\t"
    "ds_read_b64_tr_b16 v[194:195], v248 offset:4352\n\t"
    "v_exp_f32 v143, v143\n\t"
    "v_cvt_pk_bf16_f32 v161, v130, v131\n\t"
    "v_exp_f32 v36, v36\n\t"
    "v_mfma_f32_32x32x16_bf16 v[144:159], a[228:231], a[192:195], v[144:159]\n\t"
    "ds_read_b64_tr_b16 v[196:197], v249 offset:5120\n\t"
    "ds_read_b64_tr_b16 v[198:199], v249 offset:5376\n\t"
    "v_permlane16_swap_b32 v160, v164\n\t"
    "v_cvt_pk_bf16_f32 v165, v134, v135\n\t"
    "v_cvt_pk_bf16_f32 v162, v136, v137\n\t"
    "v_mfma_f32_32x32x16_bf16 v[144:159], a[232:235], a[196:199], v[144:159]\n\t"
    "ds_read_b64_tr_b16 v[200:201], v248 offset:6144\n\t"
    "ds_read_b64_tr_b16 v[202:203], v248 offset:6400\n\t"
    "v_cvt_pk_bf16_f32 v166, v140, v141\n\t"
    "v_permlane16_swap_b32 v161, v165\n\t"
    "v_exp_f32_e64 v37, v37\n\t"
    "v_mfma_f32_32x32x16_bf16 v[144:159], a[236:239], a[200:203], v[144:159]\n\t"
    "ds_read_b64_tr_b16 v[204:205], v249 offset:7168\n\t"
    "ds_read_b64_tr_b16 v[206:207], v249 offset:7424\n\t"
    "v_cvt_pk_bf16_f32 v163, v138, v139\n\t"
    "v_cvt_pk_bf16_f32 v167, v142, v143\n\t"
    "v_permlane16_swap_b32 v162, v166\n\t"
    "v_add_f32 v36, 1.0, v36\n\t"
    "v_mfma_f32_32x32x16_bf16 v[144:159], a[240:243], a[204:207], v[144:159]\n\t"
    "ds_read_b64_tr_b16 v[208:209], v248 offset:8192\n\t"
    "ds_read_b64_tr_b16 v[210:211], v248 offset:8448\n\t"
    "v_permlane16_swap_b32 v163, v167\n\t"
    "v_add_f32 v37, 1.0, v37\n\t"
    "v_rcp_f32 v36, v36\n\t"
    "v_mfma_f32_32x32x16_bf16 v[144:159], a[244:247], a[208:211], v[144:159]\n\t"
    "v_rcp_f32 v37, v37\n\t"
    "v_mul_f32 v14, v14, v36\n\t"
    "v_mul_f32 v15, v15, v37\n\t"
    "v_mul_f32 v36, v32, v32\n\t"
    "v_mul_f32 v37, v33, v33\n\t"
    "v_fma_f32 v36, v36, s62, v63\n\t"
    "v_mfma_f32_32x32x16_bf16 v[144:159], a[248:251], a[212:215], v[144:159]\n\t"
    "s_waitcnt vmcnt(11)\n\t"
    "s_waitcnt lgkmcnt(0)\n\t"
    "v_fma_f32 v37, v37, s62, v63\n\t"
    "v_mul_f32 v36, v36, v32\n\t"
    "v_mul_f32 v37, v37, v33\n\t"
    "v_exp_f32_e64 v36, v36\n\t"
    "v_mfma_f32_16x16x32_bf16 a[0:3], v[176:179], v[160:163], a[0:3]\n\t"
    "ds_read_b128 a[216:219], v96 offset:0\n\t"
    "ds_read_b128 a[220:223], v97 offset:1024\n\t"
    "v_mfma_f32_16x16x32_bf16 a[8:11], v[176:179], v[164:167], a[8:11]\n\t"
    "ds_read_b128 a[224:227], v96 offset:2048\n\t"
    "ds_read_b128 a[228:231], v97 offset:3072\n\t"
    "v_mfma_f32_16x16x32_bf16 a[4:7], v[180:183], v[160:163], a[4:7]\n\t"
    "ds_read_b128 a[232:235], v96 offset:4096\n\t"
    "ds_read_b128 a[236:239], v97 offset:5120\n\t"
    "v_mfma_f32_16x16x32_bf16 a[12:15], v[180:183], v[164:167], a[12:15]\n\t"
    "ds_read_b128 a[240:243], v96 offset:6144\n\t"
    "ds_read_b128 a[244:247], v97 offset:7168\n\t"
    "v_mfma_f32_16x16x32_bf16 a[16:19], v[184:187], v[160:163], a[16:19]\n\t"
    "ds_read_b128 a[248:251], v96 offset:8192\n\t"
    "v_exp_f32 v37, v37\n\t"
    "v_mfma_f32_16x16x32_bf16 a[24:27], v[184:187], v[164:167], a[24:27]\n\t"
    "v_add_f32 v36, 1.0, v36\n\t"
    "v_add_f32 v37, 1.0, v37\n\t"
    "v_mfma_f32_16x16x32_bf16 a[20:23], v[188:191], v[160:163], a[20:23]\n\t"
    "v_exp_f32 v144, v144\n\t"
    "v_mfma_f32_16x16x32_bf16 a[28:31], v[188:191], v[164:167], a[28:31]\n\t"
    "v_exp_f32 v145, v145\n\t"
    "v_mfma_f32_16x16x32_bf16 a[32:35], v[192:195], v[160:163], a[32:35]\n\t"
    "v_exp_f32 v148, v148\n\t"
    "v_mfma_f32_16x16x32_bf16 a[40:43], v[192:195], v[164:167], a[40:43]\n\t"
    "v_exp_f32 v149, v149\n\t"
    "v_mfma_f32_16x16x32_bf16 a[36:39], v[196:199], v[160:163], a[36:39]\n\t"
    "v_exp_f32 v146, v146\n\t"
    "v_mfma_f32_16x16x32_bf16 a[44:47], v[196:199], v[164:167], a[44:47]\n\t"
    "v_exp_f32 v147, v147\n\t"
    "v_mfma_f32_16x16x32_bf16 a[48:51], v[200:203], v[160:163], a[48:51]\n\t"
    "v_exp_f32 v150, v150\n\t"
    "v_mfma_f32_16x16x32_bf16 a[56:59], v[200:203], v[164:167], a[56:59]\n\t"
    "v_exp_f32 v151, v151\n\t"
    "v_mfma_f32_16x16x32_bf16 a[52:55], v[204:207], v[160:163], a[52:55]\n\t"
    "v_exp_f32 v152, v152\n\t"
    "v_mfma_f32_16x16x32_bf16 a[60:63], v[204:207], v[164:167], a[60:63]\n\t"
    "s_mov_b32 s72, 46080\n\t"
    "v_exp_f32_e64 v153, v153\n\t"
    "v_mfma_f32_16x16x32_bf16 a[128:131], v[208:211], v[160:163], a[128:131]\n\t"
    "s_add_u32 m0, s61, 73728\n\t"
    "v_exp_f32 v156, v156\n\t"
    "v_mfma_f32_16x16x32_bf16 a[132:135], v[208:211], v[164:167], a[132:135]\n\t"
    "s_waitcnt lgkmcnt(0)\n\t"
    "v_exp_f32 v157, v157\n\t"
    "v_mfma_f32_32x32x16_bf16 v[128:143], a[216:219], a[144:147], v[64:79]\n\t"
    "buffer_load_dwordx4 v101, s[64:67], s72 offen offset:0 sc1 lds\n\t"
    "v_exp_f32 v154, v154\n\t"
    "v_exp_f32_e64 v155, v155\n\t"
    "v_mfma_f32_32x32x16_bf16 v[128:143], a[220:223], a[148:151], v[128:143]\n\t"
    "buffer_load_dwordx4 v101, s[64:67], s72 offen offset:1024 sc1 lds\n\t"
    "v_cvt_pk_bf16_f32 v168, v144, v145\n\t"
    "v_cvt_pk_bf16_f32 v172, v148, v149\n\t"
    "v_exp_f32 v158, v158\n\t"
    "v_mfma_f32_32x32x16_bf16 v[128:143], a[224:227], a[152:155], v[128:143]\n\t"
    "buffer_load_dwordx4 v102, s[64:67], s72 offen offset:2048 sc1 lds\n\t"
    "v_exp_f32 v159, v159\n\t"
    "v_cvt_pk_bf16_f32 v169, v146, v147\n\t"
    "v_rcp_f32_e64 v36, v36\n\t"
    "v_mfma_f32_32x32x16_bf16 v[128:143], a[228:231], a[156:159], v[128:143]\n\t"
    "buffer_load_dwordx4 v102, s[64:67], s72 offen offset:3072 sc1 lds\n\t"
    "s_add_u32 m0, s61, 77824\n\t"
    "v_permlane16_swap_b32 v168, v172\n\t"
    "v_cvt_pk_bf16_f32 v173, v150, v151\n\t"
    "v_cvt_pk_bf16_f32 v170, v152, v153\n\t"
    "v_mfma_f32_32x32x16_bf16 v[128:143], a[232:235], a[160:163], v[128:143]\n\t"
    "buffer_load_dwordx4 v103, s[64:67], s72 offen offset:0 sc1 lds\n\t"
    "v_cvt_pk_bf16_f32 v174, v156, v157\n\t"
    "v_permlane16_swap_b32 v169, v173\n\t"
    "v_rcp_f32_e64 v37, v37\n\t"
    "v_mfma_f32_32x32x16_bf16 v[128:143], a[236:239], a[164:167], v[128:143]\n\t"
    "buffer_load_dwordx4 v103, s[64:67], s72 offen offset:1024 sc1 lds\n\t"
    "v_cvt_pk_bf16_f32 v171, v154, v155\n\t"
    "v_cvt_pk_bf16_f32 v175, v158, v159\n\t"
    "v_permlane16_swap_b32 v170, v174\n\t"
    "v_mul_f32 v32, v32, v36\n\t"
    "v_mfma_f32_32x32x16_bf16 v[128:143], a[240:243], a[168:171], v[128:143]\n\t"
    "buffer_load_dwordx4 v104, s[64:67], s72 offen offset:2048 sc1 lds\n\t"
    "v_permlane16_swap_b32 v171, v175\n\t"
    "v_mul_f32 v33, v33, v37\n\t"
    "v_mul_f32 v36, v34, v34\n\t"
    "v_mul_f32 v37, v35, v35\n\t"
    "v_mfma_f32_32x32x16_bf16 v[128:143], a[244:247], a[172:175], v[128:143]\n\t"
    "buffer_load_dwordx4 v104, s[64:67], s72 offen offset:3072 sc1 lds\n\t"
    "s_add_u32 m0, s61, 81920\n\t"
    "v_fma_f32 v36, v36, s62, v63\n\t"
    "v_fma_f32 v37, v37, s62, v63\n\t"
    "v_mul_f32 v36, v36, v34\n\t"
    "v_mul_f32 v37, v37, v35\n\t"
    "v_mfma_f32_32x32x16_bf16 v[128:143], a[248:251], a[176:179], v[128:143]\n\t"
    "buffer_load_dwordx4 v105, s[64:67], s72 offen offset:0 sc1 lds\n\t"
    "v_exp_f32 v36, v36\n\t"
    "v_exp_f32 v37, v37\n\t"
    "v_add_f32 v36, 1.0, v36\n\t"
    "v_mfma_f32_16x16x32_bf16 a[64:67], v[176:179], v[168:171], a[64:67]\n\t"
    "v_add_f32 v37, 1.0, v37\n\t"
    "v_rcp_f32_e64 v36, v36\n\t"
    "v_mfma_f32_16x16x32_bf16 a[72:75], v[176:179], v[172:175], a[72:75]\n\t"
    "buffer_store_dwordx2 v[228:229], v114, s[80:83], s51 offen sc1\n\t"
    "v_mfma_f32_16x16x32_bf16 a[68:71], v[180:183], v[168:171], a[68:71]\n\t"
    "ds_read_b64_tr_b16 v[176:177], v98 offset:0\n\t"
    "ds_read_b64_tr_b16 v[178:179], v98 offset:256\n\t"
    "v_mfma_f32_16x16x32_bf16 a[76:79], v[180:183], v[172:175], a[76:79]\n\t"
    "v_rcp_f32 v37, v37\n\t"
    "v_mul_f32 v34, v34, v36\n\t"
    "v_mfma_f32_16x16x32_bf16 a[80:83], v[184:187], v[168:171], a[80:83]\n\t"
    "ds_read_b64_tr_b16 v[180:181], v99 offset:1024\n\t"
    "ds_read_b64_tr_b16 v[182:183], v99 offset:1280\n\t"
    "v_mul_f32 v35, v35, v37\n\t"
    "v_mfma_f32_16x16x32_bf16 a[88:91], v[184:187], v[172:175], a[88:91]\n\t"
    "v_add_f32 v36, v0, v1\n\t"
    "v_mul_f32 v37, v0, v0\n\t"
    "v_add_f32 v38, v32, v33\n\t"
    "v_mfma_f32_16x16x32_bf16 a[84:87], v[188:191], v[168:171], a[84:87]\n\t"
    "v_exp_f32 v128, v128\n\t"
    "v_mul_f32 v39, v32, v32\n\t"
    "v_mfma_f32_16x16x32_bf16 a[92:95], v[188:191], v[172:175], a[92:95]\n\t"
    "v_exp_f32 v129, v129\n\t"
    "v_fmac_f32 v37, v1, v1\n\t"
    "v_mfma_f32_16x16x32_bf16 a[96:99], v[192:195], v[168:171], a[96:99]\n\t"
    "v_exp_f32 v132, v132\n\t"
    "v_fmac_f32 v39, v33, v33\n\t"
    "v_mfma_f32_16x16x32_bf16 a[104:107], v[192:195], v[172:175], a[104:107]\n\t"
    "v_exp_f32 v133, v133\n\t"
    "v_add_f32 v38, v38, v34\n\t"
    "v_mfma_f32_16x16x32_bf16 a[100:103], v[196:199], v[168:171], a[100:103]\n\t"
    "v_exp_f32 v130, v130\n\t"
    "v_fmac_f32 v39, v34, v34\n\t"
    "v_mfma_f32_16x16x32_bf16 a[108:111], v[196:199], v[172:175], a[108:111]\n\t"
    "v_exp_f32 v131, v131\n\t"
    "v_add_f32 v38, v38, v35\n\t"
    "v_mfma_f32_16x16x32_bf16 a[112:115], v[200:203], v[168:171], a[112:115]\n\t"
    "v_exp_f32 v134, v134\n\t"
    "v_fmac_f32 v39, v35, v35\n\t"
    "v_mfma_f32_16x16x32_bf16 a[120:123], v[200:203], v[172:175], a[120:123]\n\t"
    "v_exp_f32 v135, v135\n\t"
    "v_add_f32 v36, v36, v2\n\t"
    "v_mfma_f32_16x16x32_bf16 a[116:119], v[204:207], v[168:171], a[116:119]\n\t"
    "v_exp_f32 v136, v136\n\t"
    "v_fmac_f32 v37, v2, v2\n\t"
    "v_mfma_f32_16x16x32_bf16 a[124:127], v[204:207], v[172:175], a[124:127]\n\t"
    "v_exp_f32 v137, v137\n\t"
    "v_add_f32 v36, v36, v3\n\t"
    "v_mfma_f32_16x16x32_bf16 a[136:139], v[208:211], v[168:171], a[136:139]\n\t"
    "v_exp_f32 v140, v140\n\t"
    "v_fmac_f32 v37, v3, v3\n\t"
    "v_mfma_f32_16x16x32_bf16 a[140:143], v[208:211], v[172:175], a[140:143]\n\t"
    "v_exp_f32 v141, v141\n\t"
    "v_add_f32 v36, v36, v4\n\t"
    "v_mfma_f32_32x32x16_bf16 v[144:159], a[216:219], a[180:183], v[80:95]\n\t"
    "ds_read_b64_tr_b16 v[184:185], v98 offset:2048\n\t"
    "ds_read_b64_tr_b16 v[186:187], v98 offset:2304\n\t"
    "v_exp_f32 v138, v138\n\t"
    "v_exp_f32 v139, v139\n\t"
    "v_fmac_f32_e64 v37, v4, v4\n\t"
    "v_mfma_f32_32x32x16_bf16 v[144:159], a[220:223], a[184:187], v[144:159]\n\t"
    "ds_read_b64_tr_b16 v[188:189], v99 offset:3072\n\t"
    "ds_read_b64_tr_b16 v[190:191], v99 offset:3328\n\t"
    "v_cvt_pk_bf16_f32 v160, v128, v129\n\t"
    "v_cvt_pk_bf16_f32 v164, v132, v133\n\t"
    "v_exp_f32 v142, v142\n\t"
    "v_add_f32 v36, v36, v5\n\t"
    "v_mfma_f32_32x32x16_bf16 v[144:159], a[224:227], a[188:191], v[144:159]\n\t"
    "ds_read_b64_tr_b16 v[192:193], v98 offset:4096\n\t"
    "ds_read_b64_tr_b16 v[194:195], v98 offset:4352\n\t"
    "v_exp_f32 v143, v143\n\t"
    "v_cvt_pk_bf16_f32 v161, v130, v131\n\t"
    "v_fmac_f32 v37, v5, v5\n\t"
    "v_add_f32_e64 v36, v36, v6\n\t"
    "v_mfma_f32_32x32x16_bf16 v[144:159], a[228:231], a[192:195], v[144:159]\n\t"
    "ds_read_b64_tr_b16 v[196:197], v99 offset:5120\n\t"
    "ds_read_b64_tr_b16 v[198:199], v99 offset:5376\n\t"
    "v_permlane16_swap_b32 v160, v164\n\t"
    "v_cvt_pk_bf16_f32 v165, v134, v135\n\t"
    "v_cvt_pk_bf16_f32 v162, v136, v137\n\t"
    "v_fmac_f32 v37, v6, v6\n\t"
    "v_mfma_f32_32x32x16_bf16 v[144:159], a[232:235], a[196:199], v[144:159]\n\t"
    "ds_read_b64_tr_b16 v[200:201], v98 offset:6144\n\t"
    "ds_read_b64_tr_b16 v[202:203], v98 offset:6400\n\t"
    "v_cvt_pk_bf16_f32 v166, v140, v141\n\t"
    "v_permlane16_swap_b32 v161, v165\n\t"
    "v_add_f32 v36, v36, v7\n\t"
    "v_fmac_f32_e64 v37, v7, v7\n\t"
    "v_mfma_f32_32x32x16_bf16 v[144:159], a[236:239], a[200:203], v[144:159]\n\t"
    "ds_read_b64_tr_b16 v[204:205], v99 offset:7168\n\t"
    "ds_read_b64_tr_b16 v[206:207], v99 offset:7424\n\t"
    "v_cvt_pk_bf16_f32 v163, v138, v139\n\t"
    "v_cvt_pk_bf16_f32 v167, v142, v143\n\t"
    "v_permlane16_swap_b32 v162, v166\n\t"
    "v_add_f32 v36, v36, v8\n\t"
    "v_mfma_f32_32x32x16_bf16 v[144:159], a[240:243], a[204:207], v[144:159]\n\t"
    "ds_read_b64_tr_b16 v[208:209], v98 offset:8192\n\t"
    "ds_read_b64_tr_b16 v[210:211], v98 offset:8448\n\t"
    "v_permlane16_swap_b32 v163, v167\n\t"
    "v_fmac_f32 v37, v8, v8\n\t"
    "v_add_f32 v36, v36, v9\n\t"
    "v_fmac_f32 v37, v9, v9\n\t"
    "v_mfma_f32_32x32x16_bf16 v[144:159], a[244:247], a[208:211], v[144:159]\n\t"
    "v_add_f32 v36, v36, v10\n\t"
    "v_fmac_f32 v37, v10, v10\n\t"
    "v_add_f32 v36, v36, v11\n\t"
    "v_fmac_f32 v37, v11, v11\n\t"
    "v_add_f32 v36, v36, v12\n\t"
    "v_fmac_f32 v37, v12, v12\n\t"
    "v_add_f32 v36, v36, v13\n\t"
    "v_mfma_f32_32x32x16_bf16 v[144:159], a[248:251], a[212:215], v[144:159]\n\t"
    "s_waitcnt vmcnt(11)\n\t"
    "s_waitcnt lgkmcnt(0)\n\t"
    "v_fmac_f32 v37, v13, v13\n\t"
    "v_add_f32 v36, v36, v14\n\t"
    "v_fmac_f32 v37, v14, v14\n\t"
    "v_add_f32 v36, v36, v15\n\t"
    "v_fmac_f32 v37, v15, v15\n\t"
    "v_mov_b32_e64 v40, v38\n\t"
    "v_mfma_f32_16x16x32_bf16 a[0:3], v[176:179], v[160:163], a[0:3]\n\t"
    "ds_read_b128 a[216:219], v96 offset:36864\n\t"
    "ds_read_b128 a[220:223], v97 offset:37888\n\t"
    "v_mov_b32_e64 v41, v39\n\t"
    "v_mfma_f32_16x16x32_bf16 a[8:11], v[176:179], v[164:167], a[8:11]\n\t"
    "ds_read_b128 a[224:227], v96 offset:38912\n\t"
    "ds_read_b128 a[228:231], v97 offset:39936\n\t"
    "v_mov_b32_e64 v42, v36\n\t"
    "v_mfma_f32_16x16x32_bf16 a[4:7], v[180:183], v[160:163], a[4:7]\n\t"
    "ds_read_b128 a[232:235], v96 offset:40960\n\t"
    "ds_read_b128 a[236:239], v97 offset:41984\n\t"
    "v_mov_b32_e64 v43, v37\n\t"
    "v_mfma_f32_16x16x32_bf16 a[12:15], v[180:183], v[164:167], a[12:15]\n\t"
    "ds_read_b128 a[240:243], v96 offset:43008\n\t"
    "ds_read_b128 a[244:247], v97 offset:44032\n\t"
    "v_mfma_f32_16x16x32_bf16 a[16:19], v[184:187], v[160:163], a[16:19]\n\t"
    "ds_read_b128 a[248:251], v96 offset:45056\n\t"
    "v_permlane16_swap_b32 v40, v38\n\t"
    "v_mfma_f32_16x16x32_bf16 a[24:27], v[184:187], v[164:167], a[24:27]\n\t"
    "v_permlane16_swap_b32 v41, v39\n\t"
    "v_mfma_f32_16x16x32_bf16 a[20:23], v[188:191], v[160:163], a[20:23]\n\t"
    "v_exp_f32 v144, v144\n\t"
    "v_mfma_f32_16x16x32_bf16 a[28:31], v[188:191], v[164:167], a[28:31]\n\t"
    "v_exp_f32 v145, v145\n\t"
    "v_mfma_f32_16x16x32_bf16 a[32:35], v[192:195], v[160:163], a[32:35]\n\t"
    "v_exp_f32 v148, v148\n\t"
    "v_mfma_f32_16x16x32_bf16 a[40:43], v[192:195], v[164:167], a[40:43]\n\t"
    "v_exp_f32 v149, v149\n\t"
    "v_mfma_f32_16x16x32_bf16 a[36:39], v[196:199], v[160:163], a[36:39]\n\t"
    "v_exp_f32 v146, v146\n\t"
    "v_mfma_f32_16x16x32_bf16 a[44:47], v[196:199], v[164:167], a[44:47]\n\t"
    "v_exp_f32 v147, v147\n\t"
    "v_mfma_f32_16x16x32_bf16 a[48:51], v[200:203], v[160:163], a[48:51]\n\t"
    "v_exp_f32 v150, v150\n\t"
    "v_mfma_f32_16x16x32_bf16 a[56:59], v[200:203], v[164:167], a[56:59]\n\t"
    "v_exp_f32 v151, v151\n\t"
    "v_mfma_f32_16x16x32_bf16 a[52:55], v[204:207], v[160:163], a[52:55]\n\t"
    "v_exp_f32_e64 v152, v152\n\t"
    "v_mfma_f32_16x16x32_bf16 a[60:63], v[204:207], v[164:167], a[60:63]\n\t"
    "s_mov_b32 s72, 55296\n\t"
    "v_exp_f32 v153, v153\n\t"
    "v_mfma_f32_16x16x32_bf16 a[128:131], v[208:211], v[160:163], a[128:131]\n\t"
    "s_add_u32 m0, s61, 0\n\t"
    "v_exp_f32 v156, v156\n\t"
    "v_mfma_f32_16x16x32_bf16 a[132:135], v[208:211], v[164:167], a[132:135]\n\t"
    "s_waitcnt lgkmcnt(0)\n\t"
    "v_exp_f32 v157, v157\n\t"
    "v_mfma_f32_32x32x16_bf16 v[128:143], a[216:219], a[144:147], v[64:79]\n\t"
    "buffer_load_dwordx4 v101, s[64:67], s72 offen offset:0 sc1 lds\n\t"
    "v_exp_f32 v154, v154\n\t"
    "v_exp_f32_e64 v155, v155\n\t"
    "v_mfma_f32_32x32x16_bf16 v[128:143], a[220:223], a[148:151], v[128:143]\n\t"
    "buffer_load_dwordx4 v101, s[64:67], s72 offen offset:1024 sc1 lds\n\t"
    "v_cvt_pk_bf16_f32 v168, v144, v145\n\t"
    "v_cvt_pk_bf16_f32 v172, v148, v149\n\t"
    "v_exp_f32 v158, v158\n\t"
    "v_mfma_f32_32x32x16_bf16 v[128:143], a[224:227], a[152:155], v[128:143]\n\t"
    "buffer_load_dwordx4 v102, s[64:67], s72 offen offset:2048 sc1 lds\n\t"
    "v_exp_f32_e64 v159, v159\n\t"
    "v_cvt_pk_bf16_f32 v169, v146, v147\n\t"
    "v_permlane32_swap_b32 v42, v36\n\t"
    "v_mfma_f32_32x32x16_bf16 v[128:143], a[228:231], a[156:159], v[128:143]\n\t"
    "buffer_load_dwordx4 v102, s[64:67], s72 offen offset:3072 sc1 lds\n\t"
    "s_add_u32 m0, s61, 4096\n\t"
    "v_permlane16_swap_b32 v168, v172\n\t"
    "v_cvt_pk_bf16_f32 v173, v150, v151\n\t"
    "v_cvt_pk_bf16_f32 v170, v152, v153\n\t"
    "v_mfma_f32_32x32x16_bf16 v[128:143], a[232:235], a[160:163], v[128:143]\n\t"
    "buffer_load_dwordx4 v103, s[64:67], s72 offen offset:0 sc1 lds\n\t"
    "v_cvt_pk_bf16_f32 v174, v156, v157\n\t"
    "v_permlane16_swap_b32 v169, v173\n\t"
    "v_permlane32_swap_b32 v43, v37\n\t"
    "v_mfma_f32_32x32x16_bf16 v[128:143], a[236:239], a[164:167], v[128:143]\n\t"
    "buffer_load_dwordx4 v103, s[64:67], s72 offen offset:1024 sc1 lds\n\t"
    "v_cvt_pk_bf16_f32 v171, v154, v155\n\t"
    "v_cvt_pk_bf16_f32 v175, v158, v159\n\t"
    "v_permlane16_swap_b32 v170, v174\n\t"
    "v_add_f32 v38, v38, v40\n\t"
    "v_mfma_f32_32x32x16_bf16 v[128:143], a[240:243], a[168:171], v[128:143]\n\t"
    "buffer_load_dwordx4 v104, s[64:67], s72 offen offset:2048 sc1 lds\n\t"
    "v_permlane16_swap_b32 v171, v175\n\t"
    "v_add_f32 v39, v39, v41\n\t"
    "v_add_f32 v36, v36, v42\n\t"
    "v_add_f32_e64 v37, v37, v43\n\t"
    "v_mfma_f32_32x32x16_bf16 v[128:143], a[244:247], a[172:175], v[128:143]\n\t"
    "buffer_load_dwordx4 v104, s[64:67], s72 offen offset:3072 sc1 lds\n\t"
    "s_add_u32 m0, s61, 8192\n\t"
    "v_mov_b32 v40, v38\n\t"
    "v_mov_b32 v41, v39\n\t"
    "s_nop 0\n\t"
    "v_permlane32_swap_b32 v40, v38\n\t"
    "v_mfma_f32_32x32x16_bf16 v[128:143], a[248:251], a[176:179], v[128:143]\n\t"
    "buffer_load_dwordx4 v105, s[64:67], s72 offen offset:0 sc1 lds\n\t"
    "v_permlane32_swap_b32 v41, v39\n\t"
    "v_add_f32 v38, v38, v40\n\t"
    "v_add_f32 v39, v39, v41\n\t"
    "v_mfma_f32_16x16x32_bf16 a[64:67], v[176:179], v[168:171], a[64:67]\n\t"
    "v_mul_f32 v60, v16, v16\n\t"
    "v_mul_f32_e64 v61, v17, v17\n\t"
    "v_mfma_f32_16x16x32_bf16 a[72:75], v[176:179], v[172:175], a[72:75]\n\t"
    "v_fma_f32 v60, v60, s62, v63\n\t"
    "v_fma_f32 v61, v61, s62, v63\n\t"
    "v_mfma_f32_16x16x32_bf16 a[68:71], v[180:183], v[168:171], a[68:71]\n\t"
    "ds_read_b64_tr_b16 v[176:177], v98 offset:36864\n\t"
    "ds_read_b64_tr_b16 v[178:179], v98 offset:37120\n\t"
    "v_mfma_f32_16x16x32_bf16 a[76:79], v[180:183], v[172:175], a[76:79]\n\t"
    "v_mul_f32 v60, v60, v16\n\t"
    "v_mul_f32 v61, v61, v17\n\t"
    "v_mfma_f32_16x16x32_bf16 a[80:83], v[184:187], v[168:171], a[80:83]\n\t"
    "ds_read_b64_tr_b16 v[180:181], v99 offset:37888\n\t"
    "ds_read_b64_tr_b16 v[182:183], v99 offset:38144\n\t"
    "v_mfma_f32_16x16x32_bf16 a[88:91], v[184:187], v[172:175], a[88:91]\n\t"
    "v_exp_f32 v60, v60\n\t"
    "v_mfma_f32_16x16x32_bf16 a[84:87], v[188:191], v[168:171], a[84:87]\n\t"
    "v_exp_f32 v128, v128\n\t"
    "v_mfma_f32_16x16x32_bf16 a[92:95], v[188:191], v[172:175], a[92:95]\n\t"
    "v_exp_f32 v129, v129\n\t"
    "v_mfma_f32_16x16x32_bf16 a[96:99], v[192:195], v[168:171], a[96:99]\n\t"
    "v_exp_f32 v132, v132\n\t"
    "v_mfma_f32_16x16x32_bf16 a[104:107], v[192:195], v[172:175], a[104:107]\n\t"
    "v_exp_f32 v133, v133\n\t"
    "v_mfma_f32_16x16x32_bf16 a[100:103], v[196:199], v[168:171], a[100:103]\n\t"
    "v_exp_f32 v130, v130\n\t"
    "v_mfma_f32_16x16x32_bf16 a[108:111], v[196:199], v[172:175], a[108:111]\n\t"
    "v_exp_f32 v131, v131\n\t"
    "v_mfma_f32_16x16x32_bf16 a[112:115], v[200:203], v[168:171], a[112:115]\n\t"
    "v_exp_f32 v134, v134\n\t"
    "v_mfma_f32_16x16x32_bf16 a[120:123], v[200:203], v[172:175], a[120:123]\n\t"
    "v_exp_f32 v135, v135\n\t"
    "v_mfma_f32_16x16x32_bf16 a[116:119], v[204:207], v[168:171], a[116:119]\n\t"
    "v_exp_f32 v136, v136\n\t"
    "v_mfma_f32_16x16x32_bf16 a[124:127], v[204:207], v[172:175], a[124:127]\n\t"
    "v_exp_f32 v137, v137\n\t"
    "v_mfma_f32_16x16x32_bf16 a[136:139], v[208:211], v[168:171], a[136:139]\n\t"
    "v_exp_f32 v140, v140\n\t"
    "v_mfma_f32_16x16x32_bf16 a[140:143], v[208:211], v[172:175], a[140:143]\n\t"
    "v_exp_f32_e64 v141, v141\n\t"
    "v_mfma_f32_32x32x16_bf16 v[144:159], a[216:219], a[180:183], v[80:95]\n\t"
    "ds_read_b64_tr_b16 v[184:185], v98 offset:38912\n\t"
    "ds_read_b64_tr_b16 v[186:187], v98 offset:39168\n\t"
    "v_exp_f32 v138, v138\n\t"
    "v_exp_f32 v139, v139\n\t"
    "v_mfma_f32_32x32x16_bf16 v[144:159], a[220:223], a[184:187], v[144:159]\n\t"
    "ds_read_b64_tr_b16 v[188:189], v99 offset:39936\n\t"
    "ds_read_b64_tr_b16 v[190:191], v99 offset:40192\n\t"
    "v_cvt_pk_bf16_f32 v160, v128, v129\n\t"
    "v_cvt_pk_bf16_f32 v164, v132, v133\n\t"
    "v_exp_f32_e64 v142, v142\n\t"
    "v_mfma_f32_32x32x16_bf16 v[144:159], a[224:227], a[188:191], v[144:159]\n\t"
    "ds_read_b64_tr_b16 v[192:193], v98 offset:40960\n\t"
    "ds_read_b64_tr_b16 v[194:195], v98 offset:41216\n\t"
    "v_exp_f32_e64 v143, v143\n\t"
    "v_cvt_pk_bf16_f32 v161, v130, v131\n\t"
    "v_mfma_f32_32x32x16_bf16 v[144:159], a[228:231], a[192:195], v[144:159]\n\t"
    "ds_read_b64_tr_b16 v[196:197], v99 offset:41984\n\t"
    "ds_read_b64_tr_b16 v[198:199], v99 offset:42240\n\t"
    "v_permlane16_swap_b32 v160, v164\n\t"
    "v_cvt_pk_bf16_f32 v165, v134, v135\n\t"
    "v_cvt_pk_bf16_f32 v162, v136, v137\n\t"
    "v_mfma_f32_32x32x16_bf16 v[144:159], a[232:235], a[196:199], v[144:159]\n\t"
    "ds_read_b64_tr_b16 v[200:201], v98 offset:43008\n\t"
    "ds_read_b64_tr_b16 v[202:203], v98 offset:43264\n\t"
    "v_cvt_pk_bf16_f32 v166, v140, v141\n\t"
    "v_permlane16_swap_b32 v161, v165\n\t"
    "v_mfma_f32_32x32x16_bf16 v[144:159], a[236:239], a[200:203], v[144:159]\n\t"
    "ds_read_b64_tr_b16 v[204:205], v99 offset:44032\n\t"
    "ds_read_b64_tr_b16 v[206:207], v99 offset:44288\n\t"
    "v_cvt_pk_bf16_f32 v163, v138, v139\n\t"
    "v_cvt_pk_bf16_f32 v167, v142, v143\n\t"
    "v_permlane16_swap_b32 v162, v166\n\t"
    "v_mfma_f32_32x32x16_bf16 v[144:159], a[240:243], a[204:207], v[144:159]\n\t"
    "ds_read_b64_tr_b16 v[208:209], v98 offset:45056\n\t"
    "ds_read_b64_tr_b16 v[210:211], v98 offset:45312\n\t"
    "v_permlane16_swap_b32 v163, v167\n\t"
    "v_exp_f32_e64 v61, v61\n\t"
    "v_mfma_f32_32x32x16_bf16 v[144:159], a[244:247], a[208:211], v[144:159]\n\t"
    "ds_write_b64 v242, v[36:37] offset:8\n\t"
    "ds_write_b64 v251, v[38:39] offset:2056\n\t"
    "v_mfma_f32_32x32x16_bf16 v[144:159], a[248:251], a[212:215], v[144:159]\n\t"
    "s_waitcnt vmcnt(10)\n\t"
    "s_waitcnt lgkmcnt(2)\n\t"
    "v_add_f32 v60, 1.0, v60\n\t"
    "v_add_f32 v61, 1.0, v61\n\t"
    "v_rcp_f32_e64 v60, v60\n\t"
    "v_mfma_f32_16x16x32_bf16 a[0:3], v[176:179], v[160:163], a[0:3]\n\t"
    "ds_read_b128 a[216:219], v246 offset:0\n\t"
    "ds_read_b128 a[220:223], v247 offset:1024\n\t"
    "v_mfma_f32_16x16x32_bf16 a[8:11], v[176:179], v[164:167], a[8:11]\n\t"
    "ds_read_b128 a[224:227], v246 offset:2048\n\t"
    "ds_read_b128 a[228:231], v247 offset:3072\n\t"
    "v_mfma_f32_16x16x32_bf16 a[4:7], v[180:183], v[160:163], a[4:7]\n\t"
    "ds_read_b128 a[232:235], v246 offset:4096\n\t"
    "ds_read_b128 a[236:239], v247 offset:5120\n\t"
    "v_mfma_f32_16x16x32_bf16 a[12:15], v[180:183], v[164:167], a[12:15]\n\t"
    "ds_read_b128 a[240:243], v246 offset:6144\n\t"
    "ds_read_b128 a[244:247], v247 offset:7168\n\t"
    "v_mfma_f32_16x16x32_bf16 a[16:19], v[184:187], v[160:163], a[16:19]\n\t"
    "ds_read_b128 a[248:251], v246 offset:8192\n\t"
    "v_mfma_f32_16x16x32_bf16 a[24:27], v[184:187], v[164:167], a[24:27]\n\t"
    "s_waitcnt lgkmcnt(9)\n\t"
    "s_barrier\n\t"
    "ds_read_b64 v[40:41], v243 offset:8\n\t"
    "ds_read_b64 v[42:43], v243 offset:520\n\t"
    "v_mfma_f32_16x16x32_bf16 a[20:23], v[188:191], v[160:163], a[20:23]\n\t"
    "ds_read_b64 v[44:45], v243 offset:1032\n\t"
    "v_exp_f32_e64 v144, v144\n\t"
    "v_mfma_f32_16x16x32_bf16 a[28:31], v[188:191], v[164:167], a[28:31]\n\t"
    "ds_read_b64 v[46:47], v243 offset:1544\n\t"
    "v_exp_f32_e64 v145, v145\n\t"
    "v_mfma_f32_16x16x32_bf16 a[32:35], v[192:195], v[160:163], a[32:35]\n\t"
    "ds_read_b64 v[48:49], v243 offset:2056\n\t"
    "v_exp_f32_e64 v148, v148\n\t"
    "v_mfma_f32_16x16x32_bf16 a[40:43], v[192:195], v[164:167], a[40:43]\n\t"
    "ds_read_b64 v[50:51], v244 offset:8\n\t"
    "v_exp_f32_e64 v149, v149\n\t"
    "v_mfma_f32_16x16x32_bf16 a[36:39], v[196:199], v[160:163], a[36:39]\n\t"
    "ds_read_b64 v[52:53], v244 offset:520\n\t"
    "v_exp_f32_e64 v146, v146\n\t"
    "v_mfma_f32_16x16x32_bf16 a[44:47], v[196:199], v[164:167], a[44:47]\n\t"
    "ds_read_b64 v[54:55], v244 offset:1032\n\t"
    "v_exp_f32_e64 v147, v147\n\t"
    "v_mfma_f32_16x16x32_bf16 a[48:51], v[200:203], v[160:163], a[48:51]\n\t"
    "ds_read_b64 v[56:57], v244 offset:1544\n\t"
    "v_exp_f32_e64 v150, v150\n\t"
    "v_mfma_f32_16x16x32_bf16 a[56:59], v[200:203], v[164:167], a[56:59]\n\t"
    "ds_read_b64 v[38:39], v244 offset:2056\n\t"
    "v_exp_f32 v151, v151\n\t"
    "v_mfma_f32_16x16x32_bf16 a[52:55], v[204:207], v[160:163], a[52:55]\n\t"
    "v_exp_f32 v152, v152\n\t"
    "v_mfma_f32_16x16x32_bf16 a[60:63], v[204:207], v[164:167], a[60:63]\n\t"
    "s_mov_b32 s72, 64512\n\t"
    "v_exp_f32_e64 v153, v153\n\t"
    "v_mfma_f32_16x16x32_bf16 a[128:131], v[208:211], v[160:163], a[128:131]\n\t"
    "s_add_u32 m0, s61, 36864\n\t"
    "v_exp_f32 v156, v156\n\t"
    "v_mfma_f32_16x16x32_bf16 a[132:135], v[208:211], v[164:167], a[132:135]\n\t"
    "s_waitcnt lgkmcnt(10)\n\t"
    "v_exp_f32 v157, v157\n\t"
    "v_mfma_f32_32x32x16_bf16 v[128:143], a[216:219], a[144:147], v[64:79]\n\t"
    "buffer_load_dwordx4 v101, s[64:67], s72 offen offset:0 sc1 lds\n\t"
    "v_exp_f32 v154, v154\n\t"
    "v_exp_f32_e64 v155, v155\n\t"
    "v_mfma_f32_32x32x16_bf16 v[128:143], a[220:223], a[148:151], v[128:143]\n\t"
    "buffer_load_dwordx4 v101, s[64:67], s72 offen offset:1024 sc1 lds\n\t"
    "v_cvt_pk_bf16_f32 v168, v144, v145\n\t"
    "v_cvt_pk_bf16_f32 v172, v148, v149\n\t"
    "v_exp_f32 v158, v158\n\t"
    "v_mfma_f32_32x32x16_bf16 v[128:143], a[224:227], a[152:155], v[128:143]\n\t"
    "buffer_load_dwordx4 v102, s[64:67], s72 offen offset:2048 sc1 lds\n\t"
    "v_exp_f32 v159, v159\n\t"
    "v_cvt_pk_bf16_f32 v169, v146, v147\n\t"
    "s_waitcnt lgkmcnt(8)\n\t"
    "v_add_f32 v40, v40, v42\n\t"
    "v_add_f32_e64 v41, v41, v43\n\t"
    "v_mfma_f32_32x32x16_bf16 v[128:143], a[228:231], a[156:159], v[128:143]\n\t"
    "buffer_load_dwordx4 v102, s[64:67], s72 offen offset:3072 sc1 lds\n\t"
    "s_add_u32 m0, s61, 40960\n\t"
    "v_permlane16_swap_b32 v168, v172\n\t"
    "v_cvt_pk_bf16_f32 v173, v150, v151\n\t"
    "v_cvt_pk_bf16_f32 v170, v152, v153\n\t"
    "v_mfma_f32_32x32x16_bf16 v[128:143], a[232:235], a[160:163], v[128:143]\n\t"
    "buffer_load_dwordx4 v103, s[64:67], s72 offen offset:0 sc1 lds\n\t"
    "v_cvt_pk_bf16_f32 v174, v156, v157\n\t"
    "v_permlane16_swap_b32 v169, v173\n\t"
    "s_waitcnt lgkmcnt(6)\n\t"
    "v_add_f32 v44, v44, v46\n\t"
    "v_add_f32_e64 v45, v45, v47\n\t"
    "v_mfma_f32_32x32x16_bf16 v[128:143], a[236:239], a[164:167], v[128:143]\n\t"
    "buffer_load_dwordx4 v103, s[64:67], s72 offen offset:1024 sc1 lds\n\t"
    "v_cvt_pk_bf16_f32 v171, v154, v155\n\t"
    "v_cvt_pk_bf16_f32 v175, v158, v159\n\t"
    "v_permlane16_swap_b32 v170, v174\n\t"
    "s_waitcnt lgkmcnt(3)\n\t"
    "v_add_f32 v50, v50, v52\n\t"
    "v_mfma_f32_32x32x16_bf16 v[128:143], a[240:243], a[168:171], v[128:143]\n\t"
    "buffer_load_dwordx4 v104, s[64:67], s72 offen offset:2048 sc1 lds\n\t"
    "v_permlane16_swap_b32 v171, v175\n\t"
    "v_add_f32 v51, v51, v53\n\t"
    "s_waitcnt lgkmcnt(1)\n\t"
    "v_add_f32 v54, v54, v56\n\t"
    "v_add_f32 v55, v55, v57\n\t"
    "v_mfma_f32_32x32x16_bf16 v[128:143], a[244:247], a[172:175], v[128:143]\n\t"
    "buffer_load_dwordx4 v104, s[64:67], s72 offen offset:3072 sc1 lds\n\t"
    "s_add_u32 m0, s61, 45056\n\t"
    "v_add_f32 v40, v40, v48\n\t"
    "v_add_f32 v41, v41, v49\n\t"
    "s_waitcnt lgkmcnt(0)\n\t"
    "v_add_f32 v38, v38, v50\n\t"
    "v_add_f32 v39, v39, v51\n\t"
    "v_mfma_f32_32x32x16_bf16 v[128:143], a[248:251], a[176:179], v[128:143]\n\t"
    "buffer_load_dwordx4 v105, s[64:67], s72 offen offset:0 sc1 lds\n\t"
    "v_add_f32 v40, v40, v44\n\t"
    "v_add_f32 v41, v41, v45\n\t"
    "v_add_f32 v38, v38, v54\n\t"
    "v_add_f32_e64 v39, v39, v55\n\t"
    "v_mul_f32 v40, 0x3be38e39, v40\n\t"
    "v_mfma_f32_16x16x32_bf16 a[64:67], v[176:179], v[168:171], a[64:67]\n\t"
    "v_mul_f32 v38, 0x3be38e39, v38\n\t"
    "v_mul_f32 v41, 0x3be38e39, v41\n\t"
    "v_mul_f32 v39, 0x3be38e39, v39\n\t"
    "v_mfma_f32_16x16x32_bf16 a[72:75], v[176:179], v[172:175], a[72:75]\n\t"
    "v_fma_f32 v41, -v40, v40, v41\n\t"
    "v_fma_f32 v39, -v38, v38, v39\n\t"
    "v_max_f32_e64 v41, 0, v41\n\t"
    "v_mfma_f32_16x16x32_bf16 a[68:71], v[180:183], v[168:171], a[68:71]\n\t"
    "ds_read_b64_tr_b16 v[176:177], v248 offset:0\n\t"
    "ds_read_b64_tr_b16 v[178:179], v248 offset:256\n\t"
    "v_max_f32_e64 v39, 0, v39\n\t"
    "v_mfma_f32_16x16x32_bf16 a[76:79], v[180:183], v[172:175], a[76:79]\n\t"
    "v_add_f32 v41, 0x341b32b8, v41\n\t"
    "v_add_f32 v39, 0x341b32b8, v39\n\t"
    "v_mfma_f32_16x16x32_bf16 a[80:83], v[184:187], v[168:171], a[80:83]\n\t"
    "ds_read_b64_tr_b16 v[180:181], v249 offset:1024\n\t"
    "ds_read_b64_tr_b16 v[182:183], v249 offset:1280\n\t"
    "v_mfma_f32_16x16x32_bf16 a[88:91], v[184:187], v[172:175], a[88:91]\n\t"
    "v_rsq_f32 v42, v41\n\t"
    "v_mfma_f32_16x16x32_bf16 a[84:87], v[188:191], v[168:171], a[84:87]\n\t"
    "v_exp_f32 v128, v128\n\t"
    "v_mfma_f32_16x16x32_bf16 a[92:95], v[188:191], v[172:175], a[92:95]\n\t"
    "v_exp_f32 v129, v129\n\t"
    "v_mfma_f32_16x16x32_bf16 a[96:99], v[192:195], v[168:171], a[96:99]\n\t"
    "v_exp_f32 v132, v132\n\t"
    "v_mfma_f32_16x16x32_bf16 a[104:107], v[192:195], v[172:175], a[104:107]\n\t"
    "v_exp_f32 v133, v133\n\t"
    "v_mfma_f32_16x16x32_bf16 a[100:103], v[196:199], v[168:171], a[100:103]\n\t"
    "v_exp_f32 v130, v130\n\t"
    "v_mfma_f32_16x16x32_bf16 a[108:111], v[196:199], v[172:175], a[108:111]\n\t"
    "v_exp_f32 v131, v131\n\t"
    "v_mfma_f32_16x16x32_bf16 a[112:115], v[200:203], v[168:171], a[112:115]\n\t"
    "v_exp_f32 v134, v134\n\t"
    "v_mfma_f32_16x16x32_bf16 a[120:123], v[200:203], v[172:175], a[120:123]\n\t"
    "v_exp_f32 v135, v135\n\t"
    "v_mfma_f32_16x16x32_bf16 a[116:119], v[204:207], v[168:171], a[116:119]\n\t"
    "v_exp_f32 v136, v136\n\t"
    "v_mfma_f32_16x16x32_bf16 a[124:127], v[204:207], v[172:175], a[124:127]\n\t"
    "v_exp_f32 v137, v137\n\t"
    "v_mfma_f32_16x16x32_bf16 a[136:139], v[208:211], v[168:171], a[136:139]\n\t"
    "v_exp_f32 v140, v140\n\t"
    "v_mfma_f32_16x16x32_bf16 a[140:143], v[208:211], v[172:175], a[140:143]\n\t"
    "v_exp_f32_e64 v141, v141\n\t"
    "v_mfma_f32_32x32x16_bf16 v[144:159], a[216:219], a[180:183], v[80:95]\n\t"
    "ds_read_b64_tr_b16 v[184:185], v248 offset:2048\n\t"
    "ds_read_b64_tr_b16 v[186:187], v248 offset:2304\n\t"
    "v_exp_f32 v138, v138\n\t"
    "v_exp_f32 v139, v139\n\t"
    "v_mfma_f32_32x32x16_bf16 v[144:159], a[220:223], a[184:187], v[144:159]\n\t"
    "ds_read_b64_tr_b16 v[188:189], v249 offset:3072\n\t"
    "ds_read_b64_tr_b16 v[190:191], v249 offset:3328\n\t"
    "v_cvt_pk_bf16_f32 v160, v128, v129\n\t"
    "v_cvt_pk_bf16_f32 v164, v132, v133\n\t"
    "v_exp_f32_e64 v142, v142\n\t"
    "v_mfma_f32_32x32x16_bf16 v[144:159], a[224:227], a[188:191], v[144:159]\n\t"
    "ds_read_b64_tr_b16 v[192:193], v248 offset:4096\n\t"
    "ds_read_b64_tr_b16 v[194:195], v248 offset:4352\n\t"
    "v_exp_f32 v143, v143\n\t"
    "v_cvt_pk_bf16_f32 v161, v130, v131\n\t"
    "v_rsq_f32 v36, v39\n\t"
    "v_mfma_f32_32x32x16_bf16 v[144:159], a[228:231], a[192:195], v[144:159]\n\t"
    "ds_read_b64_tr_b16 v[196:197], v249 offset:5120\n\t"
    "ds_read_b64_tr_b16 v[198:199], v249 offset:5376\n\t"
    "v_permlane16_swap_b32 v160, v164\n\t"
    "v_cvt_pk_bf16_f32 v165, v134, v135\n\t"
    "v_cvt_pk_bf16_f32 v162, v136, v137\n\t"
    "v_mul_f32 v43, -v40, v42\n\t"
    "v_mfma_f32_32x32x16_bf16 v[144:159], a[232:235], a[196:199], v[144:159]\n\t"
    "ds_read_b64_tr_b16 v[200:201], v248 offset:6144\n\t"
    "ds_read_b64_tr_b16 v[202:203], v248 offset:6400\n\t"
    "v_cvt_pk_bf16_f32 v166, v140, v141\n\t"
    "v_permlane16_swap_b32 v161, v165\n\t"
    "v_mul_f32 v37, -v38, v36\n\t"
    "v_cndmask_b32 v36, 1.0, v36, s[74:75]\n\t"
    "v_mfma_f32_32x32x16_bf16 v[144:159], a[236:239], a[200:203], v[144:159]\n\t"
    "ds_read_b64_tr_b16 v[204:205], v249 offset:7168\n\t"
    "ds_read_b64_tr_b16 v[206:207], v249 offset:7424\n\t"
    "v_cvt_pk_bf16_f32 v163, v138, v139\n\t"
    "v_cvt_pk_bf16_f32 v167, v142, v143\n\t"
    "v_permlane16_swap_b32 v162, v166\n\t"
    "v_cndmask_b32 v37, 0, v37, s[74:75]\n\t"
    "v_mfma_f32_32x32x16_bf16 v[144:159], a[240:243], a[204:207], v[144:159]\n\t"
    "ds_read_b64_tr_b16 v[208:209], v248 offset:8192\n\t"
    "ds_read_b64_tr_b16 v[210:211], v248 offset:8448\n\t"
    "v_permlane16_swap_b32 v163, v167\n\t"
    "v_fma_f32 v0, v0, v42, v43\n\t"
    "v_fma_f32 v1, v1, v42, v43\n\t"
    "v_cvt_pk_bf16_f32 v0, v0, v1\n\t"
    "v_mfma_f32_32x32x16_bf16 v[144:159], a[244:247], a[208:211], v[144:159]\n\t"
    "v_fma_f32 v2, v2, v42, v43\n\t"
    "v_fma_f32 v3, v3, v42, v43\n\t"
    "v_cvt_pk_bf16_f32 v1, v2, v3\n\t"
    "v_fma_f32 v4, v4, v42, v43\n\t"
    "v_fma_f32 v5, v5, v42, v43\n\t"
    "v_cvt_pk_bf16_f32 v2, v4, v5\n\t"
    "v_fma_f32 v6, v6, v42, v43\n\t"
    "v_mfma_f32_32x32x16_bf16 v[144:159], a[248:251], a[212:215], v[144:159]\n\t"
    "s_waitcnt vmcnt(9)\n\t"
    "s_waitcnt lgkmcnt(0)\n\t"
    "v_fma_f32 v7, v7, v42, v43\n\t"
    "v_cvt_pk_bf16_f32 v3, v6, v7\n\t"
    "v_fma_f32 v8, v8, v42, v43\n\t"
    "v_fma_f32 v9, v9, v42, v43\n\t"
    "v_cvt_pk_bf16_f32 v4, v8, v9\n\t"
    "v_fma_f32 v10, v10, v42, v43\n\t"
    "v_mfma_f32_16x16x32_bf16 a[0:3], v[176:179], v[160:163], a[0:3]\n\t"
    "ds_read_b128 a[216:219], v96 offset:0\n\t"
    "ds_read_b128 a[220:223], v97 offset:1024\n\t"
    "v_fma_f32 v11, v11, v42, v43\n\t"
    "v_mfma_f32_16x16x32_bf16 a[8:11], v[176:179], v[164:167], a[8:11]\n\t"
    "ds_read_b128 a[224:227], v96 offset:2048\n\t"
    "ds_read_b128 a[228:231], v97 offset:3072\n\t"
    "v_cvt_pk_bf16_f32 v5, v10, v11\n\t"
    "v_mfma_f32_16x16x32_bf16 a[4:7], v[180:183], v[160:163], a[4:7]\n\t"
    "ds_read_b128 a[232:235], v96 offset:4096\n\t"
    "ds_read_b128 a[236:239], v97 offset:5120\n\t"
    "v_fma_f32 v12, v12, v42, v43\n\t"
    "v_mfma_f32_16x16x32_bf16 a[12:15], v[180:183], v[164:167], a[12:15]\n\t"
    "ds_read_b128 a[240:243], v96 offset:6144\n\t"
    "ds_read_b128 a[244:247], v97 offset:7168\n\t"
    "v_fma_f32 v13, v13, v42, v43\n\t"
    "v_mfma_f32_16x16x32_bf16 a[16:19], v[184:187], v[160:163], a[16:19]\n\t"
    "ds_read_b128 a[248:251], v96 offset:8192\n\t"
    "v_cvt_pk_bf16_f32 v6, v12, v13\n\t"
    "v_fma_f32 v14, v14, v42, v43\n\t"
    "v_mfma_f32_16x16x32_bf16 a[24:27], v[184:187], v[164:167], a[24:27]\n\t"
    "v_fma_f32 v15, v15, v42, v43\n\t"
    "v_cvt_pk_bf16_f32 v7, v14, v15\n\t"
    "v_fma_f32 v32, v32, v36, v37\n\t"
    "v_mfma_f32_16x16x32_bf16 a[20:23], v[188:191], v[160:163], a[20:23]\n\t"
    "v_exp_f32_e64 v144, v144\n\t"
    "v_fma_f32 v33, v33, v36, v37\n\t"
    "v_mfma_f32_16x16x32_bf16 a[28:31], v[188:191], v[164:167], a[28:31]\n\t"
    "v_exp_f32_e64 v145, v145\n\t"
    "v_cvt_pk_bf16_f32 v32, v32, v33\n\t"
    "v_mfma_f32_16x16x32_bf16 a[32:35], v[192:195], v[160:163], a[32:35]\n\t"
    "v_exp_f32_e64 v148, v148\n\t"
    "v_fma_f32 v34, v34, v36, v37\n\t"
    "v_mfma_f32_16x16x32_bf16 a[40:43], v[192:195], v[164:167], a[40:43]\n\t"
    "v_exp_f32_e64 v149, v149\n\t"
    "v_fma_f32 v35, v35, v36, v37\n\t"
    "v_mfma_f32_16x16x32_bf16 a[36:39], v[196:199], v[160:163], a[36:39]\n\t"
    "v_exp_f32_e64 v146, v146\n\t"
    "v_cvt_pk_bf16_f32 v33, v34, v35\n\t"
    "v_mfma_f32_16x16x32_bf16 a[44:47], v[196:199], v[164:167], a[44:47]\n\t"
    "ds_write_b64 v212, v[0:1] offset:38400\n\t"
    "v_exp_f32 v147, v147\n\t"
    "v_mfma_f32_16x16x32_bf16 a[48:51], v[200:203], v[160:163], a[48:51]\n\t"
    "v_exp_f32 v150, v150\n\t"
    "v_mfma_f32_16x16x32_bf16 a[56:59], v[200:203], v[164:167], a[56:59]\n\t"
    "v_exp_f32_e64 v151, v151\n\t"
    "v_mfma_f32_16x16x32_bf16 a[52:55], v[204:207], v[160:163], a[52:55]\n\t"
    "ds_write_b64 v213, v[2:3] offset:38912\n\t"
    "v_exp_f32_e64 v152, v152\n\t"
    "v_mfma_f32_16x16x32_bf16 a[60:63], v[204:207], v[164:167], a[60:63]\n\t"
    "s_mov_b32 s72, 73728\n\t"
    "v_exp_f32_e64 v153, v153\n\t"
    "v_mfma_f32_16x16x32_bf16 a[128:131], v[208:211], v[160:163], a[128:131]\n\t"
    "s_add_u32 m0, s61, 73728\n\t"
    "v_exp_f32 v156, v156\n\t"
    "v_mfma_f32_16x16x32_bf16 a[132:135], v[208:211], v[164:167], a[132:135]\n\t"
    "s_waitcnt lgkmcnt(2)\n\t"
    "ds_write_b64 v214, v[4:5] offset:39424\n\t"
    "v_exp_f32 v157, v157\n\t"
    "v_mfma_f32_32x32x16_bf16 v[128:143], a[216:219], a[144:147], v[64:79]\n\t"
    "buffer_load_dwordx4 v101, s[64:67], s72 offen offset:0 sc1 lds\n\t"
    "v_exp_f32 v154, v154\n\t"
    "v_exp_f32_e64 v155, v155\n\t"
    "v_mfma_f32_32x32x16_bf16 v[128:143], a[220:223], a[148:151], v[128:143]\n\t"
    "buffer_load_dwordx4 v101, s[64:67], s72 offen offset:1024 sc1 lds\n\t"
    "v_cvt_pk_bf16_f32 v168, v144, v145\n\t"
    "v_cvt_pk_bf16_f32 v172, v148, v149\n\t"
    "v_exp_f32 v158, v158\n\t"
    "v_mfma_f32_32x32x16_bf16 v[128:143], a[224:227], a[152:155], v[128:143]\n\t"
    "buffer_load_dwordx4 v102, s[64:67], s72 offen offset:2048 sc1 lds\n\t"
    "v_exp_f32 v159, v159\n\t"
    "v_cvt_pk_bf16_f32 v169, v146, v147\n\t"
    "v_mfma_f32_32x32x16_bf16 v[128:143], a[228:231], a[156:159], v[128:143]\n\t"
    "buffer_load_dwordx4 v102, s[64:67], s72 offen offset:3072 sc1 lds\n\t"
    "s_add_u32 m0, s61, 77824\n\t"
    "v_permlane16_swap_b32 v168, v172\n\t"
    "v_cvt_pk_bf16_f32 v173, v150, v151\n\t"
    "v_cvt_pk_bf16_f32 v170, v152, v153\n\t"
    "v_mfma_f32_32x32x16_bf16 v[128:143], a[232:235], a[160:163], v[128:143]\n\t"
    "buffer_load_dwordx4 v103, s[64:67], s72 offen offset:0 sc1 lds\n\t"
    "v_cvt_pk_bf16_f32 v174, v156, v157\n\t"
    "v_permlane16_swap_b32 v169, v173\n\t"
    "v_mfma_f32_32x32x16_bf16 v[128:143], a[236:239], a[164:167], v[128:143]\n\t"
    "buffer_load_dwordx4 v103, s[64:67], s72 offen offset:1024 sc1 lds\n\t"
    "v_cvt_pk_bf16_f32 v171, v154, v155\n\t"
    "v_cvt_pk_bf16_f32 v175, v158, v159\n\t"
    "v_permlane16_swap_b32 v170, v174\n\t"
    "v_mfma_f32_32x32x16_bf16 v[128:143], a[240:243], a[168:171], v[128:143]\n\t"
    "buffer_load_dwordx4 v104, s[64:67], s72 offen offset:2048 sc1 lds\n\t"
    "v_permlane16_swap_b32 v171, v175\n\t"
    "v_rcp_f32_e64 v61, v61\n\t"
    "v_mfma_f32_32x32x16_bf16 v[128:143], a[244:247], a[172:175], v[128:143]\n\t"
    "buffer_load_dwordx4 v104, s[64:67], s72 offen offset:3072 sc1 lds\n\t"
    "s_add_u32 m0, s61, 81920\n\t"
    "v_mul_f32 v16, v16, v60\n\t"
    "v_mul_f32 v17, v17, v61\n\t"
    "v_mul_f32 v60, v18, v18\n\t"
    "v_mfma_f32_32x32x16_bf16 v[128:143], a[248:251], a[176:179], v[128:143]\n\t"
    "buffer_load_dwordx4 v105, s[64:67], s72 offen offset:0 sc1 lds\n\t"
    "v_mul_f32 v61, v19, v19\n\t"
    "v_fma_f32 v60, v60, s62, v63\n\t"
    "v_fma_f32 v61, v61, s62, v63\n\t"
    "v_mul_f32 v60, v60, v18\n\t"
    "v_mfma_f32_16x16x32_bf16 a[64:67], v[176:179], v[168:171], a[64:67]\n\t"
    "v_mul_f32 v61, v61, v19\n\t"
    "v_mfma_f32_16x16x32_bf16 a[72:75], v[176:179], v[172:175], a[72:75]\n\t"
    "v_exp_f32_e64 v60, v60\n\t"
    "v_mfma_f32_16x16x32_bf16 a[68:71], v[180:183], v[168:171], a[68:71]\n\t"
    "ds_read_b64_tr_b16 v[176:177], v98 offset:0\n\t"
    "ds_read_b64_tr_b16 v[178:179], v98 offset:256\n\t"
    "v_mfma_f32_16x16x32_bf16 a[76:79], v[180:183], v[172:175], a[76:79]\n\t"
    "v_exp_f32_e64 v61, v61\n\t"
    "v_mfma_f32_16x16x32_bf16 a[80:83], v[184:187], v[168:171], a[80:83]\n\t"
    "ds_read_b64_tr_b16 v[180:181], v99 offset:1024\n\t"
    "ds_read_b64_tr_b16 v[182:183], v99 offset:1280\n\t"
    "v_mfma_f32_16x16x32_bf16 a[88:91], v[184:187], v[172:175], a[88:91]\n\t"
    "v_add_f32 v60, 1.0, v60\n\t"
    "v_add_f32 v61, 1.0, v61\n\t"
    "v_mfma_f32_16x16x32_bf16 a[84:87], v[188:191], v[168:171], a[84:87]\n\t"
    "v_exp_f32 v128, v128\n\t"
    "v_mfma_f32_16x16x32_bf16 a[92:95], v[188:191], v[172:175], a[92:95]\n\t"
    "v_exp_f32 v129, v129\n\t"
    "v_mfma_f32_16x16x32_bf16 a[96:99], v[192:195], v[168:171], a[96:99]\n\t"
    "v_exp_f32 v132, v132\n\t"
    "v_mfma_f32_16x16x32_bf16 a[104:107], v[192:195], v[172:175], a[104:107]\n\t"
    "v_exp_f32 v133, v133\n\t"
    "v_mfma_f32_16x16x32_bf16 a[100:103], v[196:199], v[168:171], a[100:103]\n\t"
    "v_exp_f32 v130, v130\n\t"
    "v_mfma_f32_16x16x32_bf16 a[108:111], v[196:199], v[172:175], a[108:111]\n\t"
    "v_exp_f32 v131, v131\n\t"
    "v_mfma_f32_16x16x32_bf16 a[112:115], v[200:203], v[168:171], a[112:115]\n\t"
    "v_exp_f32 v134, v134\n\t"
    "v_mfma_f32_16x16x32_bf16 a[120:123], v[200:203], v[172:175], a[120:123]\n\t"
    "v_exp_f32 v135, v135\n\t"
    "v_mfma_f32_16x16x32_bf16 a[116:119], v[204:207], v[168:171], a[116:119]\n\t"
    "v_exp_f32 v136, v136\n\t"
    "v_mfma_f32_16x16x32_bf16 a[124:127], v[204:207], v[172:175], a[124:127]\n\t"
    "v_exp_f32 v137, v137\n\t"
    "v_mfma_f32_16x16x32_bf16 a[136:139], v[208:211], v[168:171], a[136:139]\n\t"
    "v_exp_f32 v140, v140\n\t"
    "v_mfma_f32_16x16x32_bf16 a[140:143], v[208:211], v[172:175], a[140:143]\n\t"
    "v_exp_f32 v141, v141\n\t"
    "v_mfma_f32_32x32x16_bf16 v[144:159], a[216:219], a[180:183], v[80:95]\n\t"
    "ds_read_b64_tr_b16 v[184:185], v98 offset:2048\n\t"
    "ds_read_b64_tr_b16 v[186:187], v98 offset:2304\n\t"
    "v_exp_f32 v138, v138\n\t"
    "v_exp_f32 v139, v139\n\t"
    "v_mfma_f32_32x32x16_bf16 v[144:159], a[220:223], a[184:187], v[144:159]\n\t"
    "ds_read_b64_tr_b16 v[188:189], v99 offset:3072\n\t"
    "ds_read_b64_tr_b16 v[190:191], v99 offset:3328\n\t"
    "v_cvt_pk_bf16_f32 v160, v128, v129\n\t"
    "v_cvt_pk_bf16_f32 v164, v132, v133\n\t"
    "v_exp_f32_e64 v142, v142\n\t"
    "v_mfma_f32_32x32x16_bf16 v[144:159], a[224:227], a[188:191], v[144:159]\n\t"
    "ds_read_b64_tr_b16 v[192:193], v98 offset:4096\n\t"
    "ds_read_b64_tr_b16 v[194:195], v98 offset:4352\n\t"
    "v_exp_f32_e64 v143, v143\n\t"
    "v_cvt_pk_bf16_f32 v161, v130, v131\n\t"
    "v_mfma_f32_32x32x16_bf16 v[144:159], a[228:231], a[192:195], v[144:159]\n\t"
    "ds_read_b64_tr_b16 v[196:197], v99 offset:5120\n\t"
    "ds_read_b64_tr_b16 v[198:199], v99 offset:5376\n\t"
    "v_permlane16_swap_b32 v160, v164\n\t"
    "v_cvt_pk_bf16_f32 v165, v134, v135\n\t"
    "v_cvt_pk_bf16_f32 v162, v136, v137\n\t"
    "v_mfma_f32_32x32x16_bf16 v[144:159], a[232:235], a[196:199], v[144:159]\n\t"
    "ds_read_b64_tr_b16 v[200:201], v98 offset:6144\n\t"
    "ds_read_b64_tr_b16 v[202:203], v98 offset:6400\n\t"
    "v_cvt_pk_bf16_f32 v166, v140, v141\n\t"
    "v_permlane16_swap_b32 v161, v165\n\t"
    "v_mfma_f32_32x32x16_bf16 v[144:159], a[236:239], a[200:203], v[144:159]\n\t"
    "ds_read_b64_tr_b16 v[204:205], v99 offset:7168\n\t"
    "ds_read_b64_tr_b16 v[206:207], v99 offset:7424\n\t"
    "v_cvt_pk_bf16_f32 v163, v138, v139\n\t"
    "v_cvt_pk_bf16_f32 v167, v142, v143\n\t"
    "v_permlane16_swap_b32 v162, v166\n\t"
    "v_mfma_f32_32x32x16_bf16 v[144:159], a[240:243], a[204:207], v[144:159]\n\t"
    "ds_read_b64_tr_b16 v[208:209], v98 offset:8192\n\t"
    "ds_read_b64_tr_b16 v[210:211], v98 offset:8448\n\t"
    "v_permlane16_swap_b32 v163, v167\n\t"
    "v_rcp_f32_e64 v60, v60\n\t"
    "v_mfma_f32_32x32x16_bf16 v[144:159], a[244:247], a[208:211], v[144:159]\n\t"
    "ds_write_b64 v215, v[6:7] offset:39936\n\t"
    "ds_write_b64 v216, v[32:33] offset:38400\n\t"
    "v_mfma_f32_32x32x16_bf16 v[144:159], a[248:251], a[212:215], v[144:159]\n\t"
    "s_waitcnt vmcnt(9)\n\t"
    "s_waitcnt lgkmcnt(2)\n\t"
    "v_rcp_f32 v61, v61\n\t"
    "v_mul_f32 v18, v18, v60\n\t"
    "v_mul_f32 v19, v19, v61\n\t"
    "v_mul_f32 v60, v20, v20\n\t"
    "v_mfma_f32_16x16x32_bf16 a[0:3], v[176:179], v[160:163], a[0:3]\n\t"
    "ds_read_b128 a[216:219], v96 offset:36864\n\t"
    "ds_read_b128 a[220:223], v97 offset:37888\n\t"
    "v_mfma_f32_16x16x32_bf16 a[8:11], v[176:179], v[164:167], a[8:11]\n\t"
    "ds_read_b128 a[224:227], v96 offset:38912\n\t"
    "ds_read_b128 a[228:231], v97 offset:39936\n\t"
    "v_mfma_f32_16x16x32_bf16 a[4:7], v[180:183], v[160:163], a[4:7]\n\t"
    "ds_read_b128 a[232:235], v96 offset:40960\n\t"
    "ds_read_b128 a[236:239], v97 offset:41984\n\t"
    "v_mfma_f32_16x16x32_bf16 a[12:15], v[180:183], v[164:167], a[12:15]\n\t"
    "ds_read_b128 a[240:243], v96 offset:43008\n\t"
    "ds_read_b128 a[244:247], v97 offset:44032\n\t"
    "v_mfma_f32_16x16x32_bf16 a[16:19], v[184:187], v[160:163], a[16:19]\n\t"
    "ds_read_b128 a[248:251], v96 offset:45056\n\t"
    "v_mul_f32_e64 v61, v21, v21\n\t"
    "v_mfma_f32_16x16x32_bf16 a[24:27], v[184:187], v[164:167], a[24:27]\n\t"
    "s_waitcnt lgkmcnt(9)\n\t"
    "s_barrier\n\t"
    "ds_read_b64_tr_b16 v[40:41], v217 offset:0\n\t"
    "ds_read_b64_tr_b16 v[42:43], v217 offset:2048\n\t"
    "v_mfma_f32_16x16x32_bf16 a[20:23], v[188:191], v[160:163], a[20:23]\n\t"
    "ds_read_b64_tr_b16 v[44:45], v217 offset:4096\n\t"
    "v_exp_f32_e64 v144, v144\n\t"
    "v_mfma_f32_16x16x32_bf16 a[28:31], v[188:191], v[164:167], a[28:31]\n\t"
    "ds_read_b64_tr_b16 v[46:47], v217 offset:6144\n\t"
    "v_exp_f32_e64 v145, v145\n\t"
    "v_mfma_f32_16x16x32_bf16 a[32:35], v[192:195], v[160:163], a[32:35]\n\t"
    "ds_read_b64_tr_b16 v[48:49], v217 offset:8192\n\t"
    "v_exp_f32 v148, v148\n\t"
    "v_mfma_f32_16x16x32_bf16 a[40:43], v[192:195], v[164:167], a[40:43]\n\t"
    "v_exp_f32 v149, v149\n\t"
    "v_mfma_f32_16x16x32_bf16 a[36:39], v[196:199], v[160:163], a[36:39]\n\t"
    "v_exp_f32 v146, v146\n\t"
    "v_mfma_f32_16x16x32_bf16 a[44:47], v[196:199], v[164:167], a[44:47]\n\t"
    "v_exp_f32 v147, v147\n\t"
    "v_mfma_f32_16x16x32_bf16 a[48:51], v[200:203], v[160:163], a[48:51]\n\t"
    "v_exp_f32 v150, v150\n\t"
    "v_mfma_f32_16x16x32_bf16 a[56:59], v[200:203], v[164:167], a[56:59]\n\t"
    "v_exp_f32 v151, v151\n\t"
    "v_mfma_f32_16x16x32_bf16 a[52:55], v[204:207], v[160:163], a[52:55]\n\t"
    "v_exp_f32_e64 v152, v152\n\t"
    "v_mfma_f32_16x16x32_bf16 a[60:63], v[204:207], v[164:167], a[60:63]\n\t"
    "s_mov_b32 s72, 82944\n\t"
    "v_exp_f32 v153, v153\n\t"
    "v_mfma_f32_16x16x32_bf16 a[128:131], v[208:211], v[160:163], a[128:131]\n\t"
    "s_add_u32 m0, s61, 0\n\t"
    "v_exp_f32 v156, v156\n\t"
    "v_mfma_f32_16x16x32_bf16 a[132:135], v[208:211], v[164:167], a[132:135]\n\t"
    "s_waitcnt lgkmcnt(5)\n\t"
    "s_waitcnt lgkmcnt(4)\n\t"
    "ds_write_b64 v218, v[40:41] offset:0\n\t"
    "v_exp_f32 v157, v157\n\t"
    "v_mfma_f32_32x32x16_bf16 v[128:143], a[216:219], a[144:147], v[64:79]\n\t"
    "buffer_load_dwordx4 v101, s[64:67], s72 offen offset:0 sc1 lds\n\t"
    "v_exp_f32 v154, v154\n\t"
    "v_exp_f32 v155, v155\n\t"
    "v_mfma_f32_32x32x16_bf16 v[128:143], a[220:223], a[148:151], v[128:143]\n\t"
    "buffer_load_dwordx4 v101, s[64:67], s72 offen offset:1024 sc1 lds\n\t"
    "v_cvt_pk_bf16_f32 v168, v144, v145\n\t"
    "v_cvt_pk_bf16_f32 v172, v148, v149\n\t"
    "v_exp_f32 v158, v158\n\t"
    "v_mfma_f32_32x32x16_bf16 v[128:143], a[224:227], a[152:155], v[128:143]\n\t"
    "buffer_load_dwordx4 v102, s[64:67], s72 offen offset:2048 sc1 lds\n\t"
    "v_exp_f32 v159, v159\n\t"
    "v_cvt_pk_bf16_f32 v169, v146, v147\n\t"
    "v_fma_f32 v60, v60, s62, v63\n\t"
    "v_mfma_f32_32x32x16_bf16 v[128:143], a[228:231], a[156:159], v[128:143]\n\t"
    "buffer_load_dwordx4 v102, s[64:67], s72 offen offset:3072 sc1 lds\n\t"
    "s_add_u32 m0, s61, 4096\n\t"
    "v_permlane16_swap_b32 v168, v172\n\t"
    "v_cvt_pk_bf16_f32 v173, v150, v151\n\t"
    "v_cvt_pk_bf16_f32 v170, v152, v153\n\t"
    "v_mfma_f32_32x32x16_bf16 v[128:143], a[232:235], a[160:163], v[128:143]\n\t"
    "buffer_load_dwordx4 v103, s[64:67], s72 offen offset:0 sc1 lds\n\t"
    "v_cvt_pk_bf16_f32 v174, v156, v157\n\t"
    "v_permlane16_swap_b32 v169, v173\n\t"
    "v_fma_f32 v61, v61, s62, v63\n\t"
    "v_mfma_f32_32x32x16_bf16 v[128:143], a[236:239], a[164:167], v[128:143]\n\t"
    "buffer_load_dwordx4 v103, s[64:67], s72 offen offset:1024 sc1 lds\n\t"
    "v_cvt_pk_bf16_f32 v171, v154, v155\n\t"
    "v_cvt_pk_bf16_f32 v175, v158, v159\n\t"
    "v_permlane16_swap_b32 v170, v174\n\t"
    "v_mfma_f32_32x32x16_bf16 v[128:143], a[240:243], a[168:171], v[128:143]\n\t"
    "buffer_load_dwordx4 v104, s[64:67], s72 offen offset:2048 sc1 lds\n\t"
    "v_permlane16_swap_b32 v171, v175\n\t"
    "v_mul_f32 v60, v60, v20\n\t"
    "v_mul_f32 v61, v61, v21\n\t"
    "v_mfma_f32_32x32x16_bf16 v[128:143], a[244:247], a[172:175], v[128:143]\n\t"
    "buffer_load_dwordx4 v104, s[64:67], s72 offen offset:3072 sc1 lds\n\t"
    "s_add_u32 m0, s61, 8192\n\t"
    "v_exp_f32 v60, v60\n\t"
    "v_mfma_f32_32x32x16_bf16 v[128:143], a[248:251], a[176:179], v[128:143]\n\t"
    "buffer_load_dwordx4 v105, s[64:67], s72 offen offset:0 sc1 lds\n\t"
    "v_exp_f32 v61, v61\n\t"
    "v_add_f32 v60, 1.0, v60\n\t"
    "v_add_f32 v61, 1.0, v61\n\t"
    "v_mfma_f32_16x16x32_bf16 a[64:67], v[176:179], v[168:171], a[64:67]\n\t"
    "v_rcp_f32 v60, v60\n\t"
    "v_mfma_f32_16x16x32_bf16 a[72:75], v[176:179], v[172:175], a[72:75]\n\t"
    "v_rcp_f32 v61, v61\n\t"
    "v_mfma_f32_16x16x32_bf16 a[68:71], v[180:183], v[168:171], a[68:71]\n\t"
    "ds_read_b64_tr_b16 v[176:177], v98 offset:36864\n\t"
    "ds_read_b64_tr_b16 v[178:179], v98 offset:37120\n\t"
    "v_mfma_f32_16x16x32_bf16 a[76:79], v[180:183], v[172:175], a[76:79]\n\t"
    "v_mul_f32 v20, v20, v60\n\t"
    "v_mul_f32 v21, v21, v61\n\t"
    "v_mfma_f32_16x16x32_bf16 a[80:83], v[184:187], v[168:171], a[80:83]\n\t"
    "ds_read_b64_tr_b16 v[180:181], v99 offset:37888\n\t"
    "ds_read_b64_tr_b16 v[182:183], v99 offset:38144\n\t"
    "v_mfma_f32_16x16x32_bf16 a[88:91], v[184:187], v[172:175], a[88:91]\n\t"
    "v_mul_f32 v60, v22, v22\n\t"
    "v_mul_f32 v61, v23, v23\n\t"
    "v_mfma_f32_16x16x32_bf16 a[84:87], v[188:191], v[168:171], a[84:87]\n\t"
    "v_exp_f32 v128, v128\n\t"
    "v_mfma_f32_16x16x32_bf16 a[92:95], v[188:191], v[172:175], a[92:95]\n\t"
    "v_exp_f32 v129, v129\n\t"
    "v_mfma_f32_16x16x32_bf16 a[96:99], v[192:195], v[168:171], a[96:99]\n\t"
    "v_exp_f32 v132, v132\n\t"
    "v_mfma_f32_16x16x32_bf16 a[104:107], v[192:195], v[172:175], a[104:107]\n\t"
    "v_exp_f32 v133, v133\n\t"
    "v_mfma_f32_16x16x32_bf16 a[100:103], v[196:199], v[168:171], a[100:103]\n\t"
    "v_exp_f32 v130, v130\n\t"
    "v_mfma_f32_16x16x32_bf16 a[108:111], v[196:199], v[172:175], a[108:111]\n\t"
    "v_exp_f32 v131, v131\n\t"
    "v_mfma_f32_16x16x32_bf16 a[112:115], v[200:203], v[168:171], a[112:115]\n\t"
    "v_exp_f32 v134, v134\n\t"
    "v_mfma_f32_16x16x32_bf16 a[120:123], v[200:203], v[172:175], a[120:123]\n\t"
    "v_exp_f32 v135, v135\n\t"
    "v_mfma_f32_16x16x32_bf16 a[116:119], v[204:207], v[168:171], a[116:119]\n\t"
    "v_exp_f32 v136, v136\n\t"
    "v_mfma_f32_16x16x32_bf16 a[124:127], v[204:207], v[172:175], a[124:127]\n\t"
    "v_exp_f32 v137, v137\n\t"
    "v_mfma_f32_16x16x32_bf16 a[136:139], v[208:211], v[168:171], a[136:139]\n\t"
    "v_exp_f32 v140, v140\n\t"
    "v_mfma_f32_16x16x32_bf16 a[140:143], v[208:211], v[172:175], a[140:143]\n\t"
    "v_exp_f32 v141, v141\n\t"
    "v_mfma_f32_32x32x16_bf16 v[144:159], a[216:219], a[180:183], v[80:95]\n\t"
    "ds_read_b64_tr_b16 v[184:185], v98 offset:38912\n\t"
    "ds_read_b64_tr_b16 v[186:187], v98 offset:39168\n\t"
    "v_exp_f32 v138, v138\n\t"
    "v_exp_f32 v139, v139\n\t"
    "v_mfma_f32_32x32x16_bf16 v[144:159], a[220:223], a[184:187], v[144:159]\n\t"
    "ds_read_b64_tr_b16 v[188:189], v99 offset:39936\n\t"
    "ds_read_b64_tr_b16 v[190:191], v99 offset:40192\n\t"
    "v_cvt_pk_bf16_f32 v160, v128, v129\n\t"
    "v_cvt_pk_bf16_f32 v164, v132, v133\n\t"
    "v_exp_f32_e64 v142, v142\n\t"
    "v_mfma_f32_32x32x16_bf16 v[144:159], a[224:227], a[188:191], v[144:159]\n\t"
    "ds_read_b64_tr_b16 v[192:193], v98 offset:40960\n\t"
    "ds_read_b64_tr_b16 v[194:195], v98 offset:41216\n\t"
    "v_exp_f32_e64 v143, v143\n\t"
    "v_cvt_pk_bf16_f32 v161, v130, v131\n\t"
    "v_fma_f32 v60, v60, s62, v63\n\t"
    "v_mfma_f32_32x32x16_bf16 v[144:159], a[228:231], a[192:195], v[144:159]\n\t"
    "ds_read_b64_tr_b16 v[196:197], v99 offset:41984\n\t"
    "ds_read_b64_tr_b16 v[198:199], v99 offset:42240\n\t"
    "v_permlane16_swap_b32 v160, v164\n\t"
    "v_cvt_pk_bf16_f32 v165, v134, v135\n\t"
    "v_cvt_pk_bf16_f32 v162, v136, v137\n\t"
    "v_mfma_f32_32x32x16_bf16 v[144:159], a[232:235], a[196:199], v[144:159]\n\t"
    "ds_read_b64_tr_b16 v[200:201], v98 offset:43008\n\t"
    "ds_read_b64_tr_b16 v[202:203], v98 offset:43264\n\t"
    "v_cvt_pk_bf16_f32 v166, v140, v141\n\t"
    "v_permlane16_swap_b32 v161, v165\n\t"
    "v_fma_f32 v61, v61, s62, v63\n\t"
    "v_mfma_f32_32x32x16_bf16 v[144:159], a[236:239], a[200:203], v[144:159]\n\t"
    "ds_read_b64_tr_b16 v[204:205], v99 offset:44032\n\t"
    "ds_read_b64_tr_b16 v[206:207], v99 offset:44288\n\t"
    "v_cvt_pk_bf16_f32 v163, v138, v139\n\t"
    "v_cvt_pk_bf16_f32 v167, v142, v143\n\t"
    "v_permlane16_swap_b32 v162, v166\n\t"
    "v_mfma_f32_32x32x16_bf16 v[144:159], a[240:243], a[204:207], v[144:159]\n\t"
    "ds_read_b64_tr_b16 v[208:209], v98 offset:45056\n\t"
    "ds_read_b64_tr_b16 v[210:211], v98 offset:45312\n\t"
    "v_permlane16_swap_b32 v163, v167\n\t"
    "v_mul_f32 v60, v60, v22\n\t"
    "v_mul_f32_e64 v61, v61, v23\n\t"
    "v_mfma_f32_32x32x16_bf16 v[144:159], a[244:247], a[208:211], v[144:159]\n\t"
    "s_waitcnt lgkmcnt(14)\n\t"
    "ds_write_b64 v218, v[42:43] offset:256\n\t"
    "ds_write_b64 v218, v[44:45] offset:512\n\t"
    "ds_write_b64 v218, v[46:47] offset:768\n\t"
    "v_mfma_f32_32x32x16_bf16 v[144:159], a[248:251], a[212:215], v[144:159]\n\t"
    "s_waitcnt vmcnt(9)\n\t"
    "s_waitcnt lgkmcnt(3)\n\t"
    "ds_write_b64 v219, v[48:49] offset:1024\n\t"
    "ds_read_b128 v[0:3], v118 offset:0\n\t"
    "ds_read_b128 v[4:7], v118 offset:1024\n\t"
    "ds_read_b128 v[8:11], v118 offset:1408\n\t"
    "v_mfma_f32_16x16x32_bf16 a[0:3], v[176:179], v[160:163], a[0:3]\n\t"
    "ds_read_b128 a[216:219], v246 offset:0\n\t"
    "ds_read_b128 a[220:223], v247 offset:1024\n\t"
    "v_mfma_f32_16x16x32_bf16 a[8:11], v[176:179], v[164:167], a[8:11]\n\t"
    "ds_read_b128 a[224:227], v246 offset:2048\n\t"
    "ds_read_b128 a[228:231], v247 offset:3072\n\t"
    "v_mfma_f32_16x16x32_bf16 a[4:7], v[180:183], v[160:163], a[4:7]\n\t"
    "ds_read_b128 a[232:235], v246 offset:4096\n\t"
    "ds_read_b128 a[236:239], v247 offset:5120\n\t"
    "v_mfma_f32_16x16x32_bf16 a[12:15], v[180:183], v[164:167], a[12:15]\n\t"
    "ds_read_b128 a[240:243], v246 offset:6144\n\t"
    "ds_read_b128 a[244:247], v247 offset:7168\n\t"
    "v_mfma_f32_16x16x32_bf16 a[16:19], v[184:187], v[160:163], a[16:19]\n\t"
    "ds_read_b128 a[248:251], v246 offset:8192\n\t"
    "v_mfma_f32_16x16x32_bf16 a[24:27], v[184:187], v[164:167], a[24:27]\n\t"
    "v_exp_f32 v60, v60\n\t"
    "v_mfma_f32_16x16x32_bf16 a[20:23], v[188:191], v[160:163], a[20:23]\n\t"
    "v_exp_f32 v144, v144\n\t"
    "v_mfma_f32_16x16x32_bf16 a[28:31], v[188:191], v[164:167], a[28:31]\n\t"
    "v_exp_f32 v145, v145\n\t"
    "v_mfma_f32_16x16x32_bf16 a[32:35], v[192:195], v[160:163], a[32:35]\n\t"
    "v_exp_f32 v148, v148\n\t"
    "v_mfma_f32_16x16x32_bf16 a[40:43], v[192:195], v[164:167], a[40:43]\n\t"
    "v_exp_f32 v149, v149\n\t"
    "v_mfma_f32_16x16x32_bf16 a[36:39], v[196:199], v[160:163], a[36:39]\n\t"
    "v_exp_f32 v146, v146\n\t"
    "v_mfma_f32_16x16x32_bf16 a[44:47], v[196:199], v[164:167], a[44:47]\n\t"
    "v_exp_f32 v147, v147\n\t"
    "v_mfma_f32_16x16x32_bf16 a[48:51], v[200:203], v[160:163], a[48:51]\n\t"
    "v_exp_f32 v150, v150\n\t"
    "v_mfma_f32_16x16x32_bf16 a[56:59], v[200:203], v[164:167], a[56:59]\n\t"
    "v_exp_f32 v151, v151\n\t"
    "v_mfma_f32_16x16x32_bf16 a[52:55], v[204:207], v[160:163], a[52:55]\n\t"
    "v_exp_f32 v152, v152\n\t"
    "v_mfma_f32_16x16x32_bf16 a[60:63], v[204:207], v[164:167], a[60:63]\n\t"
    "s_mov_b32 s72, 92160\n\t"
    "v_exp_f32_e64 v153, v153\n\t"
    "v_mfma_f32_16x16x32_bf16 a[128:131], v[208:211], v[160:163], a[128:131]\n\t"
    "s_add_u32 m0, s61, 36864\n\t"
    "v_exp_f32 v156, v156\n\t"
    "v_mfma_f32_16x16x32_bf16 a[132:135], v[208:211], v[164:167], a[132:135]\n\t"
    "s_waitcnt lgkmcnt(0)\n\t"
    "v_exp_f32 v157, v157\n\t"
    "v_mfma_f32_32x32x16_bf16 v[128:143], a[216:219], a[144:147], v[64:79]\n\t"
    "buffer_load_dwordx4 v101, s[64:67], s72 offen offset:0 sc1 lds\n\t"
    "v_exp_f32 v154, v154\n\t"
    "v_exp_f32_e64 v155, v155\n\t"
    "v_mfma_f32_32x32x16_bf16 v[128:143], a[220:223], a[148:151], v[128:143]\n\t"
    "buffer_load_dwordx4 v101, s[64:67], s72 offen offset:1024 sc1 lds\n\t"
    "v_cvt_pk_bf16_f32 v168, v144, v145\n\t"
    "v_cvt_pk_bf16_f32 v172, v148, v149\n\t"
    "v_exp_f32 v158, v158\n\t"
    "v_mfma_f32_32x32x16_bf16 v[128:143], a[224:227], a[152:155], v[128:143]\n\t"
    "buffer_load_dwordx4 v102, s[64:67], s72 offen offset:2048 sc1 lds\n\t"
    "v_exp_f32 v159, v159\n\t"
    "v_cvt_pk_bf16_f32 v169, v146, v147\n\t"
    "v_mfma_f32_32x32x16_bf16 v[128:143], a[228:231], a[156:159], v[128:143]\n\t"
    "buffer_load_dwordx4 v102, s[64:67], s72 offen offset:3072 sc1 lds\n\t"
    "s_add_u32 m0, s61, 40960\n\t"
    "v_permlane16_swap_b32 v168, v172\n\t"
    "v_cvt_pk_bf16_f32 v173, v150, v151\n\t"
    "v_cvt_pk_bf16_f32 v170, v152, v153\n\t"
    "v_mfma_f32_32x32x16_bf16 v[128:143], a[232:235], a[160:163], v[128:143]\n\t"
    "buffer_load_dwordx4 v103, s[64:67], s72 offen offset:0 sc1 lds\n\t"
    "v_cvt_pk_bf16_f32 v174, v156, v157\n\t"
    "v_permlane16_swap_b32 v169, v173\n\t"
    "v_mfma_f32_32x32x16_bf16 v[128:143], a[236:239], a[164:167], v[128:143]\n\t"
    "buffer_load_dwordx4 v103, s[64:67], s72 offen offset:1024 sc1 lds\n\t"
    "v_cvt_pk_bf16_f32 v171, v154, v155\n\t"
    "v_cvt_pk_bf16_f32 v175, v158, v159\n\t"
    "v_permlane16_swap_b32 v170, v174\n\t"
    "v_mfma_f32_32x32x16_bf16 v[128:143], a[240:243], a[168:171], v[128:143]\n\t"
    "buffer_load_dwordx4 v104, s[64:67], s72 offen offset:2048 sc1 lds\n\t"
    "v_permlane16_swap_b32 v171, v175\n\t"
    "v_exp_f32_e64 v61, v61\n\t"
    "v_mfma_f32_32x32x16_bf16 v[128:143], a[244:247], a[172:175], v[128:143]\n\t"
    "buffer_load_dwordx4 v104, s[64:67], s72 offen offset:3072 sc1 lds\n\t"
    "s_add_u32 m0, s61, 45056\n\t"
    "v_add_f32 v60, 1.0, v60\n\t"
    "v_add_f32 v61, 1.0, v61\n\t"
    "v_mfma_f32_32x32x16_bf16 v[128:143], a[248:251], a[176:179], v[128:143]\n\t"
    "buffer_load_dwordx4 v105, s[64:67], s72 offen offset:0 sc1 lds\n\t"
    "v_rcp_f32 v60, v60\n\t"
    "v_rcp_f32 v61, v61\n\t"
    "v_mfma_f32_16x16x32_bf16 a[64:67], v[176:179], v[168:171], a[64:67]\n\t"
    "v_mul_f32 v22, v22, v60\n\t"
    "v_mul_f32 v23, v23, v61\n\t"
    "v_mfma_f32_16x16x32_bf16 a[72:75], v[176:179], v[172:175], a[72:75]\n\t"
    "buffer_store_dwordx4 v[0:3], v118, s[80:83], s52 offen offset:0 sc1\n\t"
    "v_mfma_f32_16x16x32_bf16 a[68:71], v[180:183], v[168:171], a[68:71]\n\t"
    "ds_read_b64_tr_b16 v[176:177], v248 offset:0\n\t"
    "ds_read_b64_tr_b16 v[178:179], v248 offset:256\n\t"
    "v_mfma_f32_16x16x32_bf16 a[76:79], v[180:183], v[172:175], a[76:79]\n\t"
    "v_mul_f32 v60, v24, v24\n\t"
    "v_mul_f32 v61, v25, v25\n\t"
    "v_mfma_f32_16x16x32_bf16 a[80:83], v[184:187], v[168:171], a[80:83]\n\t"
    "ds_read_b64_tr_b16 v[180:181], v249 offset:1024\n\t"
    "ds_read_b64_tr_b16 v[182:183], v249 offset:1280\n\t"
    "v_mfma_f32_16x16x32_bf16 a[88:91], v[184:187], v[172:175], a[88:91]\n\t"
    "v_fma_f32 v60, v60, s62, v63\n\t"
    "v_fma_f32 v61, v61, s62, v63\n\t"
    "v_mfma_f32_16x16x32_bf16 a[84:87], v[188:191], v[168:171], a[84:87]\n\t"
    "v_exp_f32 v128, v128\n\t"
    "v_mfma_f32_16x16x32_bf16 a[92:95], v[188:191], v[172:175], a[92:95]\n\t"
    "v_exp_f32 v129, v129\n\t"
    "v_mfma_f32_16x16x32_bf16 a[96:99], v[192:195], v[168:171], a[96:99]\n\t"
    "v_exp_f32 v132, v132\n\t"
    "v_mfma_f32_16x16x32_bf16 a[104:107], v[192:195], v[172:175], a[104:107]\n\t"
    "v_exp_f32 v133, v133\n\t"
    "v_mfma_f32_16x16x32_bf16 a[100:103], v[196:199], v[168:171], a[100:103]\n\t"
    "v_exp_f32 v130, v130\n\t"
    "v_mfma_f32_16x16x32_bf16 a[108:111], v[196:199], v[172:175], a[108:111]\n\t"
    "v_exp_f32 v131, v131\n\t"
    "v_mfma_f32_16x16x32_bf16 a[112:115], v[200:203], v[168:171], a[112:115]\n\t"
    "v_exp_f32 v134, v134\n\t"
    "v_mfma_f32_16x16x32_bf16 a[120:123], v[200:203], v[172:175], a[120:123]\n\t"
    "v_exp_f32 v135, v135\n\t"
    "v_mfma_f32_16x16x32_bf16 a[116:119], v[204:207], v[168:171], a[116:119]\n\t"
    "v_exp_f32 v136, v136\n\t"
    "v_mfma_f32_16x16x32_bf16 a[124:127], v[204:207], v[172:175], a[124:127]\n\t"
    "v_exp_f32 v137, v137\n\t"
    "v_mfma_f32_16x16x32_bf16 a[136:139], v[208:211], v[168:171], a[136:139]\n\t"
    "v_exp_f32 v140, v140\n\t"
    "v_mfma_f32_16x16x32_bf16 a[140:143], v[208:211], v[172:175], a[140:143]\n\t"
    "v_exp_f32 v141, v141\n\t"
    "v_mfma_f32_32x32x16_bf16 v[144:159], a[216:219], a[180:183], v[80:95]\n\t"
    "ds_read_b64_tr_b16 v[184:185], v248 offset:2048\n\t"
    "ds_read_b64_tr_b16 v[186:187], v248 offset:2304\n\t"
    "v_exp_f32 v138, v138\n\t"
    "v_exp_f32 v139, v139\n\t"
    "v_mfma_f32_32x32x16_bf16 v[144:159], a[220:223], a[184:187], v[144:159]\n\t"
    "ds_read_b64_tr_b16 v[188:189], v249 offset:3072\n\t"
    "ds_read_b64_tr_b16 v[190:191], v249 offset:3328\n\t"
    "v_cvt_pk_bf16_f32 v160, v128, v129\n\t"
    "v_cvt_pk_bf16_f32 v164, v132, v133\n\t"
    "v_exp_f32_e64 v142, v142\n\t"
    "v_mfma_f32_32x32x16_bf16 v[144:159], a[224:227], a[188:191], v[144:159]\n\t"
    "ds_read_b64_tr_b16 v[192:193], v248 offset:4096\n\t"
    "ds_read_b64_tr_b16 v[194:195], v248 offset:4352\n\t"
    "v_exp_f32 v143, v143\n\t"
    "v_cvt_pk_bf16_f32 v161, v130, v131\n\t"
    "v_mul_f32 v60, v60, v24\n\t"
    "v_mfma_f32_32x32x16_bf16 v[144:159], a[228:231], a[192:195], v[144:159]\n\t"
    "ds_read_b64_tr_b16 v[196:197], v249 offset:5120\n\t"
    "ds_read_b64_tr_b16 v[198:199], v249 offset:5376\n\t"
    "v_permlane16_swap_b32 v160, v164\n\t"
    "v_cvt_pk_bf16_f32 v165, v134, v135\n\t"
    "v_cvt_pk_bf16_f32 v162, v136, v137\n\t"
    "v_mfma_f32_32x32x16_bf16 v[144:159], a[232:235], a[196:199], v[144:159]\n\t"
    "ds_read_b64_tr_b16 v[200:201], v248 offset:6144\n\t"
    "ds_read_b64_tr_b16 v[202:203], v248 offset:6400\n\t"
    "v_cvt_pk_bf16_f32 v166, v140, v141\n\t"
    "v_permlane16_swap_b32 v161, v165\n\t"
    "v_mul_f32_e64 v61, v61, v25\n\t"
    "v_mfma_f32_32x32x16_bf16 v[144:159], a[236:239], a[200:203], v[144:159]\n\t"
    "ds_read_b64_tr_b16 v[204:205], v249 offset:7168\n\t"
    "ds_read_b64_tr_b16 v[206:207], v249 offset:7424\n\t"
    "v_cvt_pk_bf16_f32 v163, v138, v139\n\t"
    "v_cvt_pk_bf16_f32 v167, v142, v143\n\t"
    "v_permlane16_swap_b32 v162, v166\n\t"
    "v_mfma_f32_32x32x16_bf16 v[144:159], a[240:243], a[204:207], v[144:159]\n\t"
    "ds_read_b64_tr_b16 v[208:209], v248 offset:8192\n\t"
    "ds_read_b64_tr_b16 v[210:211], v248 offset:8448\n\t"
    "v_permlane16_swap_b32 v163, v167\n\t"
    "v_exp_f32 v60, v60\n\t"
    "v_mfma_f32_32x32x16_bf16 v[144:159], a[244:247], a[208:211], v[144:159]\n\t"
    "buffer_store_dwordx4 v[4:7], v118, s[80:83], s52 offen offset:1024 sc1\n\t"
    "v_exp_f32 v61, v61\n\t"
    "v_add_f32 v60, 1.0, v60\n\t"
    "v_add_f32 v61, 1.0, v61\n\t"
    "v_mfma_f32_32x32x16_bf16 v[144:159], a[248:251], a[212:215], v[144:159]\n\t"
    "s_waitcnt vmcnt(11)\n\t"
    "s_waitcnt lgkmcnt(0)\n\t"
    "v_rcp_f32 v60, v60\n\t"
    "v_rcp_f32 v61, v61\n\t"
    "v_mul_f32_e64 v24, v24, v60\n\t"
    "v_mfma_f32_16x16x32_bf16 a[0:3], v[176:179], v[160:163], a[0:3]\n\t"
    "ds_read_b128 a[216:219], v96 offset:0\n\t"
    "ds_read_b128 a[220:223], v97 offset:1024\n\t"
    "v_mfma_f32_16x16x32_bf16 a[8:11], v[176:179], v[164:167], a[8:11]\n\t"
    "ds_read_b128 a[224:227], v96 offset:2048\n\t"
    "ds_read_b128 a[228:231], v97 offset:3072\n\t"
    "v_mfma_f32_16x16x32_bf16 a[4:7], v[180:183], v[160:163], a[4:7]\n\t"
    "ds_read_b128 a[232:235], v96 offset:4096\n\t"
    "ds_read_b128 a[236:239], v97 offset:5120\n\t"
    "v_mfma_f32_16x16x32_bf16 a[12:15], v[180:183], v[164:167], a[12:15]\n\t"
    "ds_read_b128 a[240:243], v96 offset:6144\n\t"
    "ds_read_b128 a[244:247], v97 offset:7168\n\t"
    "v_mfma_f32_16x16x32_bf16 a[16:19], v[184:187], v[160:163], a[16:19]\n\t"
    "ds_read_b128 a[248:251], v96 offset:8192\n\t"
    "v_mul_f32 v25, v25, v61\n\t"
    "v_mfma_f32_16x16x32_bf16 a[24:27], v[184:187], v[164:167], a[24:27]\n\t"
    "v_mul_f32 v60, v26, v26\n\t"
    "v_mul_f32 v61, v27, v27\n\t"
    "v_mfma_f32_16x16x32_bf16 a[20:23], v[188:191], v[160:163], a[20:23]\n\t"
    "v_exp_f32 v144, v144\n\t"
    "v_mfma_f32_16x16x32_bf16 a[28:31], v[188:191], v[164:167], a[28:31]\n\t"
    "v_exp_f32 v145, v145\n\t"
    "v_mfma_f32_16x16x32_bf16 a[32:35], v[192:195], v[160:163], a[32:35]\n\t"
    "v_exp_f32 v148, v148\n\t"
    "v_mfma_f32_16x16x32_bf16 a[40:43], v[192:195], v[164:167], a[40:43]\n\t"
    "v_exp_f32 v149, v149\n\t"
    "v_mfma_f32_16x16x32_bf16 a[36:39], v[196:199], v[160:163], a[36:39]\n\t"
    "v_exp_f32 v146, v146\n\t"
    "v_mfma_f32_16x16x32_bf16 a[44:47], v[196:199], v[164:167], a[44:47]\n\t"
    "v_exp_f32 v147, v147\n\t"
    "v_mfma_f32_16x16x32_bf16 a[48:51], v[200:203], v[160:163], a[48:51]\n\t"
    "v_exp_f32 v150, v150\n\t"
    "v_mfma_f32_16x16x32_bf16 a[56:59], v[200:203], v[164:167], a[56:59]\n\t"
    "v_exp_f32 v151, v151\n\t"
    "v_mfma_f32_16x16x32_bf16 a[52:55], v[204:207], v[160:163], a[52:55]\n\t"
    "v_exp_f32 v152, v152\n\t"
    "v_mfma_f32_16x16x32_bf16 a[60:63], v[204:207], v[164:167], a[60:63]\n\t"
    "s_mov_b32 s72, 101376\n\t"
    "v_exp_f32_e64 v153, v153\n\t"
    "v_mfma_f32_16x16x32_bf16 a[128:131], v[208:211], v[160:163], a[128:131]\n\t"
    "s_add_u32 m0, s61, 73728\n\t"
    "v_exp_f32 v156, v156\n\t"
    "v_mfma_f32_16x16x32_bf16 a[132:135], v[208:211], v[164:167], a[132:135]\n\t"
    "s_waitcnt lgkmcnt(0)\n\t"
    "v_exp_f32 v157, v157\n\t"
    "v_mfma_f32_32x32x16_bf16 v[128:143], a[216:219], a[144:147], v[64:79]\n\t"
    "buffer_load_dwordx4 v101, s[64:67], s72 offen offset:0 sc1 lds\n\t"
    "v_exp_f32 v154, v154\n\t"
    "v_exp_f32_e64 v155, v155\n\t"
    "v_mfma_f32_32x32x16_bf16 v[128:143], a[220:223], a[148:151], v[128:143]\n\t"
    "buffer_load_dwordx4 v101, s[64:67], s72 offen offset:1024 sc1 lds\n\t"
    "v_cvt_pk_bf16_f32 v168, v144, v145\n\t"
    "v_cvt_pk_bf16_f32 v172, v148, v149\n\t"
    "v_exp_f32 v158, v158\n\t"
    "v_mfma_f32_32x32x16_bf16 v[128:143], a[224:227], a[152:155], v[128:143]\n\t"
    "buffer_load_dwordx4 v102, s[64:67], s72 offen offset:2048 sc1 lds\n\t"
    "v_exp_f32 v159, v159\n\t"
    "v_cvt_pk_bf16_f32 v169, v146, v147\n\t"
    "v_fma_f32 v60, v60, s62, v63\n\t"
    "v_mfma_f32_32x32x16_bf16 v[128:143], a[228:231], a[156:159], v[128:143]\n\t"
    "buffer_load_dwordx4 v102, s[64:67], s72 offen offset:3072 sc1 lds\n\t"
    "s_add_u32 m0, s61, 77824\n\t"
    "v_permlane16_swap_b32 v168, v172\n\t"
    "v_cvt_pk_bf16_f32 v173, v150, v151\n\t"
    "v_cvt_pk_bf16_f32 v170, v152, v153\n\t"
    "v_mfma_f32_32x32x16_bf16 v[128:143], a[232:235], a[160:163], v[128:143]\n\t"
    "buffer_load_dwordx4 v103, s[64:67], s72 offen offset:0 sc1 lds\n\t"
    "v_cvt_pk_bf16_f32 v174, v156, v157\n\t"
    "v_permlane16_swap_b32 v169, v173\n\t"
    "v_fma_f32 v61, v61, s62, v63\n\t"
    "v_mfma_f32_32x32x16_bf16 v[128:143], a[236:239], a[164:167], v[128:143]\n\t"
    "buffer_load_dwordx4 v103, s[64:67], s72 offen offset:1024 sc1 lds\n\t"
    "v_cvt_pk_bf16_f32 v171, v154, v155\n\t"
    "v_cvt_pk_bf16_f32 v175, v158, v159\n\t"
    "v_permlane16_swap_b32 v170, v174\n\t"
    "v_mfma_f32_32x32x16_bf16 v[128:143], a[240:243], a[168:171], v[128:143]\n\t"
    "buffer_load_dwordx4 v104, s[64:67], s72 offen offset:2048 sc1 lds\n\t"
    "v_permlane16_swap_b32 v171, v175\n\t"
    "v_mul_f32 v60, v60, v26\n\t"
    "v_mul_f32 v61, v61, v27\n\t"
    "v_mfma_f32_32x32x16_bf16 v[128:143], a[244:247], a[172:175], v[128:143]\n\t"
    "buffer_load_dwordx4 v104, s[64:67], s72 offen offset:3072 sc1 lds\n\t"
    "s_add_u32 m0, s61, 81920\n\t"
    "v_exp_f32 v60, v60\n\t"
    "v_mfma_f32_32x32x16_bf16 v[128:143], a[248:251], a[176:179], v[128:143]\n\t"
    "buffer_load_dwordx4 v105, s[64:67], s72 offen offset:0 sc1 lds\n\t"
    "v_exp_f32 v61, v61\n\t"
    "v_add_f32 v60, 1.0, v60\n\t"
    "v_add_f32 v61, 1.0, v61\n\t"
    "v_mfma_f32_16x16x32_bf16 a[64:67], v[176:179], v[168:171], a[64:67]\n\t"
    "v_rcp_f32_e64 v60, v60\n\t"
    "v_mfma_f32_16x16x32_bf16 a[72:75], v[176:179], v[172:175], a[72:75]\n\t"
    "buffer_store_dwordx4 v[8:11], v118, s[80:83], s52 offen offset:1408 sc1\n\t"
    "v_mfma_f32_16x16x32_bf16 a[68:71], v[180:183], v[168:171], a[68:71]\n\t"
    "ds_read_b64_tr_b16 v[176:177], v98 offset:0\n\t"
    "ds_read_b64_tr_b16 v[178:179], v98 offset:256\n\t"
    "v_mfma_f32_16x16x32_bf16 a[76:79], v[180:183], v[172:175], a[76:79]\n\t"
    "v_rcp_f32_e64 v61, v61\n\t"
    "v_mfma_f32_16x16x32_bf16 a[80:83], v[184:187], v[168:171], a[80:83]\n\t"
    "ds_read_b64_tr_b16 v[180:181], v99 offset:1024\n\t"
    "ds_read_b64_tr_b16 v[182:183], v99 offset:1280\n\t"
    "v_mfma_f32_16x16x32_bf16 a[88:91], v[184:187], v[172:175], a[88:91]\n\t"
    "v_mul_f32 v26, v26, v60\n\t"
    "v_mul_f32 v27, v27, v61\n\t"
    "v_mfma_f32_16x16x32_bf16 a[84:87], v[188:191], v[168:171], a[84:87]\n\t"
    "v_exp_f32 v128, v128\n\t"
    "v_mfma_f32_16x16x32_bf16 a[92:95], v[188:191], v[172:175], a[92:95]\n\t"
    "v_exp_f32 v129, v129\n\t"
    "v_mfma_f32_16x16x32_bf16 a[96:99], v[192:195], v[168:171], a[96:99]\n\t"
    "v_exp_f32 v132, v132\n\t"
    "v_mfma_f32_16x16x32_bf16 a[104:107], v[192:195], v[172:175], a[104:107]\n\t"
    "v_exp_f32 v133, v133\n\t"
    "v_mfma_f32_16x16x32_bf16 a[100:103], v[196:199], v[168:171], a[100:103]\n\t"
    "v_exp_f32 v130, v130\n\t"
    "v_mfma_f32_16x16x32_bf16 a[108:111], v[196:199], v[172:175], a[108:111]\n\t"
    "v_exp_f32 v131, v131\n\t"
    "v_mfma_f32_16x16x32_bf16 a[112:115], v[200:203], v[168:171], a[112:115]\n\t"
    "v_exp_f32 v134, v134\n\t"
    "v_mfma_f32_16x16x32_bf16 a[120:123], v[200:203], v[172:175], a[120:123]\n\t"
    "v_exp_f32 v135, v135\n\t"
    "v_mfma_f32_16x16x32_bf16 a[116:119], v[204:207], v[168:171], a[116:119]\n\t"
    "v_exp_f32 v136, v136\n\t"
    "v_mfma_f32_16x16x32_bf16 a[124:127], v[204:207], v[172:175], a[124:127]\n\t"
    "v_exp_f32 v137, v137\n\t"
    "v_mfma_f32_16x16x32_bf16 a[136:139], v[208:211], v[168:171], a[136:139]\n\t"
    "v_exp_f32 v140, v140\n\t"
    "v_mfma_f32_16x16x32_bf16 a[140:143], v[208:211], v[172:175], a[140:143]\n\t"
    "v_exp_f32 v141, v141\n\t"
    "v_mfma_f32_32x32x16_bf16 v[144:159], a[216:219], a[180:183], v[80:95]\n\t"
    "ds_read_b64_tr_b16 v[184:185], v98 offset:2048\n\t"
    "ds_read_b64_tr_b16 v[186:187], v98 offset:2304\n\t"
    "v_exp_f32 v138, v138\n\t"
    "v_exp_f32 v139, v139\n\t"
    "v_mfma_f32_32x32x16_bf16 v[144:159], a[220:223], a[184:187], v[144:159]\n\t"
    "ds_read_b64_tr_b16 v[188:189], v99 offset:3072\n\t"
    "ds_read_b64_tr_b16 v[190:191], v99 offset:3328\n\t"
    "v_cvt_pk_bf16_f32 v160, v128, v129\n\t"
    "v_cvt_pk_bf16_f32 v164, v132, v133\n\t"
    "v_exp_f32_e64 v142, v142\n\t"
    "v_mfma_f32_32x32x16_bf16 v[144:159], a[224:227], a[188:191], v[144:159]\n\t"
    "ds_read_b64_tr_b16 v[192:193], v98 offset:4096\n\t"
    "ds_read_b64_tr_b16 v[194:195], v98 offset:4352\n\t"
    "v_exp_f32 v143, v143\n\t"
    "v_cvt_pk_bf16_f32 v161, v130, v131\n\t"
    "v_mul_f32 v60, v28, v28\n\t"
    "v_mfma_f32_32x32x16_bf16 v[144:159], a[228:231], a[192:195], v[144:159]\n\t"
    "ds_read_b64_tr_b16 v[196:197], v99 offset:5120\n\t"
    "ds_read_b64_tr_b16 v[198:199], v99 offset:5376\n\t"
    "v_permlane16_swap_b32 v160, v164\n\t"
    "v_cvt_pk_bf16_f32 v165, v134, v135\n\t"
    "v_cvt_pk_bf16_f32 v162, v136, v137\n\t"
    "v_mfma_f32_32x32x16_bf16 v[144:159], a[232:235], a[196:199], v[144:159]\n\t"
    "ds_read_b64_tr_b16 v[200:201], v98 offset:6144\n\t"
    "ds_read_b64_tr_b16 v[202:203], v98 offset:6400\n\t"
    "v_cvt_pk_bf16_f32 v166, v140, v141\n\t"
    "v_permlane16_swap_b32 v161, v165\n\t"
    "v_mul_f32_e64 v61, v29, v29\n\t"
    "v_mfma_f32_32x32x16_bf16 v[144:159], a[236:239], a[200:203], v[144:159]\n\t"
    "ds_read_b64_tr_b16 v[204:205], v99 offset:7168\n\t"
    "ds_read_b64_tr_b16 v[206:207], v99 offset:7424\n\t"
    "v_cvt_pk_bf16_f32 v163, v138, v139\n\t"
    "v_cvt_pk_bf16_f32 v167, v142, v143\n\t"
    "v_permlane16_swap_b32 v162, v166\n\t"
    "v_mfma_f32_32x32x16_bf16 v[144:159], a[240:243], a[204:207], v[144:159]\n\t"
    "ds_read_b64_tr_b16 v[208:209], v98 offset:8192\n\t"
    "ds_read_b64_tr_b16 v[210:211], v98 offset:8448\n\t"
    "v_permlane16_swap_b32 v163, v167\n\t"
    "v_fma_f32 v60, v60, s62, v63\n\t"
    "v_fma_f32 v61, v61, s62, v63\n\t"
    "v_mfma_f32_32x32x16_bf16 v[144:159], a[244:247], a[208:211], v[144:159]\n\t"
    "v_mul_f32 v60, v60, v28\n\t"
    "v_mul_f32 v61, v61, v29\n\t"
    "v_exp_f32 v60, v60\n\t"
    "v_exp_f32 v61, v61\n\t"
    "v_mfma_f32_32x32x16_bf16 v[144:159], a[248:251], a[212:215], v[144:159]\n\t"
    "s_waitcnt vmcnt(12)\n\t"
    "s_waitcnt lgkmcnt(0)\n\t"
    "v_add_f32 v60, 1.0, v60\n\t"
    "v_add_f32 v61, 1.0, v61\n\t"
    "v_rcp_f32_e64 v60, v60\n\t"
    "v_mfma_f32_16x16x32_bf16 a[0:3], v[176:179], v[160:163], a[0:3]\n\t"
    "ds_read_b128 a[216:219], v96 offset:36864\n\t"
    "ds_read_b128 a[220:223], v97 offset:37888\n\t"
    "v_mfma_f32_16x16x32_bf16 a[8:11], v[176:179], v[164:167], a[8:11]\n\t"
    "ds_read_b128 a[224:227], v96 offset:38912\n\t"
    "ds_read_b128 a[228:231], v97 offset:39936\n\t"
    "v_mfma_f32_16x16x32_bf16 a[4:7], v[180:183], v[160:163], a[4:7]\n\t"
    "ds_read_b128 a[232:235], v96 offset:40960\n\t"
    "ds_read_b128 a[236:239], v97 offset:41984\n\t"
    "v_mfma_f32_16x16x32_bf16 a[12:15], v[180:183], v[164:167], a[12:15]\n\t"
    "ds_read_b128 a[240:243], v96 offset:43008\n\t"
    "ds_read_b128 a[244:247], v97 offset:44032\n\t"
    "v_mfma_f32_16x16x32_bf16 a[16:19], v[184:187], v[160:163], a[16:19]\n\t"
    "ds_read_b128 a[248:251], v96 offset:45056\n\t"
    "v_mfma_f32_16x16x32_bf16 a[24:27], v[184:187], v[164:167], a[24:27]\n\t"
    "v_rcp_f32 v61, v61\n\t"
    "v_mfma_f32_16x16x32_bf16 a[20:23], v[188:191], v[160:163], a[20:23]\n\t"
    "v_exp_f32 v144, v144\n\t"
    "v_mfma_f32_16x16x32_bf16 a[28:31], v[188:191], v[164:167], a[28:31]\n\t"
    "v_exp_f32 v145, v145\n\t"
    "v_mfma_f32_16x16x32_bf16 a[32:35], v[192:195], v[160:163], a[32:35]\n\t"
    "v_exp_f32 v148, v148\n\t"
    "v_mfma_f32_16x16x32_bf16 a[40:43], v[192:195], v[164:167], a[40:43]\n\t"
    "v_exp_f32 v149, v149\n\t"
    "v_mfma_f32_16x16x32_bf16 a[36:39], v[196:199], v[160:163], a[36:39]\n\t"
    "v_exp_f32 v146, v146\n\t"
    "v_mfma_f32_16x16x32_bf16 a[44:47], v[196:199], v[164:167], a[44:47]\n\t"
    "v_exp_f32 v147, v147\n\t"
    "v_mfma_f32_16x16x32_bf16 a[48:51], v[200:203], v[160:163], a[48:51]\n\t"
    "v_exp_f32 v150, v150\n\t"
    "v_mfma_f32_16x16x32_bf16 a[56:59], v[200:203], v[164:167], a[56:59]\n\t"
    "v_exp_f32 v151, v151\n\t"
    "v_mfma_f32_16x16x32_bf16 a[52:55], v[204:207], v[160:163], a[52:55]\n\t"
    "v_exp_f32 v152, v152\n\t"
    "v_mfma_f32_16x16x32_bf16 a[60:63], v[204:207], v[164:167], a[60:63]\n\t"
    "s_mov_b32 s72, 110592\n\t"
    "v_exp_f32 v153, v153\n\t"
    "v_mfma_f32_16x16x32_bf16 a[128:131], v[208:211], v[160:163], a[128:131]\n\t"
    "s_add_u32 m0, s61, 0\n\t"
    "v_exp_f32 v156, v156\n\t"
    "v_mfma_f32_16x16x32_bf16 a[132:135], v[208:211], v[164:167], a[132:135]\n\t"
    "s_waitcnt lgkmcnt(0)\n\t"
    "v_exp_f32 v157, v157\n\t"
    "v_mfma_f32_32x32x16_bf16 v[128:143], a[216:219], a[144:147], v[64:79]\n\t"
    "buffer_load_dwordx4 v101, s[64:67], s72 offen offset:0 sc1 lds\n\t"
    "v_exp_f32 v154, v154\n\t"
    "v_exp_f32_e64 v155, v155\n\t"
    "v_mfma_f32_32x32x16_bf16 v[128:143], a[220:223], a[148:151], v[128:143]\n\t"
    "buffer_load_dwordx4 v101, s[64:67], s72 offen offset:1024 sc1 lds\n\t"
    "v_cvt_pk_bf16_f32 v168, v144, v145\n\t"
    "v_cvt_pk_bf16_f32 v172, v148, v149\n\t"
    "v_exp_f32 v158, v158\n\t"
    "v_mfma_f32_32x32x16_bf16 v[128:143], a[224:227], a[152:155], v[128:143]\n\t"
    "buffer_load_dwordx4 v102, s[64:67], s72 offen offset:2048 sc1 lds\n\t"
    "v_exp_f32 v159, v159\n\t"
    "v_cvt_pk_bf16_f32 v169, v146, v147\n\t"
    "v_mul_f32_e64 v28, v28, v60\n\t"
    "v_mfma_f32_32x32x16_bf16 v[128:143], a[228:231], a[156:159], v[128:143]\n\t"
    "buffer_load_dwordx4 v102, s[64:67], s72 offen offset:3072 sc1 lds\n\t"
    "s_add_u32 m0, s61, 4096\n\t"
    "v_permlane16_swap_b32 v168, v172\n\t"
    "v_cvt_pk_bf16_f32 v173, v150, v151\n\t"
    "v_cvt_pk_bf16_f32 v170, v152, v153\n\t"
    "v_mfma_f32_32x32x16_bf16 v[128:143], a[232:235], a[160:163], v[128:143]\n\t"
    "buffer_load_dwordx4 v103, s[64:67], s72 offen offset:0 sc1 lds\n\t"
    "v_cvt_pk_bf16_f32 v174, v156, v157\n\t"
    "v_permlane16_swap_b32 v169, v173\n\t"
    "v_mul_f32_e64 v29, v29, v61\n\t"
    "v_mfma_f32_32x32x16_bf16 v[128:143], a[236:239], a[164:167], v[128:143]\n\t"
    "buffer_load_dwordx4 v103, s[64:67], s72 offen offset:1024 sc1 lds\n\t"
    "v_cvt_pk_bf16_f32 v171, v154, v155\n\t"
    "v_cvt_pk_bf16_f32 v175, v158, v159\n\t"
    "v_permlane16_swap_b32 v170, v174\n\t"
    "v_mfma_f32_32x32x16_bf16 v[128:143], a[240:243], a[168:171], v[128:143]\n\t"
    "buffer_load_dwordx4 v104, s[64:67], s72 offen offset:2048 sc1 lds\n\t"
    "v_permlane16_swap_b32 v171, v175\n\t"
    "v_mul_f32 v60, v30, v30\n\t"
    "v_mul_f32 v61, v31, v31\n\t"
    "v_mfma_f32_32x32x16_bf16 v[128:143], a[244:247], a[172:175], v[128:143]\n\t"
    "buffer_load_dwordx4 v104, s[64:67], s72 offen offset:3072 sc1 lds\n\t"
    "s_add_u32 m0, s61, 8192\n\t"
    "v_fma_f32 v60, v60, s62, v63\n\t"
    "v_fma_f32 v61, v61, s62, v63\n\t"
    "v_mul_f32 v60, v60, v30\n\t"
    "v_mfma_f32_32x32x16_bf16 v[128:143], a[248:251], a[176:179], v[128:143]\n\t"
    "buffer_load_dwordx4 v105, s[64:67], s72 offen offset:0 sc1 lds\n\t"
    "v_mul_f32 v61, v61, v31\n\t"
    "v_exp_f32 v60, v60\n\t"
    "v_mfma_f32_16x16x32_bf16 a[64:67], v[176:179], v[168:171], a[64:67]\n\t"
    "v_exp_f32 v61, v61\n\t"
    "v_mfma_f32_16x16x32_bf16 a[72:75], v[176:179], v[172:175], a[72:75]\n\t"
    "v_add_f32 v60, 1.0, v60\n\t"
    "v_add_f32 v61, 1.0, v61\n\t"
    "v_mfma_f32_16x16x32_bf16 a[68:71], v[180:183], v[168:171], a[68:71]\n\t"
    "ds_read_b64_tr_b16 v[176:177], v98 offset:36864\n\t"
    "ds_read_b64_tr_b16 v[178:179], v98 offset:37120\n\t"
    "v_mfma_f32_16x16x32_bf16 a[76:79], v[180:183], v[172:175], a[76:79]\n\t"
    "v_rcp_f32_e64 v60, v60\n\t"
    "v_mfma_f32_16x16x32_bf16 a[80:83], v[184:187], v[168:171], a[80:83]\n\t"
    "ds_read_b64_tr_b16 v[180:181], v99 offset:37888\n\t"
    "ds_read_b64_tr_b16 v[182:183], v99 offset:38144\n\t"
    "v_mfma_f32_16x16x32_bf16 a[88:91], v[184:187], v[172:175], a[88:91]\n\t"
    "v_rcp_f32 v61, v61\n\t"
    "v_mfma_f32_16x16x32_bf16 a[84:87], v[188:191], v[168:171], a[84:87]\n\t"
    "v_exp_f32 v128, v128\n\t"
    "v_mfma_f32_16x16x32_bf16 a[92:95], v[188:191], v[172:175], a[92:95]\n\t"
    "v_exp_f32 v129, v129\n\t"
    "v_mfma_f32_16x16x32_bf16 a[96:99], v[192:195], v[168:171], a[96:99]\n\t"
    "v_exp_f32 v132, v132\n\t"
    "v_mfma_f32_16x16x32_bf16 a[104:107], v[192:195], v[172:175], a[104:107]\n\t"
    "v_exp_f32 v133, v133\n\t"
    "v_mfma_f32_16x16x32_bf16 a[100:103], v[196:199], v[168:171], a[100:103]\n\t"
    "v_exp_f32 v130, v130\n\t"
    "v_mfma_f32_16x16x32_bf16 a[108:111], v[196:199], v[172:175], a[108:111]\n\t"
    "v_exp_f32 v131, v131\n\t"
    "v_mfma_f32_16x16x32_bf16 a[112:115], v[200:203], v[168:171], a[112:115]\n\t"
    "v_exp_f32 v134, v134\n\t"
    "v_mfma_f32_16x16x32_bf16 a[120:123], v[200:203], v[172:175], a[120:123]\n\t"
    "v_exp_f32 v135, v135\n\t"
    "v_mfma_f32_16x16x32_bf16 a[116:119], v[204:207], v[168:171], a[116:119]\n\t"
    "v_exp_f32 v136, v136\n\t"
    "v_mfma_f32_16x16x32_bf16 a[124:127], v[204:207], v[172:175], a[124:127]\n\t"
    "v_exp_f32 v137, v137\n\t"
    "v_mfma_f32_16x16x32_bf16 a[136:139], v[208:211], v[168:171], a[136:139]\n\t"
    "v_exp_f32 v140, v140\n\t"
    "v_mfma_f32_16x16x32_bf16 a[140:143], v[208:211], v[172:175], a[140:143]\n\t"
    "v_exp_f32_e64 v141, v141\n\t"
    "v_mfma_f32_32x32x16_bf16 v[144:159], a[216:219], a[180:183], v[80:95]\n\t"
    "ds_read_b64_tr_b16 v[184:185], v98 offset:38912\n\t"
    "ds_read_b64_tr_b16 v[186:187], v98 offset:39168\n\t"
    "v_exp_f32 v138, v138\n\t"
    "v_exp_f32 v139, v139\n\t"
    "v_mfma_f32_32x32x16_bf16 v[144:159], a[220:223], a[184:187], v[144:159]\n\t"
    "ds_read_b64_tr_b16 v[188:189], v99 offset:39936\n\t"
    "ds_read_b64_tr_b16 v[190:191], v99 offset:40192\n\t"
    "v_cvt_pk_bf16_f32 v160, v128, v129\n\t"
    "v_cvt_pk_bf16_f32 v164, v132, v133\n\t"
    "v_exp_f32_e64 v142, v142\n\t"
    "v_mfma_f32_32x32x16_bf16 v[144:159], a[224:227], a[188:191], v[144:159]\n\t"
    "ds_read_b64_tr_b16 v[192:193], v98 offset:40960\n\t"
    "ds_read_b64_tr_b16 v[194:195], v98 offset:41216\n\t"
    "v_exp_f32 v143, v143\n\t"
    "v_cvt_pk_bf16_f32 v161, v130, v131\n\t"
    "v_mul_f32 v30, v30, v60\n\t"
    "v_mfma_f32_32x32x16_bf16 v[144:159], a[228:231], a[192:195], v[144:159]\n\t"
    "ds_read_b64_tr_b16 v[196:197], v99 offset:41984\n\t"
    "ds_read_b64_tr_b16 v[198:199], v99 offset:42240\n\t"
    "v_permlane16_swap_b32 v160, v164\n\t"
    "v_cvt_pk_bf16_f32 v165, v134, v135\n\t"
    "v_cvt_pk_bf16_f32 v162, v136, v137\n\t"
    "v_mfma_f32_32x32x16_bf16 v[144:159], a[232:235], a[196:199], v[144:159]\n\t"
    "ds_read_b64_tr_b16 v[200:201], v98 offset:43008\n\t"
    "ds_read_b64_tr_b16 v[202:203], v98 offset:43264\n\t"
    "v_cvt_pk_bf16_f32 v166, v140, v141\n\t"
    "v_permlane16_swap_b32 v161, v165\n\t"
    "v_mul_f32_e64 v31, v31, v61\n\t"
    "v_mfma_f32_32x32x16_bf16 v[144:159], a[236:239], a[200:203], v[144:159]\n\t"
    "ds_read_b64_tr_b16 v[204:205], v99 offset:44032\n\t"
    "ds_read_b64_tr_b16 v[206:207], v99 offset:44288\n\t"
    "v_cvt_pk_bf16_f32 v163, v138, v139\n\t"
    "v_cvt_pk_bf16_f32 v167, v142, v143\n\t"
    "v_permlane16_swap_b32 v162, v166\n\t"
    "v_mfma_f32_32x32x16_bf16 v[144:159], a[240:243], a[204:207], v[144:159]\n\t"
    "ds_read_b64_tr_b16 v[208:209], v98 offset:45056\n\t"
    "ds_read_b64_tr_b16 v[210:211], v98 offset:45312\n\t"
    "v_permlane16_swap_b32 v163, v167\n\t"
    "v_cvt_pk_bf16_f32 v16, v16, v17\n\t"
    "v_cvt_pk_bf16_f32 v17, v18, v19\n\t"
    "v_mfma_f32_32x32x16_bf16 v[144:159], a[244:247], a[208:211], v[144:159]\n\t"
    "v_cvt_pk_bf16_f32 v18, v20, v21\n\t"
    "v_cvt_pk_bf16_f32 v19, v22, v23\n\t"
    "v_cvt_pk_bf16_f32 v20, v24, v25\n\t"
    "v_cvt_pk_bf16_f32 v21, v26, v27\n\t"
    "v_cvt_pk_bf16_f32 v22, v28, v29\n\t"
    "v_cvt_pk_bf16_f32 v23, v30, v31\n\t"
    "v_mfma_f32_32x32x16_bf16 v[144:159], a[248:251], a[212:215], v[144:159]\n\t"
    "s_waitcnt vmcnt(10)\n\t"
    "s_waitcnt lgkmcnt(0)\n\t"
    "ds_write_b64 v220, v[16:17] offset:0\n\t"
    "ds_write_b64 v220, v[18:19] offset:2432\n\t"
    "v_mfma_f32_16x16x32_bf16 a[0:3], v[176:179], v[160:163], a[0:3]\n\t"
    "ds_read_b128 a[216:219], v246 offset:0\n\t"
    "ds_read_b128 a[220:223], v247 offset:1024\n\t"
    "v_mfma_f32_16x16x32_bf16 a[8:11], v[176:179], v[164:167], a[8:11]\n\t"
    "ds_read_b128 a[224:227], v246 offset:2048\n\t"
    "ds_read_b128 a[228:231], v247 offset:3072\n\t"
    "v_mfma_f32_16x16x32_bf16 a[4:7], v[180:183], v[160:163], a[4:7]\n\t"
    "ds_read_b128 a[232:235], v246 offset:4096\n\t"
    "ds_read_b128 a[236:239], v247 offset:5120\n\t"
    "v_mfma_f32_16x16x32_bf16 a[12:15], v[180:183], v[164:167], a[12:15]\n\t"
    "ds_read_b128 a[240:243], v246 offset:6144\n\t"
    "ds_read_b128 a[244:247], v247 offset:7168\n\t"
    "v_mfma_f32_16x16x32_bf16 a[16:19], v[184:187], v[160:163], a[16:19]\n\t"
    "ds_read_b128 a[248:251], v246 offset:8192\n\t"
    "v_mfma_f32_16x16x32_bf16 a[24:27], v[184:187], v[164:167], a[24:27]\n\t"
    "ds_write_b64 v220, v[20:21] offset:4864\n\t"
    "v_mfma_f32_16x16x32_bf16 a[20:23], v[188:191], v[160:163], a[20:23]\n\t"
    "v_exp_f32 v144, v144\n\t"
    "v_mfma_f32_16x16x32_bf16 a[28:31], v[188:191], v[164:167], a[28:31]\n\t"
    "v_exp_f32 v145, v145\n\t"
    "v_mfma_f32_16x16x32_bf16 a[32:35], v[192:195], v[160:163], a[32:35]\n\t"
    "ds_write_b64 v220, v[22:23] offset:7296\n\t"
    "v_exp_f32_e64 v148, v148\n\t"
    "v_mfma_f32_16x16x32_bf16 a[40:43], v[192:195], v[164:167], a[40:43]\n\t"
    "s_add_u32 s73, s54, 4096\n\t"
    "v_exp_f32_e64 v149, v149\n\t"
    "v_mfma_f32_16x16x32_bf16 a[36:39], v[196:199], v[160:163], a[36:39]\n\t"
    "s_add_u32 s76, s54, 18432\n\t"
    "v_exp_f32_e64 v146, v146\n\t"
    "v_mfma_f32_16x16x32_bf16 a[44:47], v[196:199], v[164:167], a[44:47]\n\t"
    "s_add_u32 s50, s54, 22528\n\t"
    "v_exp_f32_e64 v147, v147\n\t"
    "v_mfma_f32_16x16x32_bf16 a[48:51], v[200:203], v[160:163], a[48:51]\n\t"
    "s_mov_b32 s46, 5231616\n\t"
    "v_exp_f32_e64 v150, v150\n\t"
    "v_mfma_f32_16x16x32_bf16 a[56:59], v[200:203], v[164:167], a[56:59]\n\t"
    "s_mov_b32 s47, 5235712\n\t"
    "v_exp_f32_e64 v151, v151\n\t"
    "v_mfma_f32_16x16x32_bf16 a[52:55], v[204:207], v[160:163], a[52:55]\n\t"
    "s_add_u32 s92, s57, 4096\n\t"
    "v_exp_f32_e64 v152, v152\n\t"
    "v_mfma_f32_16x16x32_bf16 a[60:63], v[204:207], v[164:167], a[60:63]\n\t"
    "s_mov_b32 s72, 119808\n\t"
    "v_exp_f32_e64 v153, v153\n\t"
    "v_mfma_f32_16x16x32_bf16 a[128:131], v[208:211], v[160:163], a[128:131]\n\t"
    "s_add_u32 m0, s61, 36864\n\t"
    "v_exp_f32 v156, v156\n\t"
    "v_mfma_f32_16x16x32_bf16 a[132:135], v[208:211], v[164:167], a[132:135]\n\t"
    "s_waitcnt lgkmcnt(2)\n\t"
    "v_exp_f32 v157, v157\n\t"
    "v_mfma_f32_32x32x16_bf16 v[128:143], a[216:219], a[144:147], v[64:79]\n\t"
    "buffer_load_dwordx4 v101, s[64:67], s72 offen offset:0 sc1 lds\n\t"
    "v_exp_f32 v154, v154\n\t"
    "v_exp_f32_e64 v155, v155\n\t"
    "v_mfma_f32_32x32x16_bf16 v[128:143], a[220:223], a[148:151], v[128:143]\n\t"
    "buffer_load_dwordx4 v101, s[64:67], s72 offen offset:1024 sc1 lds\n\t"
    "v_cvt_pk_bf16_f32 v168, v144, v145\n\t"
    "v_cvt_pk_bf16_f32 v172, v148, v149\n\t"
    "v_exp_f32 v158, v158\n\t"
    "v_mfma_f32_32x32x16_bf16 v[128:143], a[224:227], a[152:155], v[128:143]\n\t"
    "buffer_load_dwordx4 v102, s[64:67], s72 offen offset:2048 sc1 lds\n\t"
    "v_exp_f32 v159, v159\n\t"
    "v_cvt_pk_bf16_f32 v169, v146, v147\n\t"
    "s_add_u32 s93, s57, 8192\n\t"
    "v_mfma_f32_32x32x16_bf16 v[128:143], a[228:231], a[156:159], v[128:143]\n\t"
    "buffer_load_dwordx4 v102, s[64:67], s72 offen offset:3072 sc1 lds\n\t"
    "s_add_u32 m0, s61, 40960\n\t"
    "v_permlane16_swap_b32 v168, v172\n\t"
    "v_cvt_pk_bf16_f32 v173, v150, v151\n\t"
    "v_cvt_pk_bf16_f32 v170, v152, v153\n\t"
    "v_mfma_f32_32x32x16_bf16 v[128:143], a[232:235], a[160:163], v[128:143]\n\t"
    "buffer_load_dwordx4 v103, s[64:67], s72 offen offset:0 sc1 lds\n\t"
    "v_cvt_pk_bf16_f32 v174, v156, v157\n\t"
    "v_permlane16_swap_b32 v169, v173\n\t"
    "s_add_u32 s94, s59, 41472\n\t"
    "v_mfma_f32_32x32x16_bf16 v[128:143], a[236:239], a[164:167], v[128:143]\n\t"
    "buffer_load_dwordx4 v103, s[64:67], s72 offen offset:1024 sc1 lds\n\t"
    "v_cvt_pk_bf16_f32 v171, v154, v155\n\t"
    "v_cvt_pk_bf16_f32 v175, v158, v159\n\t"
    "v_permlane16_swap_b32 v170, v174\n\t"
    "v_mfma_f32_32x32x16_bf16 v[128:143], a[240:243], a[168:171], v[128:143]\n\t"
    "buffer_load_dwordx4 v104, s[64:67], s72 offen offset:2048 sc1 lds\n\t"
    "v_permlane16_swap_b32 v171, v175\n\t"
    "s_add_u32 s78, s59, 45568\n\t"
    "v_mfma_f32_32x32x16_bf16 v[128:143], a[244:247], a[172:175], v[128:143]\n\t"
    "buffer_load_dwordx4 v104, s[64:67], s72 offen offset:3072 sc1 lds\n\t"
    "s_add_u32 m0, s61, 45056\n\t"
    "v_mfma_f32_32x32x16_bf16 v[128:143], a[248:251], a[176:179], v[128:143]\n\t"
    "buffer_load_dwordx4 v105, s[64:67], s72 offen offset:0 sc1 lds\n\t"
    "v_mfma_f32_16x16x32_bf16 a[64:67], v[176:179], v[168:171], a[64:67]\n\t"
    "buffer_load_dwordx2 v[54:55], v121, s[68:71], 0 offen offset:0\n\t"
    "v_mfma_f32_16x16x32_bf16 a[72:75], v[176:179], v[172:175], a[72:75]\n\t"
    "v_mfma_f32_16x16x32_bf16 a[68:71], v[180:183], v[168:171], a[68:71]\n\t"
    "ds_read_b64_tr_b16 v[176:177], v248 offset:0\n\t"
    "ds_read_b64_tr_b16 v[178:179], v248 offset:256\n\t"
    "v_mfma_f32_16x16x32_bf16 a[76:79], v[180:183], v[172:175], a[76:79]\n\t"
    "buffer_load_dwordx2 v[56:57], v121, s[68:71], 0 offen offset:16\n\t"
    "v_mfma_f32_16x16x32_bf16 a[80:83], v[184:187], v[168:171], a[80:83]\n\t"
    "ds_read_b64_tr_b16 v[180:181], v249 offset:1024\n\t"
    "ds_read_b64_tr_b16 v[182:183], v249 offset:1280\n\t"
    "v_mfma_f32_16x16x32_bf16 a[88:91], v[184:187], v[172:175], a[88:91]\n\t"
    "v_mfma_f32_16x16x32_bf16 a[84:87], v[188:191], v[168:171], a[84:87]\n\t"
    "v_exp_f32 v128, v128\n\t"
    "v_mfma_f32_16x16x32_bf16 a[92:95], v[188:191], v[172:175], a[92:95]\n\t"
    "v_exp_f32 v129, v129\n\t"
    "v_mfma_f32_16x16x32_bf16 a[96:99], v[192:195], v[168:171], a[96:99]\n\t"
    "v_exp_f32 v132, v132\n\t"
    "v_mfma_f32_16x16x32_bf16 a[104:107], v[192:195], v[172:175], a[104:107]\n\t"
    "v_exp_f32 v133, v133\n\t"
    "v_mfma_f32_16x16x32_bf16 a[100:103], v[196:199], v[168:171], a[100:103]\n\t"
    "v_exp_f32 v130, v130\n\t"
    "v_mfma_f32_16x16x32_bf16 a[108:111], v[196:199], v[172:175], a[108:111]\n\t"
    "v_exp_f32 v131, v131\n\t"
    "v_mfma_f32_16x16x32_bf16 a[112:115], v[200:203], v[168:171], a[112:115]\n\t"
    "v_exp_f32 v134, v134\n\t"
    "v_mfma_f32_16x16x32_bf16 a[120:123], v[200:203], v[172:175], a[120:123]\n\t"
    "v_exp_f32 v135, v135\n\t"
    "v_mfma_f32_16x16x32_bf16 a[116:119], v[204:207], v[168:171], a[116:119]\n\t"
    "v_exp_f32 v136, v136\n\t"
    "v_mfma_f32_16x16x32_bf16 a[124:127], v[204:207], v[172:175], a[124:127]\n\t"
    "v_exp_f32 v137, v137\n\t"
    "v_mfma_f32_16x16x32_bf16 a[136:139], v[208:211], v[168:171], a[136:139]\n\t"
    "v_exp_f32 v140, v140\n\t"
    "v_mfma_f32_16x16x32_bf16 a[140:143], v[208:211], v[172:175], a[140:143]\n\t"
    "v_exp_f32 v141, v141\n\t"
    "v_mfma_f32_32x32x16_bf16 v[144:159], a[216:219], a[180:183], v[80:95]\n\t"
    "ds_read_b64_tr_b16 v[184:185], v248 offset:2048\n\t"
    "ds_read_b64_tr_b16 v[186:187], v248 offset:2304\n\t"
    "v_exp_f32 v138, v138\n\t"
    "v_exp_f32 v139, v139\n\t"
    "v_mfma_f32_32x32x16_bf16 v[144:159], a[220:223], a[184:187], v[144:159]\n\t"
    "ds_read_b64_tr_b16 v[188:189], v249 offset:3072\n\t"
    "ds_read_b64_tr_b16 v[190:191], v249 offset:3328\n\t"
    "v_cvt_pk_bf16_f32 v160, v128, v129\n\t"
    "v_cvt_pk_bf16_f32 v164, v132, v133\n\t"
    "v_exp_f32_e64 v142, v142\n\t"
    "v_mfma_f32_32x32x16_bf16 v[144:159], a[224:227], a[188:191], v[144:159]\n\t"
    "ds_read_b64_tr_b16 v[192:193], v248 offset:4096\n\t"
    "ds_read_b64_tr_b16 v[194:195], v248 offset:4352\n\t"
    "v_exp_f32_e64 v143, v143\n\t"
    "v_cvt_pk_bf16_f32 v161, v130, v131\n\t"
    "v_mfma_f32_32x32x16_bf16 v[144:159], a[228:231], a[192:195], v[144:159]\n\t"
    "ds_read_b64_tr_b16 v[196:197], v249 offset:5120\n\t"
    "ds_read_b64_tr_b16 v[198:199], v249 offset:5376\n\t"
    "v_permlane16_swap_b32 v160, v164\n\t"
    "v_cvt_pk_bf16_f32 v165, v134, v135\n\t"
    "v_cvt_pk_bf16_f32 v162, v136, v137\n\t"
    "v_mfma_f32_32x32x16_bf16 v[144:159], a[232:235], a[196:199], v[144:159]\n\t"
    "ds_read_b64_tr_b16 v[200:201], v248 offset:6144\n\t"
    "ds_read_b64_tr_b16 v[202:203], v248 offset:6400\n\t"
    "v_cvt_pk_bf16_f32 v166, v140, v141\n\t"
    "v_permlane16_swap_b32 v161, v165\n\t"
    "v_mfma_f32_32x32x16_bf16 v[144:159], a[236:239], a[200:203], v[144:159]\n\t"
    "ds_read_b64_tr_b16 v[204:205], v249 offset:7168\n\t"
    "ds_read_b64_tr_b16 v[206:207], v249 offset:7424\n\t"
    "v_cvt_pk_bf16_f32 v163, v138, v139\n\t"
    "v_cvt_pk_bf16_f32 v167, v142, v143\n\t"
    "v_permlane16_swap_b32 v162, v166\n\t"
    "v_mfma_f32_32x32x16_bf16 v[144:159], a[240:243], a[204:207], v[144:159]\n\t"
    "ds_read_b64_tr_b16 v[208:209], v248 offset:8192\n\t"
    "ds_read_b64_tr_b16 v[210:211], v248 offset:8448\n\t"
    "v_permlane16_swap_b32 v163, v167\n\t"
    "v_mfma_f32_32x32x16_bf16 v[144:159], a[244:247], a[208:211], v[144:159]\n\t"
    "v_mfma_f32_32x32x16_bf16 v[144:159], a[248:251], a[212:215], v[144:159]\n\t"
    "s_waitcnt vmcnt(11)\n\t"
    "s_waitcnt lgkmcnt(0)\n\t"
    "buffer_load_dwordx2 v[58:59], v121, s[68:71], 0 offen offset:32\n\t"
    "v_mfma_f32_16x16x32_bf16 a[0:3], v[176:179], v[160:163], a[0:3]\n\t"
    "ds_read_b128 a[216:219], v96 offset:0\n\t"
    "ds_read_b128 a[220:223], v97 offset:1024\n\t"
    "v_mfma_f32_16x16x32_bf16 a[8:11], v[176:179], v[164:167], a[8:11]\n\t"
    "ds_read_b128 a[224:227], v96 offset:2048\n\t"
    "ds_read_b128 a[228:231], v97 offset:3072\n\t"
    "v_mfma_f32_16x16x32_bf16 a[4:7], v[180:183], v[160:163], a[4:7]\n\t"
    "ds_read_b128 a[232:235], v96 offset:4096\n\t"
    "ds_read_b128 a[236:239], v97 offset:5120\n\t"
    "v_mfma_f32_16x16x32_bf16 a[12:15], v[180:183], v[164:167], a[12:15]\n\t"
    "ds_read_b128 a[240:243], v96 offset:6144\n\t"
    "ds_read_b128 a[244:247], v97 offset:7168\n\t"
    "v_mfma_f32_16x16x32_bf16 a[16:19], v[184:187], v[160:163], a[16:19]\n\t"
    "ds_read_b128 a[248:251], v96 offset:8192\n\t"
    "v_mfma_f32_16x16x32_bf16 a[24:27], v[184:187], v[164:167], a[24:27]\n\t"
    "s_mov_b64 exec, 1\n\t"
    "buffer_store_dwordx2 v[234:235], off, s[80:83], s37 sc1\n\t"
    "s_mov_b64 exec, -1\n\t"
    "v_mfma_f32_16x16x32_bf16 a[20:23], v[188:191], v[160:163], a[20:23]\n\t"
    "v_exp_f32 v144, v144\n\t"
    "v_mfma_f32_16x16x32_bf16 a[28:31], v[188:191], v[164:167], a[28:31]\n\t"
    "v_exp_f32 v145, v145\n\t"
    "v_mfma_f32_16x16x32_bf16 a[32:35], v[192:195], v[160:163], a[32:35]\n\t"
    "v_exp_f32 v148, v148\n\t"
    "v_mfma_f32_16x16x32_bf16 a[40:43], v[192:195], v[164:167], a[40:43]\n\t"
    "v_exp_f32 v149, v149\n\t"
    "v_mfma_f32_16x16x32_bf16 a[36:39], v[196:199], v[160:163], a[36:39]\n\t"
    "v_exp_f32 v146, v146\n\t"
    "v_mfma_f32_16x16x32_bf16 a[44:47], v[196:199], v[164:167], a[44:47]\n\t"
    "v_exp_f32 v147, v147\n\t"
    "v_mfma_f32_16x16x32_bf16 a[48:51], v[200:203], v[160:163], a[48:51]\n\t"
    "v_exp_f32 v150, v150\n\t"
    "v_mfma_f32_16x16x32_bf16 a[56:59], v[200:203], v[164:167], a[56:59]\n\t"
    "v_exp_f32 v151, v151\n\t"
    "v_mfma_f32_16x16x32_bf16 a[52:55], v[204:207], v[160:163], a[52:55]\n\t"
    "v_exp_f32_e64 v152, v152\n\t"
    "v_mfma_f32_16x16x32_bf16 a[60:63], v[204:207], v[164:167], a[60:63]\n\t"
    "s_mov_b32 s72, 129024\n\t"
    "v_exp_f32_e64 v153, v153\n\t"
    "v_mfma_f32_16x16x32_bf16 a[128:131], v[208:211], v[160:163], a[128:131]\n\t"
    "s_add_u32 m0, s61, 73728\n\t"
    "v_exp_f32 v156, v156\n\t"
    "v_mfma_f32_16x16x32_bf16 a[132:135], v[208:211], v[164:167], a[132:135]\n\t"
    "s_waitcnt lgkmcnt(0)\n\t"
    "v_exp_f32 v157, v157\n\t"
    "v_mfma_f32_32x32x16_bf16 v[128:143], a[216:219], a[144:147], v[64:79]\n\t"
    "buffer_load_dwordx4 v101, s[64:67], s72 offen offset:0 sc1 lds\n\t"
    "v_exp_f32 v154, v154\n\t"
    "v_exp_f32_e64 v155, v155\n\t"
    "v_mfma_f32_32x32x16_bf16 v[128:143], a[220:223], a[148:151], v[128:143]\n\t"
    "buffer_load_dwordx4 v101, s[64:67], s72 offen offset:1024 sc1 lds\n\t"
    "v_cvt_pk_bf16_f32 v168, v144, v145\n\t"
    "v_cvt_pk_bf16_f32 v172, v148, v149\n\t"
    "v_exp_f32 v158, v158\n\t"
    "v_mfma_f32_32x32x16_bf16 v[128:143], a[224:227], a[152:155], v[128:143]\n\t"
    "buffer_load_dwordx4 v102, s[64:67], s72 offen offset:2048 sc1 lds\n\t"
    "v_exp_f32 v159, v159\n\t"
    "v_cvt_pk_bf16_f32 v169, v146, v147\n\t"
    "v_mfma_f32_32x32x16_bf16 v[128:143], a[228:231], a[156:159], v[128:143]\n\t"
    "buffer_load_dwordx4 v102, s[64:67], s72 offen offset:3072 sc1 lds\n\t"
    "s_add_u32 m0, s61, 77824\n\t"
    "v_permlane16_swap_b32 v168, v172\n\t"
    "v_cvt_pk_bf16_f32 v173, v150, v151\n\t"
    "v_cvt_pk_bf16_f32 v170, v152, v153\n\t"
    "v_mfma_f32_32x32x16_bf16 v[128:143], a[232:235], a[160:163], v[128:143]\n\t"
    "buffer_load_dwordx4 v103, s[64:67], s72 offen offset:0 sc1 lds\n\t"
    "v_cvt_pk_bf16_f32 v174, v156, v157\n\t"
    "v_permlane16_swap_b32 v169, v173\n\t"
    "v_mfma_f32_32x32x16_bf16 v[128:143], a[236:239], a[164:167], v[128:143]\n\t"
    "buffer_load_dwordx4 v103, s[64:67], s72 offen offset:1024 sc1 lds\n\t"
    "v_cvt_pk_bf16_f32 v171, v154, v155\n\t"
    "v_cvt_pk_bf16_f32 v175, v158, v159\n\t"
    "v_permlane16_swap_b32 v170, v174\n\t"
    "v_mfma_f32_32x32x16_bf16 v[128:143], a[240:243], a[168:171], v[128:143]\n\t"
    "buffer_load_dwordx4 v104, s[64:67], s72 offen offset:2048 sc1 lds\n\t"
    "v_permlane16_swap_b32 v171, v175\n\t"
    "v_mfma_f32_32x32x16_bf16 v[128:143], a[244:247], a[172:175], v[128:143]\n\t"
    "buffer_load_dwordx4 v104, s[64:67], s72 offen offset:3072 sc1 lds\n\t"
    "s_add_u32 m0, s61, 81920\n\t"
    "v_mfma_f32_32x32x16_bf16 v[128:143], a[248:251], a[176:179], v[128:143]\n\t"
    "buffer_load_dwordx4 v105, s[64:67], s72 offen offset:0 sc1 lds\n\t"
    "v_mfma_f32_16x16x32_bf16 a[64:67], v[176:179], v[168:171], a[64:67]\n\t"
    "buffer_load_dwordx2 v[60:61], v121, s[68:71], 0 offen offset:48\n\t"
    "v_mfma_f32_16x16x32_bf16 a[72:75], v[176:179], v[172:175], a[72:75]\n\t"
    "v_mfma_f32_16x16x32_bf16 a[68:71], v[180:183], v[168:171], a[68:71]\n\t"
    "ds_read_b64_tr_b16 v[176:177], v98 offset:0\n\t"
    "ds_read_b64_tr_b16 v[178:179], v98 offset:256\n\t"
    "v_mfma_f32_16x16x32_bf16 a[76:79], v[180:183], v[172:175], a[76:79]\n\t"
    "buffer_load_dwordx2 v[62:63], v122, s[68:71], 0 offen\n\t"
    "v_mfma_f32_16x16x32_bf16 a[80:83], v[184:187], v[168:171], a[80:83]\n\t"
    "ds_read_b64_tr_b16 v[180:181], v99 offset:1024\n\t"
    "ds_read_b64_tr_b16 v[182:183], v99 offset:1280\n\t"
    "v_mfma_f32_16x16x32_bf16 a[88:91], v[184:187], v[172:175], a[88:91]\n\t"
    "v_mfma_f32_16x16x32_bf16 a[84:87], v[188:191], v[168:171], a[84:87]\n\t"
    "v_exp_f32 v128, v128\n\t"
    "v_mfma_f32_16x16x32_bf16 a[92:95], v[188:191], v[172:175], a[92:95]\n\t"
    "v_exp_f32 v129, v129\n\t"
    "v_mfma_f32_16x16x32_bf16 a[96:99], v[192:195], v[168:171], a[96:99]\n\t"
    "v_exp_f32 v132, v132\n\t"
    "v_mfma_f32_16x16x32_bf16 a[104:107], v[192:195], v[172:175], a[104:107]\n\t"
    "v_exp_f32 v133, v133\n\t"
    "v_mfma_f32_16x16x32_bf16 a[100:103], v[196:199], v[168:171], a[100:103]\n\t"
    "v_exp_f32 v130, v130\n\t"
    "v_mfma_f32_16x16x32_bf16 a[108:111], v[196:199], v[172:175], a[108:111]\n\t"
    "v_exp_f32 v131, v131\n\t"
    "v_mfma_f32_16x16x32_bf16 a[112:115], v[200:203], v[168:171], a[112:115]\n\t"
    "v_exp_f32 v134, v134\n\t"
    "v_mfma_f32_16x16x32_bf16 a[120:123], v[200:203], v[172:175], a[120:123]\n\t"
    "v_exp_f32 v135, v135\n\t"
    "v_mfma_f32_16x16x32_bf16 a[116:119], v[204:207], v[168:171], a[116:119]\n\t"
    "v_exp_f32 v136, v136\n\t"
    "v_mfma_f32_16x16x32_bf16 a[124:127], v[204:207], v[172:175], a[124:127]\n\t"
    "v_exp_f32 v137, v137\n\t"
    "v_mfma_f32_16x16x32_bf16 a[136:139], v[208:211], v[168:171], a[136:139]\n\t"
    "v_exp_f32 v140, v140\n\t"
    "v_mfma_f32_16x16x32_bf16 a[140:143], v[208:211], v[172:175], a[140:143]\n\t"
    "v_exp_f32 v141, v141\n\t"
    "v_mfma_f32_32x32x16_bf16 v[144:159], a[216:219], a[180:183], v[80:95]\n\t"
    "ds_read_b64_tr_b16 v[184:185], v98 offset:2048\n\t"
    "ds_read_b64_tr_b16 v[186:187], v98 offset:2304\n\t"
    "v_exp_f32 v138, v138\n\t"
    "v_exp_f32 v139, v139\n\t"
    "v_mfma_f32_32x32x16_bf16 v[144:159], a[220:223], a[184:187], v[144:159]\n\t"
    "ds_read_b64_tr_b16 v[188:189], v99 offset:3072\n\t"
    "ds_read_b64_tr_b16 v[190:191], v99 offset:3328\n\t"
    "v_cvt_pk_bf16_f32 v160, v128, v129\n\t"
    "v_cvt_pk_bf16_f32 v164, v132, v133\n\t"
    "v_exp_f32_e64 v142, v142\n\t"
    "v_mfma_f32_32x32x16_bf16 v[144:159], a[224:227], a[188:191], v[144:159]\n\t"
    "ds_read_b64_tr_b16 v[192:193], v98 offset:4096\n\t"
    "ds_read_b64_tr_b16 v[194:195], v98 offset:4352\n\t"
    "v_exp_f32_e64 v143, v143\n\t"
    "v_cvt_pk_bf16_f32 v161, v130, v131\n\t"
    "v_mfma_f32_32x32x16_bf16 v[144:159], a[228:231], a[192:195], v[144:159]\n\t"
    "ds_read_b64_tr_b16 v[196:197], v99 offset:5120\n\t"
    "ds_read_b64_tr_b16 v[198:199], v99 offset:5376\n\t"
    "v_permlane16_swap_b32 v160, v164\n\t"
    "v_cvt_pk_bf16_f32 v165, v134, v135\n\t"
    "v_cvt_pk_bf16_f32 v162, v136, v137\n\t"
    "v_mfma_f32_32x32x16_bf16 v[144:159], a[232:235], a[196:199], v[144:159]\n\t"
    "ds_read_b64_tr_b16 v[200:201], v98 offset:6144\n\t"
    "ds_read_b64_tr_b16 v[202:203], v98 offset:6400\n\t"
    "v_cvt_pk_bf16_f32 v166, v140, v141\n\t"
    "v_permlane16_swap_b32 v161, v165\n\t"
    "v_mfma_f32_32x32x16_bf16 v[144:159], a[236:239], a[200:203], v[144:159]\n\t"
    "ds_read_b64_tr_b16 v[204:205], v99 offset:7168\n\t"
    "ds_read_b64_tr_b16 v[206:207], v99 offset:7424\n\t"
    "v_cvt_pk_bf16_f32 v163, v138, v139\n\t"
    "v_cvt_pk_bf16_f32 v167, v142, v143\n\t"
    "v_permlane16_swap_b32 v162, v166\n\t"
    "v_mfma_f32_32x32x16_bf16 v[144:159], a[240:243], a[204:207], v[144:159]\n\t"
    "ds_read_b64_tr_b16 v[208:209], v98 offset:8192\n\t"
    "ds_read_b64_tr_b16 v[210:211], v98 offset:8448\n\t"
    "v_permlane16_swap_b32 v163, v167\n\t"
    "v_mfma_f32_32x32x16_bf16 v[144:159], a[244:247], a[208:211], v[144:159]\n\t"
    "v_mfma_f32_32x32x16_bf16 v[144:159], a[248:251], a[212:215], v[144:159]\n\t"
    "s_waitcnt vmcnt(15)\n\t"
    "s_waitcnt lgkmcnt(0)\n\t"
    "v_mfma_f32_16x16x32_bf16 a[0:3], v[176:179], v[160:163], a[0:3]\n\t"
    "ds_read_b128 a[216:219], v96 offset:36864\n\t"
    "ds_read_b128 a[220:223], v97 offset:37888\n\t"
    "v_mfma_f32_16x16x32_bf16 a[8:11], v[176:179], v[164:167], a[8:11]\n\t"
    "ds_read_b128 a[224:227], v96 offset:38912\n\t"
    "ds_read_b128 a[228:231], v97 offset:39936\n\t"
    "v_mfma_f32_16x16x32_bf16 a[4:7], v[180:183], v[160:163], a[4:7]\n\t"
    "ds_read_b128 a[232:235], v96 offset:40960\n\t"
    "ds_read_b128 a[236:239], v97 offset:41984\n\t"
    "v_mfma_f32_16x16x32_bf16 a[12:15], v[180:183], v[164:167], a[12:15]\n\t"
    "ds_read_b128 a[240:243], v96 offset:43008\n\t"
    "ds_read_b128 a[244:247], v97 offset:44032\n\t"
    "v_mfma_f32_16x16x32_bf16 a[16:19], v[184:187], v[160:163], a[16:19]\n\t"
    "ds_read_b128 a[248:251], v96 offset:45056\n\t"
    "v_mfma_f32_16x16x32_bf16 a[24:27], v[184:187], v[164:167], a[24:27]\n\t"
    "v_mfma_f32_16x16x32_bf16 a[20:23], v[188:191], v[160:163], a[20:23]\n\t"
    "v_exp_f32 v144, v144\n\t"
    "v_mfma_f32_16x16x32_bf16 a[28:31], v[188:191], v[164:167], a[28:31]\n\t"
    "v_exp_f32 v145, v145\n\t"
    "v_mfma_f32_16x16x32_bf16 a[32:35], v[192:195], v[160:163], a[32:35]\n\t"
    "v_exp_f32 v148, v148\n\t"
    "v_mfma_f32_16x16x32_bf16 a[40:43], v[192:195], v[164:167], a[40:43]\n\t"
    "v_exp_f32 v149, v149\n\t"
    "v_mfma_f32_16x16x32_bf16 a[36:39], v[196:199], v[160:163], a[36:39]\n\t"
    "v_exp_f32 v146, v146\n\t"
    "v_mfma_f32_16x16x32_bf16 a[44:47], v[196:199], v[164:167], a[44:47]\n\t"
    "v_exp_f32 v147, v147\n\t"
    "v_mfma_f32_16x16x32_bf16 a[48:51], v[200:203], v[160:163], a[48:51]\n\t"
    "v_exp_f32 v150, v150\n\t"
    "v_mfma_f32_16x16x32_bf16 a[56:59], v[200:203], v[164:167], a[56:59]\n\t"
    "v_exp_f32 v151, v151\n\t"
    "v_mfma_f32_16x16x32_bf16 a[52:55], v[204:207], v[160:163], a[52:55]\n\t"
    "v_exp_f32 v152, v152\n\t"
    "v_mfma_f32_16x16x32_bf16 a[60:63], v[204:207], v[164:167], a[60:63]\n\t"
    "s_mov_b32 s72, s79\n\t"
    "v_exp_f32 v153, v153\n\t"
    "v_mfma_f32_16x16x32_bf16 a[128:131], v[208:211], v[160:163], a[128:131]\n\t"
    "s_add_u32 m0, s61, 0\n\t"
    "v_exp_f32 v156, v156\n\t"
    "v_mfma_f32_16x16x32_bf16 a[132:135], v[208:211], v[164:167], a[132:135]\n\t"
    "s_waitcnt lgkmcnt(0)\n\t"
    "v_exp_f32 v157, v157\n\t"
    "s_cmp_lg_u32 s34, 0\n\t"
    "s_cbranch_scc1 SH_%=\n\t"
    "v_mfma_f32_32x32x16_bf16 v[128:143], a[216:219], a[144:147], v[64:79]\n\t"
    "buffer_load_dwordx4 v101, s[64:67], s72 offen offset:0 sc1 lds\n\t"
    "v_exp_f32 v154, v154\n\t"
    "v_exp_f32_e64 v155, v155\n\t"
    "v_mfma_f32_32x32x16_bf16 v[128:143], a[220:223], a[148:151], v[128:143]\n\t"
    "buffer_load_dwordx4 v101, s[64:67], s72 offen offset:1024 sc1 lds\n\t"
    "v_cvt_pk_bf16_f32 v168, v144, v145\n\t"
    "v_cvt_pk_bf16_f32 v172, v148, v149\n\t"
    "v_exp_f32 v158, v158\n\t"
    "v_mfma_f32_32x32x16_bf16 v[128:143], a[224:227], a[152:155], v[128:143]\n\t"
    "buffer_load_dwordx4 v102, s[64:67], s72 offen offset:2048 sc1 lds\n\t"
    "v_exp_f32 v159, v159\n\t"
    "v_cvt_pk_bf16_f32 v169, v146, v147\n\t"
    "v_mfma_f32_32x32x16_bf16 v[128:143], a[228:231], a[156:159], v[128:143]\n\t"
    "buffer_load_dwordx4 v102, s[64:67], s72 offen offset:3072 sc1 lds\n\t"
    "s_add_u32 m0, s61, 4096\n\t"
    "v_permlane16_swap_b32 v168, v172\n\t"
    "v_cvt_pk_bf16_f32 v173, v150, v151\n\t"
    "v_cvt_pk_bf16_f32 v170, v152, v153\n\t"
    "v_mfma_f32_32x32x16_bf16 v[128:143], a[232:235], a[160:163], v[128:143]\n\t"
    "buffer_load_dwordx4 v103, s[64:67], s72 offen offset:0 sc1 lds\n\t"
    "v_cvt_pk_bf16_f32 v174, v156, v157\n\t"
    "v_permlane16_swap_b32 v169, v173\n\t"
    "v_mfma_f32_32x32x16_bf16 v[128:143], a[236:239], a[164:167], v[128:143]\n\t"
    "buffer_load_dwordx4 v103, s[64:67], s72 offen offset:1024 sc1 lds\n\t"
    "v_cvt_pk_bf16_f32 v171, v154, v155\n\t"
    "v_cvt_pk_bf16_f32 v175, v158, v159\n\t"
    "v_permlane16_swap_b32 v170, v174\n\t"
    "v_mfma_f32_32x32x16_bf16 v[128:143], a[240:243], a[168:171], v[128:143]\n\t"
    "buffer_load_dwordx4 v104, s[64:67], s72 offen offset:2048 sc1 lds\n\t"
    "v_permlane16_swap_b32 v171, v175\n\t"
    "v_mfma_f32_32x32x16_bf16 v[128:143], a[244:247], a[172:175], v[128:143]\n\t"
    "buffer_load_dwordx4 v104, s[64:67], s72 offen offset:3072 sc1 lds\n\t"
    "s_add_u32 m0, s61, 8192\n\t"
    "v_mfma_f32_32x32x16_bf16 v[128:143], a[248:251], a[176:179], v[128:143]\n\t"
    "buffer_load_dwordx4 v105, s[64:67], s72 offen offset:0 sc1 lds\n\t"
    "v_mfma_f32_16x16x32_bf16 a[64:67], v[176:179], v[168:171], a[64:67]\n\t"
    "v_mfma_f32_16x16x32_bf16 a[72:75], v[176:179], v[172:175], a[72:75]\n\t"
    "v_mfma_f32_16x16x32_bf16 a[68:71], v[180:183], v[168:171], a[68:71]\n\t"
    "ds_read_b64_tr_b16 v[176:177], v98 offset:36864\n\t"
    "ds_read_b64_tr_b16 v[178:179], v98 offset:37120\n\t"
    "v_mfma_f32_16x16x32_bf16 a[76:79], v[180:183], v[172:175], a[76:79]\n\t"
    "v_mfma_f32_16x16x32_bf16 a[80:83], v[184:187], v[168:171], a[80:83]\n\t"
    "ds_read_b64_tr_b16 v[180:181], v99 offset:37888\n\t"
    "ds_read_b64_tr_b16 v[182:183], v99 offset:38144\n\t"
    "v_mfma_f32_16x16x32_bf16 a[88:91], v[184:187], v[172:175], a[88:91]\n\t"
    "v_mfma_f32_16x16x32_bf16 a[84:87], v[188:191], v[168:171], a[84:87]\n\t"
    "v_exp_f32 v128, v128\n\t"
    "v_mfma_f32_16x16x32_bf16 a[92:95], v[188:191], v[172:175], a[92:95]\n\t"
    "v_exp_f32 v129, v129\n\t"
    "v_mfma_f32_16x16x32_bf16 a[96:99], v[192:195], v[168:171], a[96:99]\n\t"
    "v_exp_f32 v132, v132\n\t"
    "v_mfma_f32_16x16x32_bf16 a[104:107], v[192:195], v[172:175], a[104:107]\n\t"
    "v_exp_f32 v133, v133\n\t"
    "v_mfma_f32_16x16x32_bf16 a[100:103], v[196:199], v[168:171], a[100:103]\n\t"
    "v_exp_f32 v130, v130\n\t"
    "v_mfma_f32_16x16x32_bf16 a[108:111], v[196:199], v[172:175], a[108:111]\n\t"
    "v_exp_f32 v131, v131\n\t"
    "v_mfma_f32_16x16x32_bf16 a[112:115], v[200:203], v[168:171], a[112:115]\n\t"
    "v_exp_f32 v134, v134\n\t"
    "v_mfma_f32_16x16x32_bf16 a[120:123], v[200:203], v[172:175], a[120:123]\n\t"
    "v_exp_f32 v135, v135\n\t"
    "v_mfma_f32_16x16x32_bf16 a[116:119], v[204:207], v[168:171], a[116:119]\n\t"
    "v_exp_f32 v136, v136\n\t"
    "v_mfma_f32_16x16x32_bf16 a[124:127], v[204:207], v[172:175], a[124:127]\n\t"
    "v_exp_f32 v137, v137\n\t"
    "v_mfma_f32_16x16x32_bf16 a[136:139], v[208:211], v[168:171], a[136:139]\n\t"
    "v_exp_f32 v140, v140\n\t"
    "v_mfma_f32_16x16x32_bf16 a[140:143], v[208:211], v[172:175], a[140:143]\n\t"
    "v_exp_f32 v141, v141\n\t"
    "v_mfma_f32_32x32x16_bf16 v[144:159], a[216:219], a[180:183], v[80:95]\n\t"
    "ds_read_b64_tr_b16 v[184:185], v98 offset:38912\n\t"
    "ds_read_b64_tr_b16 v[186:187], v98 offset:39168\n\t"
    "v_exp_f32 v138, v138\n\t"
    "v_exp_f32 v139, v139\n\t"
    "v_mfma_f32_32x32x16_bf16 v[144:159], a[220:223], a[184:187], v[144:159]\n\t"
    "ds_read_b64_tr_b16 v[188:189], v99 offset:39936\n\t"
    "ds_read_b64_tr_b16 v[190:191], v99 offset:40192\n\t"
    "v_cvt_pk_bf16_f32 v160, v128, v129\n\t"
    "v_cvt_pk_bf16_f32 v164, v132, v133\n\t"
    "v_exp_f32_e64 v142, v142\n\t"
    "v_mfma_f32_32x32x16_bf16 v[144:159], a[224:227], a[188:191], v[144:159]\n\t"
    "ds_read_b64_tr_b16 v[192:193], v98 offset:40960\n\t"
    "ds_read_b64_tr_b16 v[194:195], v98 offset:41216\n\t"
    "v_exp_f32_e64 v143, v143\n\t"
    "v_cvt_pk_bf16_f32 v161, v130, v131\n\t"
    "v_mfma_f32_32x32x16_bf16 v[144:159], a[228:231], a[192:195], v[144:159]\n\t"
    "ds_read_b64_tr_b16 v[196:197], v99 offset:41984\n\t"
    "ds_read_b64_tr_b16 v[198:199], v99 offset:42240\n\t"
    "v_permlane16_swap_b32 v160, v164\n\t"
    "v_cvt_pk_bf16_f32 v165, v134, v135\n\t"
    "v_cvt_pk_bf16_f32 v162, v136, v137\n\t"
    "v_mfma_f32_32x32x16_bf16 v[144:159], a[232:235], a[196:199], v[144:159]\n\t"
    "ds_read_b64_tr_b16 v[200:201], v98 offset:43008\n\t"
    "ds_read_b64_tr_b16 v[202:203], v98 offset:43264\n\t"
    "v_cvt_pk_bf16_f32 v166, v140, v141\n\t"
    "v_permlane16_swap_b32 v161, v165\n\t"
    "v_mfma_f32_32x32x16_bf16 v[144:159], a[236:239], a[200:203], v[144:159]\n\t"
    "ds_read_b64_tr_b16 v[204:205], v99 offset:44032\n\t"
    "ds_read_b64_tr_b16 v[206:207], v99 offset:44288\n\t"
    "v_cvt_pk_bf16_f32 v163, v138, v139\n\t"
    "v_cvt_pk_bf16_f32 v167, v142, v143\n\t"
    "v_permlane16_swap_b32 v162, v166\n\t"
    "v_mfma_f32_32x32x16_bf16 v[144:159], a[240:243], a[204:207], v[144:159]\n\t"
    "ds_read_b64_tr_b16 v[208:209], v98 offset:45056\n\t"
    "ds_read_b64_tr_b16 v[210:211], v98 offset:45312\n\t"
    "v_permlane16_swap_b32 v163, v167\n\t"
    "v_mfma_f32_32x32x16_bf16 v[144:159], a[244:247], a[208:211], v[144:159]\n\t"
    "v_mfma_f32_32x32x16_bf16 v[144:159], a[248:251], a[212:215], v[144:159]\n\t"
    "s_waitcnt vmcnt(11)\n\t"
    "s_waitcnt lgkmcnt(0)\n\t"
    "v_mfma_f32_16x16x32_bf16 a[0:3], v[176:179], v[160:163], a[0:3]\n\t"
    "ds_read_b128 a[216:219], v246 offset:0\n\t"
    "ds_read_b128 a[220:223], v247 offset:1024\n\t"
    "v_mfma_f32_16x16x32_bf16 a[8:11], v[176:179], v[164:167], a[8:11]\n\t"
    "ds_read_b128 a[224:227], v246 offset:2048\n\t"
    "ds_read_b128 a[228:231], v247 offset:3072\n\t"
    "v_mfma_f32_16x16x32_bf16 a[4:7], v[180:183], v[160:163], a[4:7]\n\t"
    "ds_read_b128 a[232:235], v246 offset:4096\n\t"
    "ds_read_b128 a[236:239], v247 offset:5120\n\t"
    "v_mfma_f32_16x16x32_bf16 a[12:15], v[180:183], v[164:167], a[12:15]\n\t"
    "ds_read_b128 a[240:243], v246 offset:6144\n\t"
    "ds_read_b128 a[244:247], v247 offset:7168\n\t"
    "v_mfma_f32_16x16x32_bf16 a[16:19], v[184:187], v[160:163], a[16:19]\n\t"
    "ds_read_b128 a[248:251], v246 offset:8192\n\t"
    "v_mfma_f32_16x16x32_bf16 a[24:27], v[184:187], v[164:167], a[24:27]\n\t"
    "v_mfma_f32_16x16x32_bf16 a[20:23], v[188:191], v[160:163], a[20:23]\n\t"
    "v_exp_f32 v144, v144\n\t"
    "v_mfma_f32_16x16x32_bf16 a[28:31], v[188:191], v[164:167], a[28:31]\n\t"
    "v_exp_f32 v145, v145\n\t"
    "v_mfma_f32_16x16x32_bf16 a[32:35], v[192:195], v[160:163], a[32:35]\n\t"
    "v_exp_f32 v148, v148\n\t"
    "v_mfma_f32_16x16x32_bf16 a[40:43], v[192:195], v[164:167], a[40:43]\n\t"
    "v_exp_f32 v149, v149\n\t"
    "v_mfma_f32_16x16x32_bf16 a[36:39], v[196:199], v[160:163], a[36:39]\n\t"
    "v_exp_f32 v146, v146\n\t"
    "v_mfma_f32_16x16x32_bf16 a[44:47], v[196:199], v[164:167], a[44:47]\n\t"
    "v_exp_f32 v147, v147\n\t"
    "v_mfma_f32_16x16x32_bf16 a[48:51], v[200:203], v[160:163], a[48:51]\n\t"
    "v_exp_f32 v150, v150\n\t"
    "v_mfma_f32_16x16x32_bf16 a[56:59], v[200:203], v[164:167], a[56:59]\n\t"
    "v_exp_f32 v151, v151\n\t"
    "v_mfma_f32_16x16x32_bf16 a[52:55], v[204:207], v[160:163], a[52:55]\n\t"
    "v_exp_f32 v152, v152\n\t"
    "v_mfma_f32_16x16x32_bf16 a[60:63], v[204:207], v[164:167], a[60:63]\n\t"
    "s_mov_b32 s72, s33\n\t"
    "v_exp_f32_e64 v153, v153\n\t"
    "v_mfma_f32_16x16x32_bf16 a[128:131], v[208:211], v[160:163], a[128:131]\n\t"
    "s_add_u32 m0, s61, 36864\n\t"
    "v_exp_f32 v156, v156\n\t"
    "v_mfma_f32_16x16x32_bf16 a[132:135], v[208:211], v[164:167], a[132:135]\n\t"
    "s_waitcnt lgkmcnt(0)\n\t"
    "v_exp_f32_e64 v157, v157\n\t"
    "v_mfma_f32_32x32x16_bf16 v[128:143], a[216:219], a[144:147], v[64:79]\n\t"
    "buffer_load_dwordx2 v[232:233], v115, s[80:83], 0 offen sc1\n\t"
    "buffer_load_dwordx4 v101, s[64:67], s72 offen offset:0 sc1 lds\n\t"
    "v_exp_f32 v154, v154\n\t"
    "v_exp_f32 v155, v155\n\t"
    "v_mfma_f32_32x32x16_bf16 v[128:143], a[220:223], a[148:151], v[128:143]\n\t"
    "buffer_load_dwordx4 v101, s[64:67], s72 offen offset:1024 sc1 lds\n\t"
    "v_cvt_pk_bf16_f32 v168, v144, v145\n\t"
    "v_cvt_pk_bf16_f32 v172, v148, v149\n\t"
    "v_exp_f32 v158, v158\n\t"
    "v_mfma_f32_32x32x16_bf16 v[128:143], a[224:227], a[152:155], v[128:143]\n\t"
    "buffer_load_dwordx4 v102, s[64:67], s72 offen offset:2048 sc1 lds\n\t"
    "v_exp_f32 v159, v159\n\t"
    "v_cvt_pk_bf16_f32 v169, v146, v147\n\t"
    "v_mfma_f32_32x32x16_bf16 v[128:143], a[228:231], a[156:159], v[128:143]\n\t"
    "buffer_load_dwordx4 v102, s[64:67], s72 offen offset:3072 sc1 lds\n\t"
    "s_add_u32 m0, s61, 40960\n\t"
    "v_permlane16_swap_b32 v168, v172\n\t"
    "v_cvt_pk_bf16_f32 v173, v150, v151\n\t"
    "v_cvt_pk_bf16_f32 v170, v152, v153\n\t"
    "v_mfma_f32_32x32x16_bf16 v[128:143], a[232:235], a[160:163], v[128:143]\n\t"
    "buffer_load_dwordx4 v103, s[64:67], s72 offen offset:0 sc1 lds\n\t"
    "v_cvt_pk_bf16_f32 v174, v156, v157\n\t"
    "v_permlane16_swap_b32 v169, v173\n\t"
    "v_mfma_f32_32x32x16_bf16 v[128:143], a[236:239], a[164:167], v[128:143]\n\t"
    "buffer_load_dwordx4 v103, s[64:67], s72 offen offset:1024 sc1 lds\n\t"
    "v_cvt_pk_bf16_f32 v171, v154, v155\n\t"
    "v_cvt_pk_bf16_f32 v175, v158, v159\n\t"
    "v_permlane16_swap_b32 v170, v174\n\t"
    "v_mfma_f32_32x32x16_bf16 v[128:143], a[240:243], a[168:171], v[128:143]\n\t"
    "buffer_load_dwordx4 v104, s[64:67], s72 offen offset:2048 sc1 lds\n\t"
    "v_permlane16_swap_b32 v171, v175\n\t"
    "v_mfma_f32_32x32x16_bf16 v[128:143], a[244:247], a[172:175], v[128:143]\n\t"
    "buffer_load_dwordx4 v104, s[64:67], s72 offen offset:3072 sc1 lds\n\t"
    "s_add_u32 m0, s61, 45056\n\t"
    "v_mfma_f32_32x32x16_bf16 v[128:143], a[248:251], a[176:179], v[128:143]\n\t"
    "buffer_load_dwordx4 v105, s[64:67], s72 offen offset:0 sc1 lds\n\t"
    "v_mfma_f32_16x16x32_bf16 a[64:67], v[176:179], v[168:171], a[64:67]\n\t"
    "v_mfma_f32_16x16x32_bf16 a[72:75], v[176:179], v[172:175], a[72:75]\n\t"
    "v_mfma_f32_16x16x32_bf16 a[68:71], v[180:183], v[168:171], a[68:71]\n\t"
    "ds_read_b64_tr_b16 v[176:177], v248 offset:0\n\t"
    "ds_read_b64_tr_b16 v[178:179], v248 offset:256\n\t"
    "v_mfma_f32_16x16x32_bf16 a[76:79], v[180:183], v[172:175], a[76:79]\n\t"
    "v_mfma_f32_16x16x32_bf16 a[80:83], v[184:187], v[168:171], a[80:83]\n\t"
    "ds_read_b64_tr_b16 v[180:181], v249 offset:1024\n\t"
    "ds_read_b64_tr_b16 v[182:183], v249 offset:1280\n\t"
    "v_mfma_f32_16x16x32_bf16 a[88:91], v[184:187], v[172:175], a[88:91]\n\t"
    "v_mfma_f32_16x16x32_bf16 a[84:87], v[188:191], v[168:171], a[84:87]\n\t"
    "v_exp_f32 v128, v128\n\t"
    "v_mfma_f32_16x16x32_bf16 a[92:95], v[188:191], v[172:175], a[92:95]\n\t"
    "v_exp_f32 v129, v129\n\t"
    "v_mfma_f32_16x16x32_bf16 a[96:99], v[192:195], v[168:171], a[96:99]\n\t"
    "v_exp_f32 v132, v132\n\t"
    "v_mfma_f32_16x16x32_bf16 a[104:107], v[192:195], v[172:175], a[104:107]\n\t"
    "v_exp_f32 v133, v133\n\t"
    "v_mfma_f32_16x16x32_bf16 a[100:103], v[196:199], v[168:171], a[100:103]\n\t"
    "v_exp_f32 v130, v130\n\t"
    "v_mfma_f32_16x16x32_bf16 a[108:111], v[196:199], v[172:175], a[108:111]\n\t"
    "v_exp_f32 v131, v131\n\t"
    "v_mfma_f32_16x16x32_bf16 a[112:115], v[200:203], v[168:171], a[112:115]\n\t"
    "v_exp_f32 v134, v134\n\t"
    "v_mfma_f32_16x16x32_bf16 a[120:123], v[200:203], v[172:175], a[120:123]\n\t"
    "v_exp_f32 v135, v135\n\t"
    "v_mfma_f32_16x16x32_bf16 a[116:119], v[204:207], v[168:171], a[116:119]\n\t"
    "v_exp_f32 v136, v136\n\t"
    "v_mfma_f32_16x16x32_bf16 a[124:127], v[204:207], v[172:175], a[124:127]\n\t"
    "v_exp_f32 v137, v137\n\t"
    "v_mfma_f32_16x16x32_bf16 a[136:139], v[208:211], v[168:171], a[136:139]\n\t"
    "v_exp_f32 v140, v140\n\t"
    "v_mfma_f32_16x16x32_bf16 a[140:143], v[208:211], v[172:175], a[140:143]\n\t"
    "v_exp_f32 v141, v141\n\t"
    "v_mfma_f32_32x32x16_bf16 v[144:159], a[216:219], a[180:183], v[80:95]\n\t"
    "ds_read_b64_tr_b16 v[184:185], v248 offset:2048\n\t"
    "ds_read_b64_tr_b16 v[186:187], v248 offset:2304\n\t"
    "v_exp_f32 v138, v138\n\t"
    "v_exp_f32 v139, v139\n\t"
    "v_mfma_f32_32x32x16_bf16 v[144:159], a[220:223], a[184:187], v[144:159]\n\t"
    "ds_read_b64_tr_b16 v[188:189], v249 offset:3072\n\t"
    "ds_read_b64_tr_b16 v[190:191], v249 offset:3328\n\t"
    "v_cvt_pk_bf16_f32 v160, v128, v129\n\t"
    "v_cvt_pk_bf16_f32 v164, v132, v133\n\t"
    "v_exp_f32_e64 v142, v142\n\t"
    "v_mfma_f32_32x32x16_bf16 v[144:159], a[224:227], a[188:191], v[144:159]\n\t"
    "ds_read_b64_tr_b16 v[192:193], v248 offset:4096\n\t"
    "ds_read_b64_tr_b16 v[194:195], v248 offset:4352\n\t"
    "v_exp_f32_e64 v143, v143\n\t"
    "v_cvt_pk_bf16_f32 v161, v130, v131\n\t"
    "v_mfma_f32_32x32x16_bf16 v[144:159], a[228:231], a[192:195], v[144:159]\n\t"
    "ds_read_b64_tr_b16 v[196:197], v249 offset:5120\n\t"
    "ds_read_b64_tr_b16 v[198:199], v249 offset:5376\n\t"
    "v_permlane16_swap_b32 v160, v164\n\t"
    "v_cvt_pk_bf16_f32 v165, v134, v135\n\t"
    "v_cvt_pk_bf16_f32 v162, v136, v137\n\t"
    "v_mfma_f32_32x32x16_bf16 v[144:159], a[232:235], a[196:199], v[144:159]\n\t"
    "ds_read_b64_tr_b16 v[200:201], v248 offset:6144\n\t"
    "ds_read_b64_tr_b16 v[202:203], v248 offset:6400\n\t"
    "v_cvt_pk_bf16_f32 v166, v140, v141\n\t"
    "v_permlane16_swap_b32 v161, v165\n\t"
    "v_mfma_f32_32x32x16_bf16 v[144:159], a[236:239], a[200:203], v[144:159]\n\t"
    "ds_read_b64_tr_b16 v[204:205], v249 offset:7168\n\t"
    "ds_read_b64_tr_b16 v[206:207], v249 offset:7424\n\t"
    "v_cvt_pk_bf16_f32 v163, v138, v139\n\t"
    "v_cvt_pk_bf16_f32 v167, v142, v143\n\t"
    "v_permlane16_swap_b32 v162, v166\n\t"
    "v_mfma_f32_32x32x16_bf16 v[144:159], a[240:243], a[204:207], v[144:159]\n\t"
    "ds_read_b64_tr_b16 v[208:209], v248 offset:8192\n\t"
    "ds_read_b64_tr_b16 v[210:211], v248 offset:8448\n\t"
    "v_permlane16_swap_b32 v163, v167\n\t"
    "v_mfma_f32_32x32x16_bf16 v[144:159], a[244:247], a[208:211], v[144:159]\n\t"
    "v_mfma_f32_32x32x16_bf16 v[144:159], a[248:251], a[212:215], v[144:159]\n\t"
    "s_waitcnt vmcnt(10)\n\t"
    "s_waitcnt lgkmcnt(0)\n\t"
    "s_waitcnt vmcnt(9)\n\t"
    "v_cmp_ne_u64 vcc, v[232:233], v[234:235]\n\t"
    "s_nop 4\n\t"
    "s_cbranch_vccnz PFS0_%=\n\t"
    "PFD0_%=:\n\t"
    "v_mfma_f32_16x16x32_bf16 a[0:3], v[176:179], v[160:163], a[0:3]\n\t"
    "ds_read_b128 a[216:219], v96 offset:0\n\t"
    "ds_read_b128 a[220:223], v97 offset:1024\n\t"
    "v_mfma_f32_16x16x32_bf16 a[8:11], v[176:179], v[164:167], a[8:11]\n\t"
    "ds_read_b128 a[224:227], v96 offset:2048\n\t"
    "ds_read_b128 a[228:231], v97 offset:3072\n\t"
    "v_mfma_f32_16x16x32_bf16 a[4:7], v[180:183], v[160:163], a[4:7]\n\t"
    "ds_read_b128 a[232:235], v96 offset:4096\n\t"
    "ds_read_b128 a[236:239], v97 offset:5120\n\t"
    "v_mfma_f32_16x16x32_bf16 a[12:15], v[180:183], v[164:167], a[12:15]\n\t"
    "ds_read_b128 a[240:243], v96 offset:6144\n\t"
    "ds_read_b128 a[244:247], v97 offset:7168\n\t"
    "v_mfma_f32_16x16x32_bf16 a[16:19], v[184:187], v[160:163], a[16:19]\n\t"
    "ds_read_b128 a[248:251], v96 offset:8192\n\t"
    "v_mfma_f32_16x16x32_bf16 a[24:27], v[184:187], v[164:167], a[24:27]\n\t"
    "s_mov_b32 m0, s40\n\t"
    "s_nop 0\n\t"
    "buffer_load_dwordx4 v117, s[80:83], s41 offen sc1 lds\n\t"
    "v_mfma_f32_16x16x32_bf16 a[20:23], v[188:191], v[160:163], a[20:23]\n\t"
    "v_exp_f32 v144, v144\n\t"
    "v_mfma_f32_16x16x32_bf16 a[28:31], v[188:191], v[164:167], a[28:31]\n\t"
    "s_mov_b32 m0, s42\n\t"
    "s_nop 0\n\t"
    "buffer_load_dwordx4 v117, s[80:83], s43 offen sc1 lds\n\t"
    "v_exp_f32 v145, v145\n\t"
    "v_mfma_f32_16x16x32_bf16 a[32:35], v[192:195], v[160:163], a[32:35]\n\t"
    "v_exp_f32 v148, v148\n\t"
    "v_mfma_f32_16x16x32_bf16 a[40:43], v[192:195], v[164:167], a[40:43]\n\t"
    "s_mov_b32 m0, s44\n\t"
    "s_nop 0\n\t"
    "buffer_load_dwordx4 v117, s[80:83], s45 offen sc1 lds\n\t"
    "v_exp_f32 v149, v149\n\t"
    "v_mfma_f32_16x16x32_bf16 a[36:39], v[196:199], v[160:163], a[36:39]\n\t"
    "v_exp_f32_e64 v146, v146\n\t"
    "v_mfma_f32_16x16x32_bf16 a[44:47], v[196:199], v[164:167], a[44:47]\n\t"
    "buffer_load_dwordx4 v[236:239], v117, s[80:83], s49 offen sc1\n\t"
    "v_exp_f32 v147, v147\n\t"
    "v_mfma_f32_16x16x32_bf16 a[48:51], v[200:203], v[160:163], a[48:51]\n\t"
    "v_exp_f32 v150, v150\n\t"
    "v_mfma_f32_16x16x32_bf16 a[56:59], v[200:203], v[164:167], a[56:59]\n\t"
    "v_exp_f32 v151, v151\n\t"
    "v_mfma_f32_16x16x32_bf16 a[52:55], v[204:207], v[160:163], a[52:55]\n\t"
    "v_exp_f32 v152, v152\n\t"
    "v_mfma_f32_16x16x32_bf16 a[60:63], v[204:207], v[164:167], a[60:63]\n\t"
    "v_exp_f32 v153, v153\n\t"
    "v_mfma_f32_16x16x32_bf16 a[128:131], v[208:211], v[160:163], a[128:131]\n\t"
    "v_exp_f32 v156, v156\n\t"
    "v_mfma_f32_16x16x32_bf16 a[132:135], v[208:211], v[164:167], a[132:135]\n\t"
    "s_waitcnt lgkmcnt(0)\n\t"
    "v_exp_f32 v157, v157\n\t"
    "v_mfma_f32_32x32x16_bf16 v[128:143], a[216:219], a[144:147], v[64:79]\n\t"
    "buffer_load_dwordx4 v[0:3], v117, s[80:83], s54 offen offset:0 sc1\n\t"
    "v_exp_f32 v154, v154\n\t"
    "v_exp_f32 v155, v155\n\t"
    "v_mfma_f32_32x32x16_bf16 v[128:143], a[220:223], a[148:151], v[128:143]\n\t"
    "buffer_load_dwordx4 v[4:7], v117, s[80:83], s54 offen offset:1024 sc1\n\t"
    "v_cvt_pk_bf16_f32 v168, v144, v145\n\t"
    "v_cvt_pk_bf16_f32 v172, v148, v149\n\t"
    "v_exp_f32 v158, v158\n\t"
    "v_mfma_f32_32x32x16_bf16 v[128:143], a[224:227], a[152:155], v[128:143]\n\t"
    "buffer_load_dwordx4 v[8:11], v117, s[80:83], s54 offen offset:2048 sc1\n\t"
    "v_exp_f32 v159, v159\n\t"
    "v_cvt_pk_bf16_f32 v169, v146, v147\n\t"
    "v_mfma_f32_32x32x16_bf16 v[128:143], a[228:231], a[156:159], v[128:143]\n\t"
    "buffer_load_dwordx4 v[12:15], v117, s[80:83], s54 offen offset:3072 sc1\n\t"
    "v_permlane16_swap_b32 v168, v172\n\t"
    "v_cvt_pk_bf16_f32 v173, v150, v151\n\t"
    "v_cvt_pk_bf16_f32 v170, v152, v153\n\t"
    "v_mfma_f32_32x32x16_bf16 v[128:143], a[232:235], a[160:163], v[128:143]\n\t"
    "buffer_load_dwordx2 v[16:17], v116, s[80:83], s73 offen sc1\n\t"
    "v_cvt_pk_bf16_f32 v174, v156, v157\n\t"
    "v_permlane16_swap_b32 v169, v173\n\t"
    "v_mfma_f32_32x32x16_bf16 v[128:143], a[236:239], a[164:167], v[128:143]\n\t"
    "buffer_load_dwordx4 v[18:21], v117, s[80:83], s76 offen offset:0 sc1\n\t"
    "v_cvt_pk_bf16_f32 v171, v154, v155\n\t"
    "v_cvt_pk_bf16_f32 v175, v158, v159\n\t"
    "v_permlane16_swap_b32 v170, v174\n\t"
    "v_mfma_f32_32x32x16_bf16 v[128:143], a[240:243], a[168:171], v[128:143]\n\t"
    "buffer_load_dwordx4 v[22:25], v117, s[80:83], s76 offen offset:1024 sc1\n\t"
    "v_permlane16_swap_b32 v171, v175\n\t"
    "v_mfma_f32_32x32x16_bf16 v[128:143], a[244:247], a[172:175], v[128:143]\n\t"
    "buffer_load_dwordx4 v[26:29], v117, s[80:83], s76 offen offset:2048 sc1\n\t"
    "v_mfma_f32_32x32x16_bf16 v[128:143], a[248:251], a[176:179], v[128:143]\n\t"
    "buffer_load_dwordx4 v[30:33], v117, s[80:83], s76 offen offset:3072 sc1\n\t"
    "v_mfma_f32_16x16x32_bf16 a[64:67], v[176:179], v[168:171], a[64:67]\n\t"
    "buffer_load_dwordx2 v[34:35], v116, s[80:83], s50 offen sc1\n\t"
    "v_mfma_f32_16x16x32_bf16 a[72:75], v[176:179], v[172:175], a[72:75]\n\t"
    "buffer_load_dwordx4 v[36:39], v117, s[80:83], s46 offen offset:0 sc1\n\t"
    "v_mfma_f32_16x16x32_bf16 a[68:71], v[180:183], v[168:171], a[68:71]\n\t"
    "ds_read_b64_tr_b16 v[176:177], v98 offset:0\n\t"
    "ds_read_b64_tr_b16 v[178:179], v98 offset:256\n\t"
    "v_mfma_f32_16x16x32_bf16 a[76:79], v[180:183], v[172:175], a[76:79]\n\t"
    "buffer_load_dwordx4 v[40:43], v117, s[80:83], s46 offen offset:1024 sc1\n\t"
    "v_mfma_f32_16x16x32_bf16 a[80:83], v[184:187], v[168:171], a[80:83]\n\t"
    "ds_read_b64_tr_b16 v[180:181], v99 offset:1024\n\t"
    "ds_read_b64_tr_b16 v[182:183], v99 offset:1280\n\t"
    "v_mfma_f32_16x16x32_bf16 a[88:91], v[184:187], v[172:175], a[88:91]\n\t"
    "buffer_load_dwordx4 v[44:47], v117, s[80:83], s46 offen offset:2048 sc1\n\t"
    "v_mfma_f32_16x16x32_bf16 a[84:87], v[188:191], v[168:171], a[84:87]\n\t"
    "v_exp_f32 v128, v128\n\t"
    "v_mfma_f32_16x16x32_bf16 a[92:95], v[188:191], v[172:175], a[92:95]\n\t"
    "v_exp_f32 v129, v129\n\t"
    "v_mfma_f32_16x16x32_bf16 a[96:99], v[192:195], v[168:171], a[96:99]\n\t"
    "v_exp_f32 v132, v132\n\t"
    "v_mfma_f32_16x16x32_bf16 a[104:107], v[192:195], v[172:175], a[104:107]\n\t"
    "v_exp_f32 v133, v133\n\t"
    "v_mfma_f32_16x16x32_bf16 a[100:103], v[196:199], v[168:171], a[100:103]\n\t"
    "v_exp_f32 v130, v130\n\t"
    "v_mfma_f32_16x16x32_bf16 a[108:111], v[196:199], v[172:175], a[108:111]\n\t"
    "v_exp_f32 v131, v131\n\t"
    "v_mfma_f32_16x16x32_bf16 a[112:115], v[200:203], v[168:171], a[112:115]\n\t"
    "v_exp_f32 v134, v134\n\t"
    "v_mfma_f32_16x16x32_bf16 a[120:123], v[200:203], v[172:175], a[120:123]\n\t"
    "v_exp_f32 v135, v135\n\t"
    "v_mfma_f32_16x16x32_bf16 a[116:119], v[204:207], v[168:171], a[116:119]\n\t"
    "v_exp_f32 v136, v136\n\t"
    "v_mfma_f32_16x16x32_bf16 a[124:127], v[204:207], v[172:175], a[124:127]\n\t"
    "v_exp_f32 v137, v137\n\t"
    "v_mfma_f32_16x16x32_bf16 a[136:139], v[208:211], v[168:171], a[136:139]\n\t"
    "v_exp_f32 v140, v140\n\t"
    "v_mfma_f32_16x16x32_bf16 a[140:143], v[208:211], v[172:175], a[140:143]\n\t"
    "v_exp_f32 v141, v141\n\t"
    "v_mfma_f32_32x32x16_bf16 v[144:159], a[216:219], a[180:183], v[80:95]\n\t"
    "ds_read_b64_tr_b16 v[184:185], v98 offset:2048\n\t"
    "ds_read_b64_tr_b16 v[186:187], v98 offset:2304\n\t"
    "v_exp_f32 v138, v138\n\t"
    "v_exp_f32 v139, v139\n\t"
    "v_mfma_f32_32x32x16_bf16 v[144:159], a[220:223], a[184:187], v[144:159]\n\t"
    "buffer_load_dwordx4 v[48:51], v117, s[80:83], s46 offen offset:3072 sc1\n\t"
    "ds_read_b64_tr_b16 v[188:189], v99 offset:3072\n\t"
    "ds_read_b64_tr_b16 v[190:191], v99 offset:3328\n\t"
    "v_cvt_pk_bf16_f32 v160, v128, v129\n\t"
    "v_cvt_pk_bf16_f32 v164, v132, v133\n\t"
    "v_exp_f32_e64 v142, v142\n\t"
    "v_mfma_f32_32x32x16_bf16 v[144:159], a[224:227], a[188:191], v[144:159]\n\t"
    "ds_read_b64_tr_b16 v[192:193], v98 offset:4096\n\t"
    "ds_read_b64_tr_b16 v[194:195], v98 offset:4352\n\t"
    "v_exp_f32_e64 v143, v143\n\t"
    "v_cvt_pk_bf16_f32 v161, v130, v131\n\t"
    "v_mfma_f32_32x32x16_bf16 v[144:159], a[228:231], a[192:195], v[144:159]\n\t"
    "buffer_load_dwordx2 v[52:53], v116, s[80:83], s47 offen sc1\n\t"
    "ds_read_b64_tr_b16 v[196:197], v99 offset:5120\n\t"
    "ds_read_b64_tr_b16 v[198:199], v99 offset:5376\n\t"
    "v_permlane16_swap_b32 v160, v164\n\t"
    "v_cvt_pk_bf16_f32 v165, v134, v135\n\t"
    "v_cvt_pk_bf16_f32 v162, v136, v137\n\t"
    "v_mfma_f32_32x32x16_bf16 v[144:159], a[232:235], a[196:199], v[144:159]\n\t"
    "ds_read_b64_tr_b16 v[200:201], v98 offset:6144\n\t"
    "ds_read_b64_tr_b16 v[202:203], v98 offset:6400\n\t"
    "v_cvt_pk_bf16_f32 v166, v140, v141\n\t"
    "v_permlane16_swap_b32 v161, v165\n\t"
    "v_mfma_f32_32x32x16_bf16 v[144:159], a[236:239], a[200:203], v[144:159]\n\t"
    "buffer_load_dwordx4 a[144:147], v117, s[80:83], s57 offen offset:0 sc1\n\t"
    "ds_read_b64_tr_b16 v[204:205], v99 offset:7168\n\t"
    "ds_read_b64_tr_b16 v[206:207], v99 offset:7424\n\t"
    "v_cvt_pk_bf16_f32 v163, v138, v139\n\t"
    "v_cvt_pk_bf16_f32 v167, v142, v143\n\t"
    "v_permlane16_swap_b32 v162, v166\n\t"
    "v_mfma_f32_32x32x16_bf16 v[144:159], a[240:243], a[204:207], v[144:159]\n\t"
    "buffer_load_dwordx4 a[148:151], v117, s[80:83], s57 offen offset:1024 sc1\n\t"
    "ds_read_b64_tr_b16 v[208:209], v98 offset:8192\n\t"
    "ds_read_b64_tr_b16 v[210:211], v98 offset:8448\n\t"
    "v_permlane16_swap_b32 v163, v167\n\t"
    "v_mfma_f32_32x32x16_bf16 v[144:159], a[244:247], a[208:211], v[144:159]\n\t"
    "buffer_load_dwordx4 a[152:155], v117, s[80:83], s57 offen offset:2048 sc1\n\t"
    "v_mfma_f32_32x32x16_bf16 v[144:159], a[248:251], a[212:215], v[144:159]\n\t"
    "s_waitcnt vmcnt(22)\n\t"
    "s_waitcnt lgkmcnt(0)\n\t"
    "buffer_load_dwordx4 a[156:159], v117, s[80:83], s57 offen offset:3072 sc1\n\t"
    "v_mfma_f32_16x16x32_bf16 a[16:19], v[184:187], v[160:163], a[16:19]\n\t"
    "ds_read_b128 a[216:219], v96 offset:36864\n\t"
    "ds_read_b128 a[220:223], v97 offset:37888\n\t"
    "v_mfma_f32_16x16x32_bf16 a[24:27], v[184:187], v[164:167], a[24:27]\n\t"
    "ds_read_b128 a[224:227], v96 offset:38912\n\t"
    "ds_read_b128 a[228:231], v97 offset:39936\n\t"
    "v_mfma_f32_16x16x32_bf16 a[20:23], v[188:191], v[160:163], a[20:23]\n\t"
    "ds_read_b128 a[232:235], v96 offset:40960\n\t"
    "ds_read_b128 a[236:239], v97 offset:41984\n\t"
    "v_mfma_f32_16x16x32_bf16 a[28:31], v[188:191], v[164:167], a[28:31]\n\t"
    "ds_read_b128 a[240:243], v96 offset:43008\n\t"
    "ds_read_b128 a[244:247], v97 offset:44032\n\t"
    "v_mfma_f32_16x16x32_bf16 a[48:51], v[200:203], v[160:163], a[48:51]\n\t"
    "ds_read_b128 a[248:251], v96 offset:45056\n\t"
    "v_mfma_f32_16x16x32_bf16 a[56:59], v[200:203], v[164:167], a[56:59]\n\t"
    "buffer_load_dwordx4 a[160:163], v117, s[80:83], s92 offen offset:0 sc1\n\t"
    "v_mfma_f32_16x16x32_bf16 a[52:55], v[204:207], v[160:163], a[52:55]\n\t"
    "v_exp_f32 v144, v144\n\t"
    "v_mfma_f32_16x16x32_bf16 a[60:63], v[204:207], v[164:167], a[60:63]\n\t"
    "v_exp_f32 v145, v145\n\t"
    "v_mfma_f32_16x16x32_bf16 a[128:131], v[208:211], v[160:163], a[128:131]\n\t"
    "v_exp_f32_e64 v148, v148\n\t"
    "v_mfma_f32_16x16x32_bf16 a[132:135], v[208:211], v[164:167], a[132:135]\n\t"
    "ds_write_b128 v106, a[16:19] offset:0\n\t"
    "v_exp_f32 v149, v149\n\t"
    "v_mfma_f32_16x16x32_bf16 a[32:35], v[192:195], v[160:163], a[32:35]\n\t"
    "v_exp_f32 v146, v146\n\t"
    "v_mfma_f32_16x16x32_bf16 a[40:43], v[192:195], v[164:167], a[40:43]\n\t"
    "v_exp_f32_e64 v147, v147\n\t"
    "v_mfma_f32_16x16x32_bf16 a[36:39], v[196:199], v[160:163], a[36:39]\n\t"
    "ds_write_b128 v106, a[20:23] offset:1024\n\t"
    "v_exp_f32 v150, v150\n\t"
    "v_mfma_f32_16x16x32_bf16 a[44:47], v[196:199], v[164:167], a[44:47]\n\t"
    "v_exp_f32 v151, v151\n\t"
    "v_mfma_f32_16x16x32_bf16 a[0:3], v[176:179], v[160:163], a[0:3]\n\t"
    "v_exp_f32_e64 v152, v152\n\t"
    "v_mfma_f32_16x16x32_bf16 a[8:11], v[176:179], v[164:167], a[8:11]\n\t"
    "ds_write_b128 v106, a[24:27] offset:2048\n\t"
    "v_exp_f32 v153, v153\n\t"
    "v_mfma_f32_16x16x32_bf16 a[4:7], v[180:183], v[160:163], a[4:7]\n\t"
    "v_exp_f32 v156, v156\n\t"
    "v_mfma_f32_16x16x32_bf16 a[12:15], v[180:183], v[164:167], a[12:15]\n\t"
    "s_waitcnt lgkmcnt(3)\n\t"
    "v_exp_f32 v157, v157\n\t"
    "v_mfma_f32_32x32x16_bf16 v[128:143], a[216:219], a[180:183], v[80:95]\n\t"
    "ds_write_b128 v106, a[28:31] offset:3072\n\t"
    "buffer_load_dwordx4 a[164:167], v117, s[80:83], s92 offen offset:1024 sc1\n\t"
    "v_exp_f32 v154, v154\n\t"
    "v_exp_f32 v155, v155\n\t"
    "v_mfma_f32_32x32x16_bf16 v[128:143], a[220:223], a[184:187], v[128:143]\n\t"
    "ds_write_b128 v107, a[48:51] offset:0\n\t"
    "v_cvt_pk_bf16_f32 v168, v144, v145\n\t"
    "v_cvt_pk_bf16_f32 v172, v148, v149\n\t"
    "v_mfma_f32_32x32x16_bf16 v[128:143], a[224:227], a[188:191], v[128:143]\n\t"
    "ds_write_b128 v107, a[52:55] offset:1024\n\t"
    "buffer_load_dwordx4 a[168:171], v117, s[80:83], s92 offen offset:2048 sc1\n\t"
    "v_exp_f32 v158, v158\n\t"
    "v_exp_f32 v159, v159\n\t"
    "v_mfma_f32_32x32x16_bf16 v[128:143], a[228:231], a[192:195], v[128:143]\n\t"
    "ds_write_b128 v107, a[56:59] offset:2048\n\t"
    "v_cvt_pk_bf16_f32 v169, v146, v147\n\t"
    "v_permlane16_swap_b32 v168, v172\n\t"
    "v_mfma_f32_32x32x16_bf16 v[128:143], a[232:235], a[196:199], v[128:143]\n\t"
    "ds_write_b128 v107, a[60:63] offset:3072\n\t"
    "buffer_load_dwordx4 a[172:175], v117, s[80:83], s92 offen offset:3072 sc1\n\t"
    "v_cvt_pk_bf16_f32 v173, v150, v151\n\t"
    "v_cvt_pk_bf16_f32 v170, v152, v153\n\t"
    "v_mfma_f32_32x32x16_bf16 v[128:143], a[236:239], a[200:203], v[128:143]\n\t"
    "ds_write_b128 v108, a[128:131] offset:0\n\t"
    "v_cvt_pk_bf16_f32 v174, v156, v157\n\t"
    "v_permlane16_swap_b32 v169, v173\n\t"
    "v_mfma_f32_32x32x16_bf16 v[128:143], a[240:243], a[204:207], v[128:143]\n\t"
    "ds_write_b128 v108, a[132:135] offset:1024\n\t"
    "buffer_load_dwordx4 a[176:179], v117, s[80:83], s93 offen offset:0 sc1\n\t"
    "v_cvt_pk_bf16_f32 v171, v154, v155\n\t"
    "v_cvt_pk_bf16_f32 v175, v158, v159\n\t"
    "v_mfma_f32_32x32x16_bf16 v[128:143], a[244:247], a[208:211], v[128:143]\n\t"
    "v_permlane16_swap_b32 v170, v174\n\t"
    "v_permlane16_swap_b32 v171, v175\n\t"
    "v_mfma_f32_32x32x16_bf16 v[128:143], a[248:251], a[212:215], v[128:143]\n\t"
    "s_nop 0\n\t"
    "v_mfma_f32_16x16x32_bf16 a[64:67], v[176:179], v[168:171], a[64:67]\n\t"
    "v_mfma_f32_16x16x32_bf16 a[72:75], v[176:179], v[172:175], a[72:75]\n\t"
    "buffer_load_dwordx4 a[216:219], v117, s[80:83], s94 offen offset:0 sc1\n\t"
    "v_mfma_f32_16x16x32_bf16 a[68:71], v[180:183], v[168:171], a[68:71]\n\t"
    "ds_read_b64_tr_b16 v[176:177], v98 offset:36864\n\t"
    "ds_read_b64_tr_b16 v[178:179], v98 offset:37120\n\t"
    "v_mfma_f32_16x16x32_bf16 a[76:79], v[180:183], v[172:175], a[76:79]\n\t"
    "buffer_load_dwordx4 a[220:223], v117, s[80:83], s94 offen offset:1024 sc1\n\t"
    "v_mfma_f32_16x16x32_bf16 a[80:83], v[184:187], v[168:171], a[80:83]\n\t"
    "ds_read_b64_tr_b16 v[180:181], v99 offset:37888\n\t"
    "ds_read_b64_tr_b16 v[182:183], v99 offset:38144\n\t"
    "v_exp_f32 v128, v128\n\t"
    "v_mfma_f32_16x16x32_bf16 a[88:91], v[184:187], v[172:175], a[88:91]\n\t"
    "v_exp_f32 v129, v129\n\t"
    "v_exp_f32 v132, v132\n\t"
    "v_mfma_f32_16x16x32_bf16 a[84:87], v[188:191], v[168:171], a[84:87]\n\t"
    "ds_read_b64_tr_b16 v[184:185], v98 offset:38912\n\t"
    "ds_read_b64_tr_b16 v[186:187], v98 offset:39168\n\t"
    "v_exp_f32 v133, v133\n\t"
    "v_mfma_f32_16x16x32_bf16 a[92:95], v[188:191], v[172:175], a[92:95]\n\t"
    "v_exp_f32 v130, v130\n\t"
    "v_exp_f32_e64 v131, v131\n\t"
    "v_mfma_f32_16x16x32_bf16 a[96:99], v[192:195], v[168:171], a[96:99]\n\t"
    "ds_read_b64_tr_b16 v[188:189], v99 offset:39936\n\t"
    "ds_read_b64_tr_b16 v[190:191], v99 offset:40192\n\t"
    "v_exp_f32 v134, v134\n\t"
    "v_mfma_f32_16x16x32_bf16 a[104:107], v[192:195], v[172:175], a[104:107]\n\t"
    "v_exp_f32 v135, v135\n\t"
    "v_exp_f32_e64 v136, v136\n\t"
    "v_mfma_f32_16x16x32_bf16 a[100:103], v[196:199], v[168:171], a[100:103]\n\t"
    "ds_read_b64_tr_b16 v[192:193], v98 offset:40960\n\t"
    "ds_read_b64_tr_b16 v[194:195], v98 offset:41216\n\t"
    "v_exp_f32 v137, v137\n\t"
    "v_mfma_f32_16x16x32_bf16 a[108:111], v[196:199], v[172:175], a[108:111]\n\t"
    "v_exp_f32 v140, v140\n\t"
    "v_exp_f32_e64 v141, v141\n\t"
    "v_mfma_f32_16x16x32_bf16 a[112:115], v[200:203], v[168:171], a[112:115]\n\t"
    "ds_read_b64_tr_b16 v[196:197], v99 offset:41984\n\t"
    "ds_read_b64_tr_b16 v[198:199], v99 offset:42240\n\t"
    "v_exp_f32 v138, v138\n\t"
    "v_mfma_f32_16x16x32_bf16 a[120:123], v[200:203], v[172:175], a[120:123]\n\t"
    "v_exp_f32 v139, v139\n\t"
    "v_cvt_pk_bf16_f32 v160, v128, v129\n\t"
    "v_mfma_f32_16x16x32_bf16 a[116:119], v[204:207], v[168:171], a[116:119]\n\t"
    "ds_read_b64_tr_b16 v[200:201], v98 offset:43008\n\t"
    "ds_read_b64_tr_b16 v[202:203], v98 offset:43264\n\t"
    "v_cvt_pk_bf16_f32 v164, v132, v133\n\t"
    "v_mfma_f32_16x16x32_bf16 a[124:127], v[204:207], v[172:175], a[124:127]\n\t"
    "v_exp_f32 v142, v142\n\t"
    "v_exp_f32 v143, v143\n\t"
    "v_mfma_f32_16x16x32_bf16 a[136:139], v[208:211], v[168:171], a[136:139]\n\t"
    "ds_read_b64_tr_b16 v[204:205], v99 offset:44032\n\t"
    "ds_read_b64_tr_b16 v[206:207], v99 offset:44288\n\t"
    "v_cvt_pk_bf16_f32 v161, v130, v131\n\t"
    "v_mfma_f32_16x16x32_bf16 a[140:143], v[208:211], v[172:175], a[140:143]\n\t"
    "v_permlane16_swap_b32 v160, v164\n\t"
    "v_cvt_pk_bf16_f32 v165, v134, v135\n\t"
    "ds_read_b64_tr_b16 v[208:209], v98 offset:45056\n\t"
    "ds_read_b64_tr_b16 v[210:211], v98 offset:45312\n\t"
    "s_waitcnt lgkmcnt(10)\n\t"
    "ds_write_b128 v106, a[32:35] offset:36864\n\t"
    "v_cvt_pk_bf16_f32 v162, v136, v137\n\t"
    "v_cvt_pk_bf16_f32 v166, v140, v141\n\t"
    "v_permlane16_swap_b32 v161, v165\n\t"
    "v_cvt_pk_bf16_f32 v163, v138, v139\n\t"
    "ds_write_b128 v106, a[36:39] offset:37888\n\t"
    "v_cvt_pk_bf16_f32 v167, v142, v143\n\t"
    "v_permlane16_swap_b32 v162, v166\n\t"
    "s_nop 0\n\t"
    "v_permlane16_swap_b32 v163, v167\n\t"
    "s_nop 1\n\t"
    "v_mfma_f32_16x16x32_bf16 a[64:67], v[176:179], v[160:163], a[64:67]\n\t"
    "ds_write_b128 v106, a[40:43] offset:38912\n\t"
    "v_mfma_f32_16x16x32_bf16 a[72:75], v[176:179], v[164:167], a[72:75]\n\t"
    "buffer_load_dwordx4 a[224:227], v117, s[80:83], s94 offen offset:2048 sc1\n\t"
    "v_mfma_f32_16x16x32_bf16 a[68:71], v[180:183], v[160:163], a[68:71]\n\t"
    "v_mfma_f32_16x16x32_bf16 a[76:79], v[180:183], v[164:167], a[76:79]\n\t"
    "ds_write_b128 v106, a[44:47] offset:39936\n\t"
    "v_mfma_f32_16x16x32_bf16 a[80:83], v[184:187], v[160:163], a[80:83]\n\t"
    "buffer_load_dwordx4 a[228:231], v117, s[80:83], s94 offen offset:3072 sc1\n\t"
    "v_mfma_f32_16x16x32_bf16 a[88:91], v[184:187], v[164:167], a[88:91]\n\t"
    "v_mfma_f32_16x16x32_bf16 a[84:87], v[188:191], v[160:163], a[84:87]\n\t"
    "buffer_load_dwordx2 a[232:233], v116, s[80:83], s78 offen sc1\n\t"
    "v_mfma_f32_16x16x32_bf16 a[92:95], v[188:191], v[164:167], a[92:95]\n\t"
    "v_accvgpr_read_b32 v144, a0\n\t"
    "v_accvgpr_read_b32 v145, a1\n\t"
    "s_waitcnt lgkmcnt(12)\n\t"
    "v_mfma_f32_16x16x32_bf16 a[96:99], v[192:195], v[160:163], a[96:99]\n\t"
    "v_accvgpr_read_b32 v146, a2\n\t"
    "v_accvgpr_read_b32 v147, a3\n\t"
    "v_mfma_f32_16x16x32_bf16 a[104:107], v[192:195], v[164:167], a[104:107]\n\t"
    "v_accvgpr_read_b32 v148, a4\n\t"
    "v_accvgpr_read_b32 v149, a5\n\t"
    "s_waitcnt lgkmcnt(10)\n\t"
    "v_mfma_f32_16x16x32_bf16 a[100:103], v[196:199], v[160:163], a[100:103]\n\t"
    "v_accvgpr_read_b32 v150, a6\n\t"
    "v_accvgpr_read_b32 v151, a7\n\t"
    "v_mfma_f32_16x16x32_bf16 a[108:111], v[196:199], v[164:167], a[108:111]\n\t"
    "v_accvgpr_read_b32 v152, a8\n\t"
    "v_accvgpr_read_b32 v153, a9\n\t"
    "s_waitcnt lgkmcnt(8)\n\t"
    "v_mfma_f32_16x16x32_bf16 a[112:115], v[200:203], v[160:163], a[112:115]\n\t"
    "v_accvgpr_read_b32 v154, a10\n\t"
    "v_accvgpr_read_b32 v155, a11\n\t"
    "v_mfma_f32_16x16x32_bf16 a[120:123], v[200:203], v[164:167], a[120:123]\n\t"
    "v_accvgpr_read_b32 v156, a12\n\t"
    "v_accvgpr_read_b32 v157, a13\n\t"
    "s_waitcnt lgkmcnt(6)\n\t"
    "v_mfma_f32_16x16x32_bf16 a[116:119], v[204:207], v[160:163], a[116:119]\n\t"
    "v_accvgpr_read_b32 v158, a14\n\t"
    "v_accvgpr_read_b32 v159, a15\n\t"
    "v_mfma_f32_16x16x32_bf16 a[124:127], v[204:207], v[164:167], a[124:127]\n\t"
    "s_waitcnt lgkmcnt(4)\n\t"
    "v_mfma_f32_16x16x32_bf16 a[136:139], v[208:211], v[160:163], a[136:139]\n\t"
    "v_mfma_f32_16x16x32_bf16 a[140:143], v[208:211], v[164:167], a[140:143]\n\t"
    "s_waitcnt vmcnt(30)\n\t"
    "s_waitcnt lgkmcnt(0)\n\t"
    "s_branch SJ_%=\n\t"
    "SH_%=:\n\t"
    "v_mfma_f32_32x32x16_bf16 v[128:143], a[216:219], a[144:147], v[64:79]\n\t"
    "buffer_load_dwordx2 v[232:233], v115, s[80:83], 0 offen sc1\n\t"
    "buffer_load_dwordx4 v101, s[64:67], s72 offen offset:0 sc1 lds\n\t"
    "v_exp_f32 v154, v154\n\t"
    "v_exp_f32_e64 v155, v155\n\t"
    "v_mfma_f32_32x32x16_bf16 v[128:143], a[220:223], a[148:151], v[128:143]\n\t"
    "buffer_load_dwordx4 v101, s[64:67], s72 offen offset:1024 sc1 lds\n\t"
    "v_cvt_pk_bf16_f32 v168, v144, v145\n\t"
    "v_cvt_pk_bf16_f32 v172, v148, v149\n\t"
    "v_exp_f32 v158, v158\n\t"
    "v_mfma_f32_32x32x16_bf16 v[128:143], a[224:227], a[152:155], v[128:143]\n\t"
    "buffer_load_dwordx4 v102, s[64:67], s72 offen offset:2048 sc1 lds\n\t"
    "v_exp_f32 v159, v159\n\t"
    "v_cvt_pk_bf16_f32 v169, v146, v147\n\t"
    "v_mfma_f32_32x32x16_bf16 v[128:143], a[228:231], a[156:159], v[128:143]\n\t"
    "buffer_load_dwordx4 v102, s[64:67], s72 offen offset:3072 sc1 lds\n\t"
    "s_add_u32 m0, s61, 4096\n\t"
    "v_permlane16_swap_b32 v168, v172\n\t"
    "v_cvt_pk_bf16_f32 v173, v150, v151\n\t"
    "v_cvt_pk_bf16_f32 v170, v152, v153\n\t"
    "v_mfma_f32_32x32x16_bf16 v[128:143], a[232:235], a[160:163], v[128:143]\n\t"
    "buffer_load_dwordx4 v103, s[64:67], s72 offen offset:0 sc1 lds\n\t"
    "v_cvt_pk_bf16_f32 v174, v156, v157\n\t"
    "v_permlane16_swap_b32 v169, v173\n\t"
    "v_mfma_f32_32x32x16_bf16 v[128:143], a[236:239], a[164:167], v[128:143]\n\t"
    "buffer_load_dwordx4 v103, s[64:67], s72 offen offset:1024 sc1 lds\n\t"
    "v_cvt_pk_bf16_f32 v171, v154, v155\n\t"
    "v_cvt_pk_bf16_f32 v175, v158, v159\n\t"
    "v_permlane16_swap_b32 v170, v174\n\t"
    "v_mfma_f32_32x32x16_bf16 v[128:143], a[240:243], a[168:171], v[128:143]\n\t"
    "buffer_load_dwordx4 v104, s[64:67], s72 offen offset:2048 sc1 lds\n\t"
    "v_permlane16_swap_b32 v171, v175\n\t"
    "v_mfma_f32_32x32x16_bf16 v[128:143], a[244:247], a[172:175], v[128:143]\n\t"
    "buffer_load_dwordx4 v104, s[64:67], s72 offen offset:3072 sc1 lds\n\t"
    "s_add_u32 m0, s61, 8192\n\t"
    "v_mfma_f32_32x32x16_bf16 v[128:143], a[248:251], a[176:179], v[128:143]\n\t"
    "buffer_load_dwordx4 v105, s[64:67], s72 offen offset:0 sc1 lds\n\t"
    "v_mfma_f32_16x16x32_bf16 a[64:67], v[176:179], v[168:171], a[64:67]\n\t"
    "v_mfma_f32_16x16x32_bf16 a[72:75], v[176:179], v[172:175], a[72:75]\n\t"
    "v_mfma_f32_16x16x32_bf16 a[68:71], v[180:183], v[168:171], a[68:71]\n\t"
    "ds_read_b64_tr_b16 v[176:177], v98 offset:36864\n\t"
    "ds_read_b64_tr_b16 v[178:179], v98 offset:37120\n\t"
    "v_mfma_f32_16x16x32_bf16 a[76:79], v[180:183], v[172:175], a[76:79]\n\t"
    "v_mfma_f32_16x16x32_bf16 a[80:83], v[184:187], v[168:171], a[80:83]\n\t"
    "ds_read_b64_tr_b16 v[180:181], v99 offset:37888\n\t"
    "ds_read_b64_tr_b16 v[182:183], v99 offset:38144\n\t"
    "v_mfma_f32_16x16x32_bf16 a[88:91], v[184:187], v[172:175], a[88:91]\n\t"
    "v_mfma_f32_16x16x32_bf16 a[84:87], v[188:191], v[168:171], a[84:87]\n\t"
    "v_exp_f32 v128, v128\n\t"
    "v_mfma_f32_16x16x32_bf16 a[92:95], v[188:191], v[172:175], a[92:95]\n\t"
    "v_exp_f32 v129, v129\n\t"
    "v_mfma_f32_16x16x32_bf16 a[96:99], v[192:195], v[168:171], a[96:99]\n\t"
    "v_exp_f32 v132, v132\n\t"
    "v_mfma_f32_16x16x32_bf16 a[104:107], v[192:195], v[172:175], a[104:107]\n\t"
    "v_exp_f32 v133, v133\n\t"
    "v_mfma_f32_16x16x32_bf16 a[100:103], v[196:199], v[168:171], a[100:103]\n\t"
    "v_exp_f32 v130, v130\n\t"
    "v_mfma_f32_16x16x32_bf16 a[108:111], v[196:199], v[172:175], a[108:111]\n\t"
    "v_exp_f32 v131, v131\n\t"
    "v_mfma_f32_16x16x32_bf16 a[112:115], v[200:203], v[168:171], a[112:115]\n\t"
    "v_exp_f32 v134, v134\n\t"
    "v_mfma_f32_16x16x32_bf16 a[120:123], v[200:203], v[172:175], a[120:123]\n\t"
    "v_exp_f32 v135, v135\n\t"
    "v_mfma_f32_16x16x32_bf16 a[116:119], v[204:207], v[168:171], a[116:119]\n\t"
    "v_exp_f32 v136, v136\n\t"
    "v_mfma_f32_16x16x32_bf16 a[124:127], v[204:207], v[172:175], a[124:127]\n\t"
    "v_exp_f32 v137, v137\n\t"
    "v_mfma_f32_16x16x32_bf16 a[136:139], v[208:211], v[168:171], a[136:139]\n\t"
    "v_exp_f32 v140, v140\n\t"
    "v_mfma_f32_16x16x32_bf16 a[140:143], v[208:211], v[172:175], a[140:143]\n\t"
    "v_exp_f32 v141, v141\n\t"
    "v_mfma_f32_32x32x16_bf16 v[144:159], a[216:219], a[180:183], v[80:95]\n\t"
    "ds_read_b64_tr_b16 v[184:185], v98 offset:38912\n\t"
    "ds_read_b64_tr_b16 v[186:187], v98 offset:39168\n\t"
    "v_exp_f32 v138, v138\n\t"
    "v_exp_f32 v139, v139\n\t"
    "v_mfma_f32_32x32x16_bf16 v[144:159], a[220:223], a[184:187], v[144:159]\n\t"
    "ds_read_b64_tr_b16 v[188:189], v99 offset:39936\n\t"
    "ds_read_b64_tr_b16 v[190:191], v99 offset:40192\n\t"
    "v_cvt_pk_bf16_f32 v160, v128, v129\n\t"
    "v_cvt_pk_bf16_f32 v164, v132, v133\n\t"
    "v_exp_f32_e64 v142, v142\n\t"
    "v_mfma_f32_32x32x16_bf16 v[144:159], a[224:227], a[188:191], v[144:159]\n\t"
    "ds_read_b64_tr_b16 v[192:193], v98 offset:40960\n\t"
    "ds_read_b64_tr_b16 v[194:195], v98 offset:41216\n\t"
    "v_exp_f32_e64 v143, v143\n\t"
    "v_cvt_pk_bf16_f32 v161, v130, v131\n\t"
    "v_mfma_f32_32x32x16_bf16 v[144:159], a[228:231], a[192:195], v[144:159]\n\t"
    "ds_read_b64_tr_b16 v[196:197], v99 offset:41984\n\t"
    "ds_read_b64_tr_b16 v[198:199], v99 offset:42240\n\t"
    "v_permlane16_swap_b32 v160, v164\n\t"
    "v_cvt_pk_bf16_f32 v165, v134, v135\n\t"
    "v_cvt_pk_bf16_f32 v162, v136, v137\n\t"
    "v_mfma_f32_32x32x16_bf16 v[144:159], a[232:235], a[196:199], v[144:159]\n\t"
    "ds_read_b64_tr_b16 v[200:201], v98 offset:43008\n\t"
    "ds_read_b64_tr_b16 v[202:203], v98 offset:43264\n\t"
    "v_cvt_pk_bf16_f32 v166, v140, v141\n\t"
    "v_permlane16_swap_b32 v161, v165\n\t"
    "v_mfma_f32_32x32x16_bf16 v[144:159], a[236:239], a[200:203], v[144:159]\n\t"
    "ds_read_b64_tr_b16 v[204:205], v99 offset:44032\n\t"
    "ds_read_b64_tr_b16 v[206:207], v99 offset:44288\n\t"
    "v_cvt_pk_bf16_f32 v163, v138, v139\n\t"
    "v_cvt_pk_bf16_f32 v167, v142, v143\n\t"
    "v_permlane16_swap_b32 v162, v166\n\t"
    "v_mfma_f32_32x32x16_bf16 v[144:159], a[240:243], a[204:207], v[144:159]\n\t"
    "ds_read_b64_tr_b16 v[208:209], v98 offset:45056\n\t"
    "ds_read_b64_tr_b16 v[210:211], v98 offset:45312\n\t"
    "v_permlane16_swap_b32 v163, v167\n\t"
    "v_mfma_f32_32x32x16_bf16 v[144:159], a[244:247], a[208:211], v[144:159]\n\t"
    "v_mfma_f32_32x32x16_bf16 v[144:159], a[248:251], a[212:215], v[144:159]\n\t"
    "s_waitcnt vmcnt(12)\n\t"
    "s_waitcnt lgkmcnt(0)\n\t"
    "s_waitcnt vmcnt(9)\n\t"
    "v_cmp_ne_u64 vcc, v[232:233], v[234:235]\n\t"
    "s_nop 4\n\t"
    "s_cbranch_vccnz PFS1_%=\n\t"
    "PFD1_%=:\n\t"
    "v_mfma_f32_16x16x32_bf16 a[0:3], v[176:179], v[160:163], a[0:3]\n\t"
    "ds_read_b128 a[216:219], v246 offset:0\n\t"
    "ds_read_b128 a[220:223], v247 offset:1024\n\t"
    "v_mfma_f32_16x16x32_bf16 a[8:11], v[176:179], v[164:167], a[8:11]\n\t"
    "ds_read_b128 a[224:227], v246 offset:2048\n\t"
    "ds_read_b128 a[228:231], v247 offset:3072\n\t"
    "v_mfma_f32_16x16x32_bf16 a[4:7], v[180:183], v[160:163], a[4:7]\n\t"
    "ds_read_b128 a[232:235], v246 offset:4096\n\t"
    "ds_read_b128 a[236:239], v247 offset:5120\n\t"
    "v_mfma_f32_16x16x32_bf16 a[12:15], v[180:183], v[164:167], a[12:15]\n\t"
    "ds_read_b128 a[240:243], v246 offset:6144\n\t"
    "ds_read_b128 a[244:247], v247 offset:7168\n\t"
    "v_mfma_f32_16x16x32_bf16 a[16:19], v[184:187], v[160:163], a[16:19]\n\t"
    "ds_read_b128 a[248:251], v246 offset:8192\n\t"
    "v_mfma_f32_16x16x32_bf16 a[24:27], v[184:187], v[164:167], a[24:27]\n\t"
    "s_mov_b32 m0, s40\n\t"
    "s_nop 0\n\t"
    "buffer_load_dwordx4 v117, s[80:83], s41 offen sc1 lds\n\t"
    "v_mfma_f32_16x16x32_bf16 a[20:23], v[188:191], v[160:163], a[20:23]\n\t"
    "v_exp_f32 v144, v144\n\t"
    "v_mfma_f32_16x16x32_bf16 a[28:31], v[188:191], v[164:167], a[28:31]\n\t"
    "s_mov_b32 m0, s42\n\t"
    "s_nop 0\n\t"
    "buffer_load_dwordx4 v117, s[80:83], s43 offen sc1 lds\n\t"
    "v_exp_f32 v145, v145\n\t"
    "v_mfma_f32_16x16x32_bf16 a[32:35], v[192:195], v[160:163], a[32:35]\n\t"
    "v_exp_f32 v148, v148\n\t"
    "v_mfma_f32_16x16x32_bf16 a[40:43], v[192:195], v[164:167], a[40:43]\n\t"
    "s_mov_b32 m0, s44\n\t"
    "s_nop 0\n\t"
    "buffer_load_dwordx4 v117, s[80:83], s45 offen sc1 lds\n\t"
    "v_exp_f32 v149, v149\n\t"
    "v_mfma_f32_16x16x32_bf16 a[36:39], v[196:199], v[160:163], a[36:39]\n\t"
    "v_exp_f32_e64 v146, v146\n\t"
    "v_mfma_f32_16x16x32_bf16 a[44:47], v[196:199], v[164:167], a[44:47]\n\t"
    "buffer_load_dwordx4 v[236:239], v117, s[80:83], s49 offen sc1\n\t"
    "v_exp_f32 v147, v147\n\t"
    "v_mfma_f32_16x16x32_bf16 a[48:51], v[200:203], v[160:163], a[48:51]\n\t"
    "v_exp_f32 v150, v150\n\t"
    "v_mfma_f32_16x16x32_bf16 a[56:59], v[200:203], v[164:167], a[56:59]\n\t"
    "v_exp_f32 v151, v151\n\t"
    "v_mfma_f32_16x16x32_bf16 a[52:55], v[204:207], v[160:163], a[52:55]\n\t"
    "v_exp_f32 v152, v152\n\t"
    "v_mfma_f32_16x16x32_bf16 a[60:63], v[204:207], v[164:167], a[60:63]\n\t"
    "v_exp_f32 v153, v153\n\t"
    "v_mfma_f32_16x16x32_bf16 a[128:131], v[208:211], v[160:163], a[128:131]\n\t"
    "v_exp_f32 v156, v156\n\t"
    "v_mfma_f32_16x16x32_bf16 a[132:135], v[208:211], v[164:167], a[132:135]\n\t"
    "s_waitcnt lgkmcnt(0)\n\t"
    "v_exp_f32 v157, v157\n\t"
    "v_mfma_f32_32x32x16_bf16 v[128:143], a[216:219], a[144:147], v[64:79]\n\t"
    "buffer_load_dwordx4 v[0:3], v117, s[80:83], s54 offen offset:0 sc1\n\t"
    "v_exp_f32 v154, v154\n\t"
    "v_exp_f32 v155, v155\n\t"
    "v_mfma_f32_32x32x16_bf16 v[128:143], a[220:223], a[148:151], v[128:143]\n\t"
    "buffer_load_dwordx4 v[4:7], v117, s[80:83], s54 offen offset:1024 sc1\n\t"
    "v_cvt_pk_bf16_f32 v168, v144, v145\n\t"
    "v_cvt_pk_bf16_f32 v172, v148, v149\n\t"
    "v_exp_f32 v158, v158\n\t"
    "v_mfma_f32_32x32x16_bf16 v[128:143], a[224:227], a[152:155], v[128:143]\n\t"
    "buffer_load_dwordx4 v[8:11], v117, s[80:83], s54 offen offset:2048 sc1\n\t"
    "v_exp_f32 v159, v159\n\t"
    "v_cvt_pk_bf16_f32 v169, v146, v147\n\t"
    "v_mfma_f32_32x32x16_bf16 v[128:143], a[228:231], a[156:159], v[128:143]\n\t"
    "buffer_load_dwordx4 v[12:15], v117, s[80:83], s54 offen offset:3072 sc1\n\t"
    "v_permlane16_swap_b32 v168, v172\n\t"
    "v_cvt_pk_bf16_f32 v173, v150, v151\n\t"
    "v_cvt_pk_bf16_f32 v170, v152, v153\n\t"
    "v_mfma_f32_32x32x16_bf16 v[128:143], a[232:235], a[160:163], v[128:143]\n\t"
    "buffer_load_dwordx2 v[16:17], v116, s[80:83], s73 offen sc1\n\t"
    "v_cvt_pk_bf16_f32 v174, v156, v157\n\t"
    "v_permlane16_swap_b32 v169, v173\n\t"
    "v_mfma_f32_32x32x16_bf16 v[128:143], a[236:239], a[164:167], v[128:143]\n\t"
    "buffer_load_dwordx4 v[18:21], v117, s[80:83], s76 offen offset:0 sc1\n\t"
    "v_cvt_pk_bf16_f32 v171, v154, v155\n\t"
    "v_cvt_pk_bf16_f32 v175, v158, v159\n\t"
    "v_permlane16_swap_b32 v170, v174\n\t"
    "v_mfma_f32_32x32x16_bf16 v[128:143], a[240:243], a[168:171], v[128:143]\n\t"
    "buffer_load_dwordx4 v[22:25], v117, s[80:83], s76 offen offset:1024 sc1\n\t"
    "v_permlane16_swap_b32 v171, v175\n\t"
    "v_mfma_f32_32x32x16_bf16 v[128:143], a[244:247], a[172:175], v[128:143]\n\t"
    "buffer_load_dwordx4 v[26:29], v117, s[80:83], s76 offen offset:2048 sc1\n\t"
    "v_mfma_f32_32x32x16_bf16 v[128:143], a[248:251], a[176:179], v[128:143]\n\t"
    "buffer_load_dwordx4 v[30:33], v117, s[80:83], s76 offen offset:3072 sc1\n\t"
    "v_mfma_f32_16x16x32_bf16 a[64:67], v[176:179], v[168:171], a[64:67]\n\t"
    "buffer_load_dwordx2 v[34:35], v116, s[80:83], s50 offen sc1\n\t"
    "v_mfma_f32_16x16x32_bf16 a[72:75], v[176:179], v[172:175], a[72:75]\n\t"
    "buffer_load_dwordx4 v[36:39], v117, s[80:83], s46 offen offset:0 sc1\n\t"
    "v_mfma_f32_16x16x32_bf16 a[68:71], v[180:183], v[168:171], a[68:71]\n\t"
    "ds_read_b64_tr_b16 v[176:177], v248 offset:0\n\t"
    "ds_read_b64_tr_b16 v[178:179], v248 offset:256\n\t"
    "v_mfma_f32_16x16x32_bf16 a[76:79], v[180:183], v[172:175], a[76:79]\n\t"
    "buffer_load_dwordx4 v[40:43], v117, s[80:83], s46 offen offset:1024 sc1\n\t"
    "v_mfma_f32_16x16x32_bf16 a[80:83], v[184:187], v[168:171], a[80:83]\n\t"
    "ds_read_b64_tr_b16 v[180:181], v249 offset:1024\n\t"
    "ds_read_b64_tr_b16 v[182:183], v249 offset:1280\n\t"
    "v_mfma_f32_16x16x32_bf16 a[88:91], v[184:187], v[172:175], a[88:91]\n\t"
    "buffer_load_dwordx4 v[44:47], v117, s[80:83], s46 offen offset:2048 sc1\n\t"
    "v_mfma_f32_16x16x32_bf16 a[84:87], v[188:191], v[168:171], a[84:87]\n\t"
    "v_exp_f32 v128, v128\n\t"
    "v_mfma_f32_16x16x32_bf16 a[92:95], v[188:191], v[172:175], a[92:95]\n\t"
    "v_exp_f32 v129, v129\n\t"
    "v_mfma_f32_16x16x32_bf16 a[96:99], v[192:195], v[168:171], a[96:99]\n\t"
    "v_exp_f32 v132, v132\n\t"
    "v_mfma_f32_16x16x32_bf16 a[104:107], v[192:195], v[172:175], a[104:107]\n\t"
    "v_exp_f32 v133, v133\n\t"
    "v_mfma_f32_16x16x32_bf16 a[100:103], v[196:199], v[168:171], a[100:103]\n\t"
    "v_exp_f32 v130, v130\n\t"
    "v_mfma_f32_16x16x32_bf16 a[108:111], v[196:199], v[172:175], a[108:111]\n\t"
    "v_exp_f32 v131, v131\n\t"
    "v_mfma_f32_16x16x32_bf16 a[112:115], v[200:203], v[168:171], a[112:115]\n\t"
    "v_exp_f32 v134, v134\n\t"
    "v_mfma_f32_16x16x32_bf16 a[120:123], v[200:203], v[172:175], a[120:123]\n\t"
    "v_exp_f32 v135, v135\n\t"
    "v_mfma_f32_16x16x32_bf16 a[116:119], v[204:207], v[168:171], a[116:119]\n\t"
    "v_exp_f32 v136, v136\n\t"
    "v_mfma_f32_16x16x32_bf16 a[124:127], v[204:207], v[172:175], a[124:127]\n\t"
    "v_exp_f32 v137, v137\n\t"
    "v_mfma_f32_16x16x32_bf16 a[136:139], v[208:211], v[168:171], a[136:139]\n\t"
    "v_exp_f32 v140, v140\n\t"
    "v_mfma_f32_16x16x32_bf16 a[140:143], v[208:211], v[172:175], a[140:143]\n\t"
    "v_exp_f32 v141, v141\n\t"
    "v_mfma_f32_32x32x16_bf16 v[144:159], a[216:219], a[180:183], v[80:95]\n\t"
    "ds_read_b64_tr_b16 v[184:185], v248 offset:2048\n\t"
    "ds_read_b64_tr_b16 v[186:187], v248 offset:2304\n\t"
    "v_exp_f32 v138, v138\n\t"
    "v_exp_f32 v139, v139\n\t"
    "v_mfma_f32_32x32x16_bf16 v[144:159], a[220:223], a[184:187], v[144:159]\n\t"
    "buffer_load_dwordx4 v[48:51], v117, s[80:83], s46 offen offset:3072 sc1\n\t"
    "ds_read_b64_tr_b16 v[188:189], v249 offset:3072\n\t"
    "ds_read_b64_tr_b16 v[190:191], v249 offset:3328\n\t"
    "v_cvt_pk_bf16_f32 v160, v128, v129\n\t"
    "v_cvt_pk_bf16_f32 v164, v132, v133\n\t"
    "v_exp_f32_e64 v142, v142\n\t"
    "v_mfma_f32_32x32x16_bf16 v[144:159], a[224:227], a[188:191], v[144:159]\n\t"
    "ds_read_b64_tr_b16 v[192:193], v248 offset:4096\n\t"
    "ds_read_b64_tr_b16 v[194:195], v248 offset:4352\n\t"
    "v_exp_f32_e64 v143, v143\n\t"
    "v_cvt_pk_bf16_f32 v161, v130, v131\n\t"
    "v_mfma_f32_32x32x16_bf16 v[144:159], a[228:231], a[192:195], v[144:159]\n\t"
    "buffer_load_dwordx2 v[52:53], v116, s[80:83], s47 offen sc1\n\t"
    "ds_read_b64_tr_b16 v[196:197], v249 offset:5120\n\t"
    "ds_read_b64_tr_b16 v[198:199], v249 offset:5376\n\t"
    "v_permlane16_swap_b32 v160, v164\n\t"
    "v_cvt_pk_bf16_f32 v165, v134, v135\n\t"
    "v_cvt_pk_bf16_f32 v162, v136, v137\n\t"
    "v_mfma_f32_32x32x16_bf16 v[144:159], a[232:235], a[196:199], v[144:159]\n\t"
    "ds_read_b64_tr_b16 v[200:201], v248 offset:6144\n\t"
    "ds_read_b64_tr_b16 v[202:203], v248 offset:6400\n\t"
    "v_cvt_pk_bf16_f32 v166, v140, v141\n\t"
    "v_permlane16_swap_b32 v161, v165\n\t"
    "v_mfma_f32_32x32x16_bf16 v[144:159], a[236:239], a[200:203], v[144:159]\n\t"
    "buffer_load_dwordx4 a[144:147], v117, s[80:83], s57 offen offset:0 sc1\n\t"
    "ds_read_b64_tr_b16 v[204:205], v249 offset:7168\n\t"
    "ds_read_b64_tr_b16 v[206:207], v249 offset:7424\n\t"
    "v_cvt_pk_bf16_f32 v163, v138, v139\n\t"
    "v_cvt_pk_bf16_f32 v167, v142, v143\n\t"
    "v_permlane16_swap_b32 v162, v166\n\t"
    "v_mfma_f32_32x32x16_bf16 v[144:159], a[240:243], a[204:207], v[144:159]\n\t"
    "buffer_load_dwordx4 a[148:151], v117, s[80:83], s57 offen offset:1024 sc1\n\t"
    "ds_read_b64_tr_b16 v[208:209], v248 offset:8192\n\t"
    "ds_read_b64_tr_b16 v[210:211], v248 offset:8448\n\t"
    "v_permlane16_swap_b32 v163, v167\n\t"
    "v_mfma_f32_32x32x16_bf16 v[144:159], a[244:247], a[208:211], v[144:159]\n\t"
    "buffer_load_dwordx4 a[152:155], v117, s[80:83], s57 offen offset:2048 sc1\n\t"
    "v_mfma_f32_32x32x16_bf16 v[144:159], a[248:251], a[212:215], v[144:159]\n\t"
    "s_waitcnt vmcnt(22)\n\t"
    "s_waitcnt lgkmcnt(0)\n\t"
    "buffer_load_dwordx4 a[156:159], v117, s[80:83], s57 offen offset:3072 sc1\n\t"
    "v_mfma_f32_16x16x32_bf16 a[32:35], v[192:195], v[160:163], a[32:35]\n\t"
    "ds_read_b128 a[216:219], v96 offset:0\n\t"
    "ds_read_b128 a[220:223], v97 offset:1024\n\t"
    "v_mfma_f32_16x16x32_bf16 a[40:43], v[192:195], v[164:167], a[40:43]\n\t"
    "ds_read_b128 a[224:227], v96 offset:2048\n\t"
    "ds_read_b128 a[228:231], v97 offset:3072\n\t"
    "v_mfma_f32_16x16x32_bf16 a[36:39], v[196:199], v[160:163], a[36:39]\n\t"
    "ds_read_b128 a[232:235], v96 offset:4096\n\t"
    "ds_read_b128 a[236:239], v97 offset:5120\n\t"
    "v_mfma_f32_16x16x32_bf16 a[44:47], v[196:199], v[164:167], a[44:47]\n\t"
    "ds_read_b128 a[240:243], v96 offset:6144\n\t"
    "ds_read_b128 a[244:247], v97 offset:7168\n\t"
    "v_mfma_f32_16x16x32_bf16 a[48:51], v[200:203], v[160:163], a[48:51]\n\t"
    "ds_read_b128 a[248:251], v96 offset:8192\n\t"
    "v_mfma_f32_16x16x32_bf16 a[56:59], v[200:203], v[164:167], a[56:59]\n\t"
    "buffer_load_dwordx4 a[160:163], v117, s[80:83], s92 offen offset:0 sc1\n\t"
    "v_mfma_f32_16x16x32_bf16 a[52:55], v[204:207], v[160:163], a[52:55]\n\t"
    "v_exp_f32 v144, v144\n\t"
    "v_mfma_f32_16x16x32_bf16 a[60:63], v[204:207], v[164:167], a[60:63]\n\t"
    "v_exp_f32 v145, v145\n\t"
    "v_mfma_f32_16x16x32_bf16 a[128:131], v[208:211], v[160:163], a[128:131]\n\t"
    "v_exp_f32_e64 v148, v148\n\t"
    "v_mfma_f32_16x16x32_bf16 a[132:135], v[208:211], v[164:167], a[132:135]\n\t"
    "ds_write_b128 v106, a[32:35] offset:36864\n\t"
    "v_exp_f32 v149, v149\n\t"
    "v_mfma_f32_16x16x32_bf16 a[16:19], v[184:187], v[160:163], a[16:19]\n\t"
    "v_exp_f32 v146, v146\n\t"
    "v_mfma_f32_16x16x32_bf16 a[24:27], v[184:187], v[164:167], a[24:27]\n\t"
    "v_exp_f32_e64 v147, v147\n\t"
    "v_mfma_f32_16x16x32_bf16 a[20:23], v[188:191], v[160:163], a[20:23]\n\t"
    "ds_write_b128 v106, a[36:39] offset:37888\n\t"
    "v_exp_f32 v150, v150\n\t"
    "v_mfma_f32_16x16x32_bf16 a[28:31], v[188:191], v[164:167], a[28:31]\n\t"
    "v_exp_f32 v151, v151\n\t"
    "v_mfma_f32_16x16x32_bf16 a[0:3], v[176:179], v[160:163], a[0:3]\n\t"
    "v_exp_f32_e64 v152, v152\n\t"
    "v_mfma_f32_16x16x32_bf16 a[8:11], v[176:179], v[164:167], a[8:11]\n\t"
    "ds_write_b128 v106, a[40:43] offset:38912\n\t"
    "v_exp_f32 v153, v153\n\t"
    "v_mfma_f32_16x16x32_bf16 a[4:7], v[180:183], v[160:163], a[4:7]\n\t"
    "v_exp_f32 v156, v156\n\t"
    "v_mfma_f32_16x16x32_bf16 a[12:15], v[180:183], v[164:167], a[12:15]\n\t"
    "s_waitcnt lgkmcnt(3)\n\t"
    "v_exp_f32 v157, v157\n\t"
    "v_mfma_f32_32x32x16_bf16 v[128:143], a[216:219], a[180:183], v[80:95]\n\t"
    "ds_write_b128 v106, a[44:47] offset:39936\n\t"
    "buffer_load_dwordx4 a[164:167], v117, s[80:83], s92 offen offset:1024 sc1\n\t"
    "v_exp_f32 v154, v154\n\t"
    "v_exp_f32 v155, v155\n\t"
    "v_mfma_f32_32x32x16_bf16 v[128:143], a[220:223], a[184:187], v[128:143]\n\t"
    "ds_write_b128 v107, a[48:51] offset:0\n\t"
    "v_cvt_pk_bf16_f32 v168, v144, v145\n\t"
    "v_cvt_pk_bf16_f32 v172, v148, v149\n\t"
    "v_mfma_f32_32x32x16_bf16 v[128:143], a[224:227], a[188:191], v[128:143]\n\t"
    "ds_write_b128 v107, a[52:55] offset:1024\n\t"
    "buffer_load_dwordx4 a[168:171], v117, s[80:83], s92 offen offset:2048 sc1\n\t"
    "v_exp_f32 v158, v158\n\t"
    "v_exp_f32 v159, v159\n\t"
    "v_mfma_f32_32x32x16_bf16 v[128:143], a[228:231], a[192:195], v[128:143]\n\t"
    "ds_write_b128 v107, a[56:59] offset:2048\n\t"
    "v_cvt_pk_bf16_f32 v169, v146, v147\n\t"
    "v_permlane16_swap_b32 v168, v172\n\t"
    "v_mfma_f32_32x32x16_bf16 v[128:143], a[232:235], a[196:199], v[128:143]\n\t"
    "ds_write_b128 v107, a[60:63] offset:3072\n\t"
    "buffer_load_dwordx4 a[172:175], v117, s[80:83], s92 offen offset:3072 sc1\n\t"
    "v_cvt_pk_bf16_f32 v173, v150, v151\n\t"
    "v_cvt_pk_bf16_f32 v170, v152, v153\n\t"
    "v_mfma_f32_32x32x16_bf16 v[128:143], a[236:239], a[200:203], v[128:143]\n\t"
    "ds_write_b128 v108, a[128:131] offset:0\n\t"
    "v_cvt_pk_bf16_f32 v174, v156, v157\n\t"
    "v_permlane16_swap_b32 v169, v173\n\t"
    "v_mfma_f32_32x32x16_bf16 v[128:143], a[240:243], a[204:207], v[128:143]\n\t"
    "ds_write_b128 v108, a[132:135] offset:1024\n\t"
    "buffer_load_dwordx4 a[176:179], v117, s[80:83], s93 offen offset:0 sc1\n\t"
    "v_cvt_pk_bf16_f32 v171, v154, v155\n\t"
    "v_cvt_pk_bf16_f32 v175, v158, v159\n\t"
    "v_mfma_f32_32x32x16_bf16 v[128:143], a[244:247], a[208:211], v[128:143]\n\t"
    "v_permlane16_swap_b32 v170, v174\n\t"
    "v_permlane16_swap_b32 v171, v175\n\t"
    "v_mfma_f32_32x32x16_bf16 v[128:143], a[248:251], a[212:215], v[128:143]\n\t"
    "s_nop 0\n\t"
    "v_mfma_f32_16x16x32_bf16 a[64:67], v[176:179], v[168:171], a[64:67]\n\t"
    "v_mfma_f32_16x16x32_bf16 a[72:75], v[176:179], v[172:175], a[72:75]\n\t"
    "buffer_load_dwordx4 a[216:219], v117, s[80:83], s94 offen offset:0 sc1\n\t"
    "v_mfma_f32_16x16x32_bf16 a[68:71], v[180:183], v[168:171], a[68:71]\n\t"
    "ds_read_b64_tr_b16 v[176:177], v98 offset:0\n\t"
    "ds_read_b64_tr_b16 v[178:179], v98 offset:256\n\t"
    "v_mfma_f32_16x16x32_bf16 a[76:79], v[180:183], v[172:175], a[76:79]\n\t"
    "buffer_load_dwordx4 a[220:223], v117, s[80:83], s94 offen offset:1024 sc1\n\t"
    "v_mfma_f32_16x16x32_bf16 a[80:83], v[184:187], v[168:171], a[80:83]\n\t"
    "ds_read_b64_tr_b16 v[180:181], v99 offset:1024\n\t"
    "ds_read_b64_tr_b16 v[182:183], v99 offset:1280\n\t"
    "v_exp_f32 v128, v128\n\t"
    "v_mfma_f32_16x16x32_bf16 a[88:91], v[184:187], v[172:175], a[88:91]\n\t"
    "v_exp_f32 v129, v129\n\t"
    "v_exp_f32 v132, v132\n\t"
    "v_mfma_f32_16x16x32_bf16 a[84:87], v[188:191], v[168:171], a[84:87]\n\t"
    "ds_read_b64_tr_b16 v[184:185], v98 offset:2048\n\t"
    "ds_read_b64_tr_b16 v[186:187], v98 offset:2304\n\t"
    "v_exp_f32 v133, v133\n\t"
    "v_mfma_f32_16x16x32_bf16 a[92:95], v[188:191], v[172:175], a[92:95]\n\t"
    "v_exp_f32 v130, v130\n\t"
    "v_exp_f32_e64 v131, v131\n\t"
    "v_mfma_f32_16x16x32_bf16 a[96:99], v[192:195], v[168:171], a[96:99]\n\t"
    "ds_read_b64_tr_b16 v[188:189], v99 offset:3072\n\t"
    "ds_read_b64_tr_b16 v[190:191], v99 offset:3328\n\t"
    "v_exp_f32 v134, v134\n\t"
    "v_mfma_f32_16x16x32_bf16 a[104:107], v[192:195], v[172:175], a[104:107]\n\t"
    "v_exp_f32 v135, v135\n\t"
    "v_exp_f32_e64 v136, v136\n\t"
    "v_mfma_f32_16x16x32_bf16 a[100:103], v[196:199], v[168:171], a[100:103]\n\t"
    "ds_read_b64_tr_b16 v[192:193], v98 offset:4096\n\t"
    "ds_read_b64_tr_b16 v[194:195], v98 offset:4352\n\t"
    "v_exp_f32 v137, v137\n\t"
    "v_mfma_f32_16x16x32_bf16 a[108:111], v[196:199], v[172:175], a[108:111]\n\t"
    "v_exp_f32 v140, v140\n\t"
    "v_exp_f32_e64 v141, v141\n\t"
    "v_mfma_f32_16x16x32_bf16 a[112:115], v[200:203], v[168:171], a[112:115]\n\t"
    "ds_read_b64_tr_b16 v[196:197], v99 offset:5120\n\t"
    "ds_read_b64_tr_b16 v[198:199], v99 offset:5376\n\t"
    "v_exp_f32 v138, v138\n\t"
    "v_mfma_f32_16x16x32_bf16 a[120:123], v[200:203], v[172:175], a[120:123]\n\t"
    "v_exp_f32 v139, v139\n\t"
    "v_cvt_pk_bf16_f32 v160, v128, v129\n\t"
    "v_mfma_f32_16x16x32_bf16 a[116:119], v[204:207], v[168:171], a[116:119]\n\t"
    "ds_read_b64_tr_b16 v[200:201], v98 offset:6144\n\t"
    "ds_read_b64_tr_b16 v[202:203], v98 offset:6400\n\t"
    "v_cvt_pk_bf16_f32 v164, v132, v133\n\t"
    "v_mfma_f32_16x16x32_bf16 a[124:127], v[204:207], v[172:175], a[124:127]\n\t"
    "v_exp_f32 v142, v142\n\t"
    "v_exp_f32 v143, v143\n\t"
    "v_mfma_f32_16x16x32_bf16 a[136:139], v[208:211], v[168:171], a[136:139]\n\t"
    "ds_read_b64_tr_b16 v[204:205], v99 offset:7168\n\t"
    "ds_read_b64_tr_b16 v[206:207], v99 offset:7424\n\t"
    "v_cvt_pk_bf16_f32 v161, v130, v131\n\t"
    "v_mfma_f32_16x16x32_bf16 a[140:143], v[208:211], v[172:175], a[140:143]\n\t"
    "v_permlane16_swap_b32 v160, v164\n\t"
    "v_cvt_pk_bf16_f32 v165, v134, v135\n\t"
    "ds_read_b64_tr_b16 v[208:209], v98 offset:8192\n\t"
    "ds_read_b64_tr_b16 v[210:211], v98 offset:8448\n\t"
    "s_waitcnt lgkmcnt(10)\n\t"
    "ds_write_b128 v106, a[16:19] offset:0\n\t"
    "v_cvt_pk_bf16_f32 v162, v136, v137\n\t"
    "v_cvt_pk_bf16_f32 v166, v140, v141\n\t"
    "v_permlane16_swap_b32 v161, v165\n\t"
    "v_cvt_pk_bf16_f32 v163, v138, v139\n\t"
    "ds_write_b128 v106, a[20:23] offset:1024\n\t"
    "v_cvt_pk_bf16_f32 v167, v142, v143\n\t"
    "v_permlane16_swap_b32 v162, v166\n\t"
    "s_nop 0\n\t"
    "v_permlane16_swap_b32 v163, v167\n\t"
    "s_nop 1\n\t"
    "v_mfma_f32_16x16x32_bf16 a[64:67], v[176:179], v[160:163], a[64:67]\n\t"
    "ds_write_b128 v106, a[24:27] offset:2048\n\t"
    "v_mfma_f32_16x16x32_bf16 a[72:75], v[176:179], v[164:167], a[72:75]\n\t"
    "buffer_load_dwordx4 a[224:227], v117, s[80:83], s94 offen offset:2048 sc1\n\t"
    "v_mfma_f32_16x16x32_bf16 a[68:71], v[180:183], v[160:163], a[68:71]\n\t"
    "v_mfma_f32_16x16x32_bf16 a[76:79], v[180:183], v[164:167], a[76:79]\n\t"
    "ds_write_b128 v106, a[28:31] offset:3072\n\t"
    "v_mfma_f32_16x16x32_bf16 a[80:83], v[184:187], v[160:163], a[80:83]\n\t"
    "buffer_load_dwordx4 a[228:231], v117, s[80:83], s94 offen offset:3072 sc1\n\t"
    "v_mfma_f32_16x16x32_bf16 a[88:91], v[184:187], v[164:167], a[88:91]\n\t"
    "v_mfma_f32_16x16x32_bf16 a[84:87], v[188:191], v[160:163], a[84:87]\n\t"
    "buffer_load_dwordx2 a[232:233], v116, s[80:83], s78 offen sc1\n\t"
    "v_mfma_f32_16x16x32_bf16 a[92:95], v[188:191], v[164:167], a[92:95]\n\t"
    "v_accvgpr_read_b32 v144, a0\n\t"
    "v_accvgpr_read_b32 v145, a1\n\t"
    "s_waitcnt lgkmcnt(12)\n\t"
    "v_mfma_f32_16x16x32_bf16 a[96:99], v[192:195], v[160:163], a[96:99]\n\t"
    "v_accvgpr_read_b32 v146, a2\n\t"
    "v_accvgpr_read_b32 v147, a3\n\t"
    "v_mfma_f32_16x16x32_bf16 a[104:107], v[192:195], v[164:167], a[104:107]\n\t"
    "v_accvgpr_read_b32 v148, a4\n\t"
    "v_accvgpr_read_b32 v149, a5\n\t"
    "s_waitcnt lgkmcnt(10)\n\t"
    "v_mfma_f32_16x16x32_bf16 a[100:103], v[196:199], v[160:163], a[100:103]\n\t"
    "v_accvgpr_read_b32 v150, a6\n\t"
    "v_accvgpr_read_b32 v151, a7\n\t"
    "v_mfma_f32_16x16x32_bf16 a[108:111], v[196:199], v[164:167], a[108:111]\n\t"
    "v_accvgpr_read_b32 v152, a8\n\t"
    "v_accvgpr_read_b32 v153, a9\n\t"
    "s_waitcnt lgkmcnt(8)\n\t"
    "v_mfma_f32_16x16x32_bf16 a[112:115], v[200:203], v[160:163], a[112:115]\n\t"
    "v_accvgpr_read_b32 v154, a10\n\t"
    "v_accvgpr_read_b32 v155, a11\n\t"
    "v_mfma_f32_16x16x32_bf16 a[120:123], v[200:203], v[164:167], a[120:123]\n\t"
    "v_accvgpr_read_b32 v156, a12\n\t"
    "v_accvgpr_read_b32 v157, a13\n\t"
    "s_waitcnt lgkmcnt(6)\n\t"
    "v_mfma_f32_16x16x32_bf16 a[116:119], v[204:207], v[160:163], a[116:119]\n\t"
    "v_accvgpr_read_b32 v158, a14\n\t"
    "v_accvgpr_read_b32 v159, a15\n\t"
    "v_mfma_f32_16x16x32_bf16 a[124:127], v[204:207], v[164:167], a[124:127]\n\t"
    "s_waitcnt lgkmcnt(4)\n\t"
    "v_mfma_f32_16x16x32_bf16 a[136:139], v[208:211], v[160:163], a[136:139]\n\t"
    "v_mfma_f32_16x16x32_bf16 a[140:143], v[208:211], v[164:167], a[140:143]\n\t"
    "s_waitcnt vmcnt(30)\n\t"
    "s_waitcnt lgkmcnt(0)\n\t"
    "SJ_%=:\n\t"
    "s_barrier\n\t"
    "ds_read_b128 v[128:131], v109 offset:0\n\t"
    "ds_read_b128 v[160:163], v110 offset:0\n\t"
    "ds_read_b128 v[176:179], v111 offset:0\n\t"
    "ds_read_b128 v[132:135], v109 offset:1024\n\t"
    "ds_read_b128 v[164:167], v110 offset:1024\n\t"
    "ds_read_b128 v[180:183], v111 offset:1024\n\t"
    "ds_read_b128 v[136:139], v109 offset:2048\n\t"
    "ds_read_b128 v[168:171], v110 offset:2048\n\t"
    "ds_read_b128 v[184:187], v111 offset:2048\n\t"
    "ds_read_b128 v[140:143], v109 offset:3072\n\t"
    "ds_read_b128 v[172:175], v110 offset:3072\n\t"
    "ds_read_b128 v[188:191], v111 offset:3072\n\t"
    "ds_read_b128 v[192:195], v112 offset:0\n\t"
    "ds_read_b128 v[196:199], v112 offset:4096\n\t"
    "ds_read_b128 v[200:203], v112 offset:8192\n\t"
    "ds_read_b128 v[204:207], v112 offset:12288\n\t"
    "s_waitcnt lgkmcnt(14)\n\t"
    "v_pk_add_f32 v[144:145], v[144:145], v[128:129]\n\t"
    "v_pk_add_f32 v[146:147], v[146:147], v[130:131]\n\t"
    "v_pk_add_f32 v[144:145], v[144:145], v[160:161]\n\t"
    "v_pk_add_f32 v[146:147], v[146:147], v[162:163]\n\t"
    "s_waitcnt lgkmcnt(13)\n\t"
    "v_pk_add_f32 v[144:145], v[144:145], v[176:177]\n\t"
    "v_pk_add_f32 v[146:147], v[146:147], v[178:179]\n\t"
    "v_cvt_pk_bf16_f32 v208, v144, v145\n\t"
    "v_cvt_pk_bf16_f32 v209, v146, v147\n\t"
    "s_waitcnt lgkmcnt(12)\n\t"
    "v_pk_add_f32 v[148:149], v[148:149], v[132:133]\n\t"
    "v_pk_add_f32 v[150:151], v[150:151], v[134:135]\n\t"
    "s_waitcnt lgkmcnt(11)\n\t"
    "v_pk_add_f32 v[148:149], v[148:149], v[164:165]\n\t"
    "v_pk_add_f32 v[150:151], v[150:151], v[166:167]\n\t"
    "s_waitcnt lgkmcnt(10)\n\t"
    "v_pk_add_f32 v[148:149], v[148:149], v[180:181]\n\t"
    "v_pk_add_f32 v[150:151], v[150:151], v[182:183]\n\t"
    "v_cvt_pk_bf16_f32 v210, v148, v149\n\t"
    "v_cvt_pk_bf16_f32 v211, v150, v151\n\t"
    "buffer_store_dwordx4 v[208:211], v113, s[80:83], s51 offen offset:0 sc1\n\t"
    "s_waitcnt lgkmcnt(9)\n\t"
    "v_pk_add_f32 v[152:153], v[152:153], v[136:137]\n\t"
    "v_pk_add_f32 v[154:155], v[154:155], v[138:139]\n\t"
    "s_waitcnt lgkmcnt(8)\n\t"
    "v_pk_add_f32 v[152:153], v[152:153], v[168:169]\n\t"
    "v_pk_add_f32 v[154:155], v[154:155], v[170:171]\n\t"
    "s_waitcnt lgkmcnt(7)\n\t"
    "v_pk_add_f32 v[152:153], v[152:153], v[184:185]\n\t"
    "v_pk_add_f32 v[154:155], v[154:155], v[186:187]\n\t"
    "v_cvt_pk_bf16_f32 v212, v152, v153\n\t"
    "v_cvt_pk_bf16_f32 v213, v154, v155\n\t"
    "s_waitcnt lgkmcnt(6)\n\t"
    "v_pk_add_f32 v[156:157], v[156:157], v[140:141]\n\t"
    "v_pk_add_f32 v[158:159], v[158:159], v[142:143]\n\t"
    "s_waitcnt lgkmcnt(5)\n\t"
    "v_pk_add_f32 v[156:157], v[156:157], v[172:173]\n\t"
    "v_pk_add_f32 v[158:159], v[158:159], v[174:175]\n\t"
    "s_waitcnt lgkmcnt(4)\n\t"
    "v_pk_add_f32 v[156:157], v[156:157], v[188:189]\n\t"
    "v_pk_add_f32 v[158:159], v[158:159], v[190:191]\n\t"
    "v_cvt_pk_bf16_f32 v214, v156, v157\n\t"
    "v_cvt_pk_bf16_f32 v215, v158, v159\n\t"
    "buffer_store_dwordx4 v[212:215], v113, s[80:83], s51 offen offset:1024 sc1\n\t"
    "s_waitcnt lgkmcnt(2)\n\t"
    "v_pk_add_f32 v[192:193], v[192:193], v[196:197]\n\t"
    "v_pk_add_f32 v[194:195], v[194:195], v[198:199]\n\t"
    "s_waitcnt lgkmcnt(0)\n\t"
    "v_pk_add_f32 v[200:201], v[200:201], v[204:205]\n\t"
    "v_pk_add_f32 v[202:203], v[202:203], v[206:207]\n\t"
    "v_pk_add_f32 v[192:193], v[192:193], v[200:201]\n\t"
    "v_pk_add_f32 v[194:195], v[194:195], v[202:203]\n\t"
    "v_cvt_pk_bf16_f32 v216, v192, v193\n\t"
    "v_cvt_pk_bf16_f32 v217, v194, v195\n\t"
    "buffer_store_dwordx2 v[216:217], v114, s[80:83], s51 offen sc1\n\t"
    "s_add_u32 s72, s58, 4096\n\t"
    "s_add_u32 s73, s58, 8192\n\t"
    "s_add_u32 s76, s59, 4096\n\t"
    "ds_read_b64 a[0:1], v123 offset:0\n\t"
    "ds_read_b64 a[26:27], v123 offset:512\n\t"
    "ds_read_b64 v[160:161], v124 offset:0\n\t"
    "ds_read_b64 a[2:3], v123 offset:1216\n\t"
    "ds_read_b64 a[28:29], v123 offset:1728\n\t"
    "ds_read_b64 v[162:163], v124 offset:1216\n\t"
    "ds_read_b64 a[4:5], v123 offset:2432\n\t"
    "ds_read_b64 a[30:31], v123 offset:2944\n\t"
    "ds_read_b64 v[164:165], v124 offset:2432\n\t"
    "ds_read_b64 a[6:7], v123 offset:3648\n\t"
    "ds_read_b64 a[32:33], v123 offset:4160\n\t"
    "ds_read_b64 v[166:167], v124 offset:3648\n\t"
    "ds_read_b64 a[8:9], v123 offset:4864\n\t"
    "ds_read_b64 a[34:35], v123 offset:5376\n\t"
    "ds_read_b64 v[168:169], v124 offset:4864\n\t"
    "ds_read_b64 a[10:11], v123 offset:6080\n\t"
    "ds_read_b64 a[36:37], v123 offset:6592\n\t"
    "ds_read_b64 v[170:171], v124 offset:6080\n\t"
    "ds_read_b64 a[12:13], v123 offset:7296\n\t"
    "ds_read_b64 a[38:39], v123 offset:7808\n\t"
    "ds_read_b64 v[172:173], v124 offset:7296\n\t"
    "ds_read_b64 a[14:15], v123 offset:8512\n\t"
    "ds_read_b64 a[40:41], v123 offset:9024\n\t"
    "ds_read_b64 v[174:175], v124 offset:8512\n\t"
    "ds_read_b64 a[16:17], v123 offset:9728\n\t"
    "ds_read_b64 a[42:43], v123 offset:10240\n\t"
    "ds_read_b64 v[176:177], v124 offset:9728\n\t"
    "ds_read_b64 a[18:19], v123 offset:10944\n\t"
    "ds_read_b64 a[44:45], v123 offset:11456\n\t"
    "ds_read_b64 a[20:21], v123 offset:12160\n\t"
    "ds_read_b64 a[46:47], v123 offset:12672\n\t"
    "ds_read_b64 a[22:23], v123 offset:13376\n\t"
    "ds_read_b64 a[48:49], v123 offset:13888\n\t"
    "ds_read_b64 a[24:25], v123 offset:14592\n\t"
    "ds_read_b64 a[50:51], v123 offset:15104\n\t"
    "ds_read_b64 v[178:179], v123 offset:19456\n\t"
    "ds_read_b64 v[180:181], v123 offset:24320\n\t"
    "ds_read_b64 v[182:183], v123 offset:19968\n\t"
    "ds_read_b64 v[184:185], v123 offset:24832\n\t"
    "ds_read_b64 v[186:187], v124 offset:19456\n\t"
    "v_lshlrev_b32 v128, 16, v54\n\t"
    "v_and_b32 v129, 0xffff0000, v54\n\t"
    "v_lshlrev_b32 v130, 16, v55\n\t"
    "v_and_b32 v131, 0xffff0000, v55\n\t"
    "v_lshlrev_b32 v132, 16, v56\n\t"
    "v_and_b32 v133, 0xffff0000, v56\n\t"
    "v_lshlrev_b32 v134, 16, v57\n\t"
    "v_and_b32 v135, 0xffff0000, v57\n\t"
    "v_lshlrev_b32 v136, 16, v58\n\t"
    "v_and_b32 v137, 0xffff0000, v58\n\t"
    "v_lshlrev_b32 v138, 16, v59\n\t"
    "v_and_b32 v139, 0xffff0000, v59\n\t"
    "v_lshlrev_b32 v140, 16, v60\n\t"
    "v_and_b32 v141, 0xffff0000, v60\n\t"
    "v_lshlrev_b32_e64 v142, 16, v61\n\t"
    "v_and_b32 v143, 0xffff0000, v61\n\t"
    "s_waitcnt vmcnt(31) lgkmcnt(14)\n\t"
    "v_mfma_f32_4x4x4_16b_bf16 v[188:191], v[0:1], a[0:1], 0\n\t"
    "s_waitcnt vmcnt(26)\n\t"
    "v_mfma_f32_4x4x4_16b_bf16 v[196:199], v[18:19], a[26:27], 0\n\t"
    "s_waitcnt vmcnt(21)\n\t"
    "v_mfma_f32_4x4x4_16b_bf16 v[204:207], v[36:37], v[160:161], 0\n\t"
    "v_mfma_f32_4x4x4_16b_bf16 v[192:195], v[0:1], a[8:9], 0\n\t"
    "v_mfma_f32_4x4x4_16b_bf16 v[200:203], v[18:19], a[34:35], 0\n\t"
    "ds_write_b128 v106, a[80:83] offset:4096\n\t"
    "buffer_load_dwordx4 v[64:67], v117, s[80:83], s58 offen offset:0 sc1\n\t"
    "v_mfma_f32_4x4x4_16b_bf16 v[188:191], v[2:3], a[2:3], v[188:191]\n\t"
    "v_mfma_f32_4x4x4_16b_bf16 v[196:199], v[20:21], a[28:29], v[196:199]\n\t"
    "v_mfma_f32_4x4x4_16b_bf16 v[204:207], v[38:39], v[162:163], v[204:207]\n\t"
    "v_mfma_f32_4x4x4_16b_bf16 v[192:195], v[2:3], a[10:11], v[192:195]\n\t"
    "v_mfma_f32_4x4x4_16b_bf16 v[200:203], v[20:21], a[36:37], v[200:203]\n\t"
    "ds_write_b128 v106, a[84:87] offset:5120\n\t"
    "buffer_load_dwordx4 v[68:71], v117, s[80:83], s58 offen offset:1024 sc1\n\t"
    "v_mfma_f32_4x4x4_16b_bf16 v[188:191], v[4:5], a[4:5], v[188:191]\n\t"
    "v_mfma_f32_4x4x4_16b_bf16 v[196:199], v[22:23], a[30:31], v[196:199]\n\t"
    "s_waitcnt vmcnt(22)\n\t"
    "v_mfma_f32_4x4x4_16b_bf16 v[204:207], v[40:41], v[164:165], v[204:207]\n\t"
    "v_mfma_f32_4x4x4_16b_bf16 v[192:195], v[4:5], a[12:13], v[192:195]\n\t"
    "v_mfma_f32_4x4x4_16b_bf16 v[200:203], v[22:23], a[38:39], v[200:203]\n\t"
    "ds_write_b128 v106, a[88:91] offset:6144\n\t"
    "buffer_load_dwordx4 v[72:75], v117, s[80:83], s58 offen offset:2048 sc1\n\t"
    "v_mfma_f32_4x4x4_16b_bf16 v[188:191], v[6:7], a[6:7], v[188:191]\n\t"
    "v_mfma_f32_4x4x4_16b_bf16 v[196:199], v[24:25], a[32:33], v[196:199]\n\t"
    "v_mfma_f32_4x4x4_16b_bf16 v[204:207], v[42:43], v[166:167], v[204:207]\n\t"
    "v_mfma_f32_4x4x4_16b_bf16 v[192:195], v[6:7], a[14:15], v[192:195]\n\t"
    "v_mfma_f32_4x4x4_16b_bf16 v[200:203], v[24:25], a[40:41], v[200:203]\n\t"
    "ds_write_b128 v106, a[92:95] offset:7168\n\t"
    "buffer_load_dwordx4 v[76:79], v117, s[80:83], s58 offen offset:3072 sc1\n\t"
    "v_mfma_f32_4x4x4_16b_bf16 v[188:191], v[8:9], a[8:9], v[188:191]\n\t"
    "v_mfma_f32_4x4x4_16b_bf16 v[196:199], v[26:27], a[34:35], v[196:199]\n\t"
    "s_waitcnt vmcnt(23)\n\t"
    "v_mfma_f32_4x4x4_16b_bf16 v[204:207], v[44:45], v[168:169], v[204:207]\n\t"
    "v_mfma_f32_4x4x4_16b_bf16 v[192:195], v[8:9], a[16:17], v[192:195]\n\t"
    "v_mfma_f32_4x4x4_16b_bf16 v[200:203], v[26:27], a[42:43], v[200:203]\n\t"
    "ds_write_b128 v106, a[96:99] offset:40960\n\t"
    "buffer_load_dwordx4 v[80:83], v117, s[80:83], s72 offen offset:0 sc1\n\t"
    "v_mfma_f32_4x4x4_16b_bf16 v[188:191], v[10:11], a[10:11], v[188:191]\n\t"
    "v_mfma_f32_4x4x4_16b_bf16 v[196:199], v[28:29], a[36:37], v[196:199]\n\t"
    "v_mfma_f32_4x4x4_16b_bf16 v[204:207], v[46:47], v[170:171], v[204:207]\n\t"
    "s_waitcnt lgkmcnt(14)\n\t"
    "v_mfma_f32_4x4x4_16b_bf16 v[192:195], v[10:11], a[18:19], v[192:195]\n\t"
    "v_mfma_f32_4x4x4_16b_bf16 v[200:203], v[28:29], a[44:45], v[200:203]\n\t"
    "ds_write_b128 v106, a[100:103] offset:41984\n\t"
    "buffer_load_dwordx4 v[84:87], v117, s[80:83], s72 offen offset:1024 sc1\n\t"
    "v_mfma_f32_4x4x4_16b_bf16 v[188:191], v[12:13], a[12:13], v[188:191]\n\t"
    "v_mfma_f32_4x4x4_16b_bf16 v[196:199], v[30:31], a[38:39], v[196:199]\n\t"
    "s_waitcnt vmcnt(24)\n\t"
    "v_mfma_f32_4x4x4_16b_bf16 v[204:207], v[48:49], v[172:173], v[204:207]\n\t"
    "v_mfma_f32_4x4x4_16b_bf16 v[192:195], v[12:13], a[20:21], v[192:195]\n\t"
    "v_mfma_f32_4x4x4_16b_bf16 v[200:203], v[30:31], a[46:47], v[200:203]\n\t"
    "ds_write_b128 v106, a[104:107] offset:43008\n\t"
    "buffer_load_dwordx4 v[88:91], v117, s[80:83], s72 offen offset:2048 sc1\n\t"
    "v_mfma_f32_4x4x4_16b_bf16 v[188:191], v[14:15], a[14:15], v[188:191]\n\t"
    "v_mfma_f32_4x4x4_16b_bf16 v[196:199], v[32:33], a[40:41], v[196:199]\n\t"
    "v_mfma_f32_4x4x4_16b_bf16 v[204:207], v[50:51], v[174:175], v[204:207]\n\t"
    "s_waitcnt lgkmcnt(14)\n\t"
    "v_mfma_f32_4x4x4_16b_bf16 v[192:195], v[14:15], a[22:23], v[192:195]\n\t"
    "v_mfma_f32_4x4x4_16b_bf16 v[200:203], v[32:33], a[48:49], v[200:203]\n\t"
    "ds_write_b128 v106, a[108:111] offset:44032\n\t"
    "buffer_load_dwordx4 v[92:95], v117, s[80:83], s72 offen offset:3072 sc1\n\t"
    "v_mfma_f32_4x4x4_16b_bf16 v[188:191], v[16:17], a[16:17], v[188:191]\n\t"
    "v_mfma_f32_4x4x4_16b_bf16 v[196:199], v[34:35], a[42:43], v[196:199]\n\t"
    "s_waitcnt vmcnt(25)\n\t"
    "v_mfma_f32_4x4x4_16b_bf16 v[204:207], v[52:53], v[176:177], v[204:207]\n\t"
    "s_waitcnt lgkmcnt(14)\n\t"
    "v_mfma_f32_4x4x4_16b_bf16 v[192:195], v[16:17], a[24:25], v[192:195]\n\t"
    "s_waitcnt lgkmcnt(13)\n\t"
    "v_mfma_f32_4x4x4_16b_bf16 v[200:203], v[34:35], a[50:51], v[200:203]\n\t"
    "ds_write_b128 v107, a[112:115] offset:4096\n\t"
    "buffer_load_dwordx4 v[96:99], v117, s[80:83], s73 offen offset:0 sc1\n\t"
    "ds_write_b128 v107, a[116:119] offset:5120\n\t"
    "s_waitcnt lgkmcnt(14)\n\t"
    "v_lshlrev_b32 v0, 16, v178\n\t"
    "v_and_b32 v1, 0xffff0000, v178\n\t"
    "v_lshlrev_b32 v2, 16, v179\n\t"
    "v_and_b32 v3, 0xffff0000, v179\n\t"
    "s_waitcnt lgkmcnt(13)\n\t"
    "v_lshlrev_b32 v4, 16, v180\n\t"
    "v_and_b32 v5, 0xffff0000, v180\n\t"
    "v_lshlrev_b32 v6, 16, v181\n\t"
    "v_and_b32 v7, 0xffff0000, v181\n\t"
    "ds_write_b128 v107, a[120:123] offset:6144\n\t"
    "s_waitcnt lgkmcnt(13)\n\t"
    "v_lshlrev_b32 v8, 16, v182\n\t"
    "v_and_b32 v9, 0xffff0000, v182\n\t"
    "v_lshlrev_b32 v10, 16, v183\n\t"
    "v_and_b32 v11, 0xffff0000, v183\n\t"
    "s_waitcnt lgkmcnt(12)\n\t"
    "v_lshlrev_b32 v12, 16, v184\n\t"
    "v_and_b32 v13, 0xffff0000, v184\n\t"
    "v_lshlrev_b32 v14, 16, v185\n\t"
    "v_and_b32 v15, 0xffff0000, v185\n\t"
    "ds_write_b128 v107, a[124:127] offset:7168\n\t"
    "s_waitcnt lgkmcnt(12)\n\t"
    "v_lshlrev_b32 v16, 16, v186\n\t"
    "v_and_b32 v17, 0xffff0000, v186\n\t"
    "v_lshlrev_b32_e64 v18, 16, v187\n\t"
    "v_and_b32 v19, 0xffff0000, v187\n\t"
    "v_pk_mul_f32 v[188:189], v[188:189], v[0:1]\n\t"
    "v_pk_mul_f32 v[190:191], v[190:191], v[2:3]\n\t"
    "v_pk_mul_f32 v[192:193], v[192:193], v[4:5]\n\t"
    "v_pk_mul_f32 v[194:195], v[194:195], v[6:7]\n\t"
    "ds_write_b128 v108, a[136:139] offset:2048\n\t"
    "v_pk_mul_f32 v[196:197], v[196:197], v[8:9]\n\t"
    "v_pk_mul_f32 v[198:199], v[198:199], v[10:11]\n\t"
    "v_pk_mul_f32 v[200:201], v[200:201], v[12:13]\n\t"
    "v_pk_mul_f32 v[202:203], v[202:203], v[14:15]\n\t"
    "v_pk_mul_f32 v[204:205], v[204:205], v[16:17]\n\t"
    "v_pk_mul_f32 v[206:207], v[206:207], v[18:19]\n\t"
    "v_cvt_pk_bf16_f32 v20, v188, v189\n\t"
    "v_cvt_pk_bf16_f32 v21, v190, v191\n\t"
    "ds_write_b128 v108, a[140:143] offset:3072\n\t"
    "v_cvt_pk_bf16_f32 v22, v192, v193\n\t"
    "v_cvt_pk_bf16_f32 v23, v194, v195\n\t"
    "v_cvt_pk_bf16_f32 v24, v196, v197\n\t"
    "v_cvt_pk_bf16_f32 v25, v198, v199\n\t"
    "v_cvt_pk_bf16_f32 v26, v200, v201\n\t"
    "v_cvt_pk_bf16_f32 v27, v202, v203\n\t"
    "v_cvt_pk_bf16_f32 v28, v204, v205\n\t"
    "v_cvt_pk_bf16_f32 v29, v206, v207\n\t"
    "ds_write_b64 v123, v[20:21] offset:19456\n\t"
    "ds_write_b64 v123, v[22:23] offset:24320\n\t"
    "ds_write_b64 v123, v[24:25] offset:19968\n\t"
    "ds_write_b64 v123, v[26:27] offset:24832\n\t"
    "s_cmp_lt_u32 s60, 2\n\t"
    "s_cbranch_scc0 EL1_%=\n\t"
    "ds_write_b64 v124, v[28:29] offset:19456\n\t"
    "EL1_%=:\n\t"
    "v_accvgpr_read_b32 v144, a64\n\t"
    "v_accvgpr_read_b32 v145, a65\n\t"
    "v_accvgpr_read_b32 v146, a66\n\t"
    "v_accvgpr_read_b32 v147, a67\n\t"
    "v_accvgpr_read_b32 v148, a68\n\t"
    "v_accvgpr_read_b32 v149, a69\n\t"
    "v_accvgpr_read_b32 v150, a70\n\t"
    "v_accvgpr_read_b32 v151, a71\n\t"
    "v_accvgpr_read_b32 v152, a72\n\t"
    "v_accvgpr_read_b32 v153, a73\n\t"
    "v_accvgpr_read_b32 v154, a74\n\t"
    "v_accvgpr_read_b32 v155, a75\n\t"
    "v_accvgpr_read_b32 v156, a76\n\t"
    "v_accvgpr_read_b32 v157, a77\n\t"
    "v_accvgpr_read_b32 v158, a78\n\t"
    "v_accvgpr_read_b32 v159, a79\n\t"
    "v_accvgpr_write_b32 a234, 0\n\t"
    "v_accvgpr_write_b32 a235, 0\n\t"
    "v_accvgpr_write_b32 a54, 0\n\t"
    "v_accvgpr_write_b32 a55, 0\n\t"
    "v_lshlrev_b32 v16, 16, v62\n\t"
    "v_and_b32 v17, 0xffff0000, v62\n\t"
    "v_lshlrev_b32 v18, 16, v63\n\t"
    "v_and_b32 v19, 0xffff0000, v63\n\t"
    "s_waitcnt lgkmcnt(0)\n\t"
    "s_barrier\n\t"
    "ds_read_b64_tr_b16 a[0:1], v125 offset:0\n\t"
    "ds_read_b64_tr_b16 a[2:3], v125 offset:32\n\t"
    "ds_read_b64_tr_b16 a[4:5], v125 offset:128\n\t"
    "ds_read_b64_tr_b16 a[6:7], v125 offset:160\n\t"
    "ds_read_b64_tr_b16 a[8:9], v125 offset:256\n\t"
    "ds_read_b64_tr_b16 a[10:11], v125 offset:288\n\t"
    "ds_read_b64_tr_b16 a[12:13], v125 offset:384\n\t"
    "ds_read_b64_tr_b16 a[14:15], v125 offset:416\n\t"
    "ds_read_b64_tr_b16 a[16:17], v125 offset:512\n\t"
    "ds_read_b64_tr_b16 a[18:19], v125 offset:544\n\t"
    "v_accvgpr_write_b32 a82, 0\n\t"
    "v_accvgpr_write_b32 a83, 0\n\t"
    "buffer_load_dwordx4 v[32:35], v113, s[80:83], s53 offen offset:0 sc1\n\t"
    "buffer_load_dwordx4 v[36:39], v113, s[80:83], s53 offen offset:1024 sc1\n\t"
    "buffer_load_dwordx2 v[40:41], v114, s[80:83], s53 offen sc1\n\t"
    "s_waitcnt vmcnt(28) lgkmcnt(8)\n\t"
    "v_mfma_f32_32x32x16_bf16 v[128:143], a[144:147], a[0:3], v[128:143]\n\t"
    "buffer_load_dwordx4 a[64:67], v117, s[80:83], s59 offen offset:0 sc1\n\t"
    "ds_read_b64_tr_b16 a[20:21], v125 offset:640\n\t"
    "ds_read_b64_tr_b16 a[22:23], v125 offset:672\n\t"
    "ds_read_b64_tr_b16 a[24:25], v125 offset:768\n\t"
    "ds_read_b64_tr_b16 a[26:27], v125 offset:800\n\t"
    "s_waitcnt vmcnt(28) lgkmcnt(10)\n\t"
    "v_mfma_f32_32x32x16_bf16 v[128:143], a[148:151], a[4:7], v[128:143]\n\t"
    "buffer_load_dwordx4 a[68:71], v117, s[80:83], s59 offen offset:1024 sc1\n\t"
    "ds_read_b64_tr_b16 a[28:29], v125 offset:896\n\t"
    "ds_read_b64_tr_b16 a[30:31], v125 offset:928\n\t"
    "ds_read_b64_tr_b16 a[32:33], v125 offset:1024\n\t"
    "ds_read_b64_tr_b16 a[34:35], v125 offset:1056\n\t"
    "s_waitcnt vmcnt(28) lgkmcnt(12)\n\t"
    "v_mfma_f32_32x32x16_bf16 v[128:143], a[152:155], a[8:11], v[128:143]\n\t"
    "buffer_load_dwordx4 a[72:75], v117, s[80:83], s59 offen offset:2048 sc1\n\t"
    "ds_read_b128 v[160:163], v109 offset:4096\n\t"
    "ds_read_b128 v[164:167], v109 offset:5120\n\t"
    "ds_read_b128 v[168:171], v109 offset:6144\n\t"
    "ds_read_b128 v[172:175], v109 offset:7168\n\t"
    "s_waitcnt vmcnt(28) lgkmcnt(14)\n\t"
    "v_mfma_f32_32x32x16_bf16 v[128:143], a[156:159], a[12:15], v[128:143]\n\t"
    "buffer_load_dwordx4 a[76:79], v117, s[80:83], s59 offen offset:3072 sc1\n\t"
    "ds_read_b128 v[176:179], v110 offset:4096\n\t"
    "ds_read_b128 v[180:183], v110 offset:5120\n\t"
    "s_waitcnt vmcnt(28) lgkmcnt(14)\n\t"
    "v_mfma_f32_32x32x16_bf16 v[128:143], a[160:163], a[16:19], v[128:143]\n\t"
    "buffer_load_dwordx2 a[80:81], v116, s[80:83], s76 offen sc1\n\t"
    "ds_read_b128 v[184:187], v110 offset:6144\n\t"
    "ds_read_b128 v[188:191], v110 offset:7168\n\t"
    "s_waitcnt vmcnt(28) lgkmcnt(14)\n\t"
    "v_mfma_f32_32x32x16_bf16 v[128:143], a[164:167], a[20:23], v[128:143]\n\t"
    "ds_read_b64_tr_b16 a[36:37], v126 offset:0\n\t"
    "ds_read_b64_tr_b16 a[38:39], v126 offset:32\n\t"
    "ds_read_b64_tr_b16 a[40:41], v126 offset:256\n\t"
    "ds_read_b64_tr_b16 a[42:43], v126 offset:288\n\t"
    "s_waitcnt vmcnt(27) lgkmcnt(14)\n\t"
    "v_mfma_f32_32x32x16_bf16 v[128:143], a[168:171], a[24:27], v[128:143]\n\t"
    "ds_read_b64_tr_b16 a[44:45], v126 offset:512\n\t"
    "ds_read_b64_tr_b16 a[46:47], v126 offset:544\n\t"
    "ds_read_b64_tr_b16 a[48:49], v126 offset:768\n\t"
    "ds_read_b64_tr_b16 a[50:51], v126 offset:800\n\t"
    "ds_read_b64_tr_b16 a[52:53], v127 offset:1024\n\t"
    "s_waitcnt vmcnt(26)\n\t"
    "v_mfma_f32_32x32x16_bf16 v[128:143], a[172:175], a[28:31], v[128:143]\n\t"
    "ds_read_b128 v[192:195], v111 offset:4096\n\t"
    "ds_read_b128 v[196:199], v111 offset:5120\n\t"
    "s_waitcnt lgkmcnt(14)\n\t"
    "v_add_f32 v144, v144, v160\n\t"
    "v_add_f32 v145, v145, v161\n\t"
    "v_add_f32 v146, v146, v162\n\t"
    "v_add_f32 v147, v147, v163\n\t"
    "s_waitcnt vmcnt(25)\n\t"
    "v_mfma_f32_32x32x16_bf16 v[128:143], a[176:179], a[32:35], v[128:143]\n\t"
    "ds_read_b128 v[200:203], v111 offset:6144\n\t"
    "ds_read_b128 v[204:207], v111 offset:7168\n\t"
    "v_add_f32 v148, v148, v164\n\t"
    "v_add_f32 v149, v149, v165\n\t"
    "v_add_f32 v150, v150, v166\n\t"
    "v_add_f32_e64 v151, v151, v167\n\t"
    "s_waitcnt vmcnt(24) lgkmcnt(11)\n\t"
    "v_mfma_f32_16x16x32_bf16 v[16:19], a[216:219], a[36:39], v[16:19]\n\t"
    "ds_read_b128 v[208:211], v112 offset:2048\n\t"
    "v_add_f32 v152, v152, v168\n\t"
    "s_waitcnt vmcnt(23) lgkmcnt(10)\n\t"
    "v_mfma_f32_16x16x32_bf16 v[16:19], a[220:223], a[40:43], v[16:19]\n\t"
    "ds_read_b128 v[4:7], v112 offset:6144\n\t"
    "v_add_f32 v153, v153, v169\n\t"
    "s_waitcnt vmcnt(22) lgkmcnt(9)\n\t"
    "v_mfma_f32_16x16x32_bf16 v[16:19], a[224:227], a[44:47], v[16:19]\n\t"
    "ds_read_b128 v[8:11], v112 offset:10240\n\t"
    "v_add_f32 v154, v154, v170\n\t"
    "v_add_f32_e64 v155, v155, v171\n\t"
    "s_waitcnt vmcnt(21) lgkmcnt(8)\n\t"
    "v_mfma_f32_16x16x32_bf16 v[16:19], a[228:231], a[48:51], v[16:19]\n\t"
    "ds_read_b128 v[12:15], v112 offset:14336\n\t"
    "v_add_f32 v156, v156, v172\n\t"
    "v_add_f32 v157, v157, v173\n\t"
    "s_waitcnt vmcnt(20) lgkmcnt(8)\n\t"
    "v_mfma_f32_16x16x32_bf16 v[16:19], a[232:235], a[52:55], v[16:19]\n\t"
    "v_add_f32 v158, v158, v174\n\t"
    "v_add_f32_e64 v159, v159, v175\n\t"
    "v_pk_add_f32 v[144:145], v[144:145], v[176:177]\n\t"
    "v_pk_add_f32 v[146:147], v[146:147], v[178:179]\n\t"
    "v_pk_add_f32 v[148:149], v[148:149], v[180:181]\n\t"
    "v_pk_add_f32 v[150:151], v[150:151], v[182:183]\n\t"
    "v_pk_add_f32 v[152:153], v[152:153], v[184:185]\n\t"
    "v_pk_add_f32 v[154:155], v[154:155], v[186:187]\n\t"
    "v_pk_add_f32 v[156:157], v[156:157], v[188:189]\n\t"
    "v_pk_add_f32 v[158:159], v[158:159], v[190:191]\n\t"
    "s_waitcnt lgkmcnt(7)\n\t"
    "v_pk_add_f32 v[144:145], v[144:145], v[192:193]\n\t"
    "v_pk_add_f32 v[146:147], v[146:147], v[194:195]\n\t"
    "s_waitcnt lgkmcnt(6)\n\t"
    "v_pk_add_f32 v[148:149], v[148:149], v[196:197]\n\t"
    "v_pk_add_f32 v[150:151], v[150:151], v[198:199]\n\t"
    "s_waitcnt lgkmcnt(5)\n\t"
    "v_pk_add_f32 v[152:153], v[152:153], v[200:201]\n\t"
    "v_pk_add_f32 v[154:155], v[154:155], v[202:203]\n\t"
    "s_waitcnt lgkmcnt(4)\n\t"
    "v_pk_add_f32 v[156:157], v[156:157], v[204:205]\n\t"
    "v_pk_add_f32 v[158:159], v[158:159], v[206:207]\n\t"
    "s_waitcnt lgkmcnt(2)\n\t"
    "v_pk_add_f32 v[208:209], v[208:209], v[4:5]\n\t"
    "v_pk_add_f32 v[210:211], v[210:211], v[6:7]\n\t"
    "s_waitcnt lgkmcnt(1)\n\t"
    "v_pk_add_f32 v[208:209], v[208:209], v[8:9]\n\t"
    "v_pk_add_f32 v[210:211], v[210:211], v[10:11]\n\t"
    "s_waitcnt lgkmcnt(0)\n\t"
    "v_pk_add_f32 v[208:209], v[208:209], v[12:13]\n\t"
    "v_pk_add_f32 v[210:211], v[210:211], v[14:15]\n\t"
    "v_add_u32 v123, 110592, v117\n\t"
    "s_waitcnt vmcnt(5)\n\t"
    "s_mov_b32 s95, 0\n\t"
    "RX_%=:\n\t"
    "v_lshlrev_b32 v160, 16, v32\n\t"
    "v_and_b32 v161, 0xffff0000, v32\n\t"
    "v_lshlrev_b32 v162, 16, v33\n\t"
    "v_and_b32 v163, 0xffff0000, v33\n\t"
    "v_lshlrev_b32 v164, 16, v34\n\t"
    "v_and_b32 v165, 0xffff0000, v34\n\t"
    "v_lshlrev_b32_e64 v166, 16, v35\n\t"
    "v_and_b32 v167, 0xffff0000, v35\n\t"
    "v_pk_add_f32 v[160:161], v[144:145], v[160:161]\n\t"
    "v_pk_add_f32 v[162:163], v[146:147], v[162:163]\n\t"
    "v_pk_add_f32 v[164:165], v[148:149], v[164:165]\n\t"
    "v_pk_add_f32 v[166:167], v[150:151], v[166:167]\n\t"
    "v_cvt_pk_bf16_f32 v180, v160, v161\n\t"
    "v_cvt_pk_bf16_f32 v181, v162, v163\n\t"
    "v_cvt_pk_bf16_f32 v182, v164, v165\n\t"
    "v_cvt_pk_bf16_f32 v183, v166, v167\n\t"
    "ds_write_b128 v240, v[180:183] offset:9216\n\t"
    "v_lshlrev_b32 v168, 16, v36\n\t"
    "v_and_b32 v169, 0xffff0000, v36\n\t"
    "v_lshlrev_b32 v170, 16, v37\n\t"
    "v_and_b32 v171, 0xffff0000, v37\n\t"
    "v_lshlrev_b32 v172, 16, v38\n\t"
    "v_and_b32 v173, 0xffff0000, v38\n\t"
    "v_lshlrev_b32 v174, 16, v39\n\t"
    "v_and_b32 v175, 0xffff0000, v39\n\t"
    "v_pk_add_f32 v[168:169], v[152:153], v[168:169]\n\t"
    "v_pk_add_f32 v[170:171], v[154:155], v[170:171]\n\t"
    "v_pk_add_f32 v[172:173], v[156:157], v[172:173]\n\t"
    "v_pk_add_f32 v[174:175], v[158:159], v[174:175]\n\t"
    "v_cvt_pk_bf16_f32 v184, v168, v169\n\t"
    "v_cvt_pk_bf16_f32 v185, v170, v171\n\t"
    "v_cvt_pk_bf16_f32 v186, v172, v173\n\t"
    "v_cvt_pk_bf16_f32 v187, v174, v175\n\t"
    "ds_write_b128 v240, v[184:187] offset:9472\n\t"
    "v_lshlrev_b32 v176, 16, v40\n\t"
    "v_and_b32 v177, 0xffff0000, v40\n\t"
    "v_lshlrev_b32 v178, 16, v41\n\t"
    "v_and_b32 v179, 0xffff0000, v41\n\t"
    "v_pk_add_f32 v[176:177], v[208:209], v[176:177]\n\t"
    "v_pk_add_f32 v[178:179], v[210:211], v[178:179]\n\t"
    "v_cvt_pk_bf16_f32 v188, v176, v177\n\t"
    "v_cvt_pk_bf16_f32 v189, v178, v179\n\t"
    "ds_write_b64 v120, v[188:189] offset:9216\n\t"
    "v_pk_add_f32 v[0:1], v[160:161], v[162:163]\n\t"
    "v_pk_add_f32 v[2:3], v[168:169], v[170:171]\n\t"
    "v_pk_add_f32 v[0:1], v[0:1], v[164:165]\n\t"
    "v_pk_add_f32 v[2:3], v[2:3], v[172:173]\n\t"
    "v_pk_add_f32 v[0:1], v[0:1], v[166:167]\n\t"
    "v_pk_add_f32 v[2:3], v[2:3], v[174:175]\n\t"
    "v_pk_add_f32 v[4:5], v[176:177], v[178:179]\n\t"
    "v_add_f32 v0, v0, v1\n\t"
    "v_add_f32 v2, v2, v3\n\t"
    "v_add_f32 v4, v4, v5\n\t"
    "v_add_f32 v6, v0, v2\n\t"
    "v_cmp_u_f32 vcc, v6, v4\n\t"
    "v_mov_b32 v1, v0\n\t"
    "v_mov_b32 v3, v2\n\t"
    "v_mov_b32 v5, v4\n\t"
    "v_permlane16_swap_b32 v1, v0\n\t"
    "v_permlane16_swap_b32 v3, v2\n\t"
    "v_permlane16_swap_b32 v5, v4\n\t"
    "v_add_f32 v0, v0, v1\n\t"
    "v_add_f32 v2, v2, v3\n\t"
    "v_add_f32 v4, v4, v5\n\t"
    "v_mov_b32 v1, v0\n\t"
    "v_mov_b32 v3, v2\n\t"
    "v_mov_b32 v5, v4\n\t"
    "v_permlane32_swap_b32 v1, v0\n\t"
    "v_permlane32_swap_b32 v3, v2\n\t"
    "v_permlane32_swap_b32 v5, v4\n\t"
    "v_add_f32 v0, v0, v1\n\t"
    "v_add_f32 v2, v2, v3\n\t"
    "v_add_f32_e64 v4, v4, v5\n\t"
    "ds_write_b32 v100, v0 offset:0\n\t"
    "ds_write_b32 v100, v2 offset:256\n\t"
    "ds_write_b32 v244, v4 offset:2048\n\t"
    "s_cbranch_vccnz RXS_%=\n\t"
    "RXD_%=:\n\t"
    "s_waitcnt lgkmcnt(0)\n\t"
    "s_barrier\n\t"
    "ds_read_b128 a[0:3], v123 offset:0\n\t"
    "ds_read_b128 a[4:7], v123 offset:1024\n\t"
    "ds_read_b128 a[8:11], v123 offset:2048\n\t"
    "ds_read_b128 a[12:15], v123 offset:3072\n\t"
    "ds_read_b128 a[16:19], v123 offset:4096\n\t"
    "ds_read_b128 a[20:23], v123 offset:5120\n\t"
    "ds_read_b128 a[24:27], v123 offset:6144\n\t"
    "ds_read_b128 a[28:31], v123 offset:7168\n\t"
    "ds_read_b128 a[32:35], v119 offset:17408\n\t"
    "s_waitcnt lgkmcnt(8)\n\t"
    "v_mfma_f32_32x32x16_bf16 v[160:175], v[64:67], a[0:3], 0\n\t"
    "ds_read_b128 a[36:39], v241 offset:0\n\t"
    "ds_read_b128 a[40:43], v241 offset:2048\n\t"
    "ds_read_b128 a[44:47], v241 offset:4096\n\t"
    "s_waitcnt lgkmcnt(10)\n\t"
    "v_mfma_f32_32x32x16_bf16 v[160:175], v[68:71], a[4:7], v[160:175]\n\t"
    "ds_read_b128 a[48:51], v241 offset:6144\n\t"
    "ds_read_b64 a[52:53], v120 offset:9216\n\t"
    "ds_read_b32 v0, v243 offset:0\n\t"
    "s_waitcnt lgkmcnt(12)\n\t"
    "v_mfma_f32_32x32x16_bf16 v[160:175], v[72:75], a[8:11], v[160:175]\n\t"
    "ds_read_b32 v1, v243 offset:512\n\t"
    "ds_read_b32 v2, v243 offset:1024\n\t"
    "ds_read_b32 v3, v243 offset:1536\n\t"
    "s_waitcnt lgkmcnt(14)\n\t"
    "v_mfma_f32_32x32x16_bf16 v[160:175], v[76:79], a[12:15], v[160:175]\n\t"
    "ds_read_b32 v4, v243 offset:2048\n\t"
    "ds_read_b32 v8, v244 offset:0\n\t"
    "ds_read_b32 v9, v244 offset:512\n\t"
    "s_waitcnt lgkmcnt(14)\n\t"
    "v_mfma_f32_32x32x16_bf16 v[160:175], v[80:83], a[16:19], v[160:175]\n\t"
    "ds_read_b32 v10, v244 offset:1024\n\t"
    "ds_read_b32 v11, v244 offset:1536\n\t"
    "ds_read_b32 v12, v244 offset:2048\n\t"
    "s_waitcnt vmcnt(4) lgkmcnt(14)\n\t"
    "v_mfma_f32_16x16x32_bf16 v[176:179], a[64:67], a[36:39], 0\n\t"
    "s_waitcnt lgkmcnt(8)\n\t"
    "v_add_f32 v0, v0, v1\n\t"
    "s_waitcnt vmcnt(3)\n\t"
    "v_mfma_f32_16x16x32_bf16 v[176:179], a[68:71], a[40:43], v[176:179]\n\t"
    "s_waitcnt lgkmcnt(6)\n\t"
    "v_add_f32 v2, v2, v3\n\t"
    "s_waitcnt vmcnt(2)\n\t"
    "v_mfma_f32_16x16x32_bf16 v[176:179], a[72:75], a[44:47], v[176:179]\n\t"
    "s_waitcnt lgkmcnt(3)\n\t"
    "v_add_f32 v8, v8, v9\n\t"
    "s_waitcnt vmcnt(1)\n\t"
    "v_mfma_f32_16x16x32_bf16 v[176:179], a[76:79], a[48:51], v[176:179]\n\t"
    "s_waitcnt lgkmcnt(1)\n\t"
    "v_add_f32 v10, v10, v11\n\t"
    "s_waitcnt vmcnt(0)\n\t"
    "v_mfma_f32_16x16x32_bf16 v[176:179], a[80:83], a[52:55], v[176:179]\n\t"
    "v_add_f32 v0, v0, v4\n\t"
    "v_mfma_f32_32x32x16_bf16 v[160:175], v[84:87], a[20:23], v[160:175]\n\t"
    "s_waitcnt lgkmcnt(0)\n\t"
    "v_add_f32 v8, v8, v12\n\t"
    "v_add_f32 v0, v0, v2\n\t"
    "v_add_f32 v8, v8, v10\n\t"
    "v_rcp_f32 v2, v0\n\t"
    "v_rcp_f32 v10, v8\n\t"
    "v_mfma_f32_32x32x16_bf16 v[160:175], v[88:91], a[24:27], v[160:175]\n\t"
    "v_and_b32 v14, 8, v121\n\t"
    "v_add_u32 v14, v14, v121\n\t"
    "v_mov_b32 v6, 0x358637bd\n\t"
    "v_mov_b32 v7, 0x7149f2ca\n\t"
    "v_mul_f32 v2, 0x42900000, v2\n\t"
    "v_mfma_f32_32x32x16_bf16 v[160:175], v[92:95], a[28:31], v[160:175]\n\t"
    "v_mul_f32 v10, 0x42900000, v10\n\t"
    "v_cmp_nlt_f32 vcc, v6, v0\n\t"
    "v_cmp_nlt_f32 s[46:47], v0, v7\n\t"
    "s_nop 0\n\t"
    "s_or_b64 vcc, vcc, s[46:47]\n\t"
    "s_or_b32 s94, vcc_lo, vcc_hi\n\t"
    "v_mfma_f32_32x32x16_bf16 v[160:175], v[96:99], a[32:35], v[160:175]\n\t"
    "v_fma_f32 v176, v176, v10, v16\n\t"
    "v_fma_f32 v177, v177, v10, v17\n\t"
    "v_fma_f32 v178, v178, v10, v18\n\t"
    "v_fma_f32 v179, v179, v10, v19\n\t"
    "v_cvt_pk_bf16_f32 v40, v176, v177\n\t"
    "v_cvt_pk_bf16_f32 v41, v178, v179\n\t"
    "s_cmp_lt_u32 s60, 2\n\t"
    "s_cbranch_scc0 EL2_%=\n\t"
    "buffer_store_dwordx2 v[40:41], v122, s[88:91], 0 offen\n\t"
    "EL2_%=:\n\t"
    "s_nop 3\n\t"
    "v_pk_fma_f32 v[160:161], v[160:161], v[2:3], v[128:129] op_sel_hi:[1,0,1]\n\t"
    "v_pk_fma_f32 v[162:163], v[162:163], v[2:3], v[130:131] op_sel_hi:[1,0,1]\n\t"
    "v_pk_fma_f32 v[164:165], v[164:165], v[2:3], v[132:133] op_sel_hi:[1,0,1]\n\t"
    "v_pk_fma_f32 v[166:167], v[166:167], v[2:3], v[134:135] op_sel_hi:[1,0,1]\n\t"
    "v_cvt_pk_bf16_f32 v32, v160, v161\n\t"
    "v_cvt_pk_bf16_f32 v34, v164, v165\n\t"
    "v_cvt_pk_bf16_f32 v33, v162, v163\n\t"
    "v_cvt_pk_bf16_f32 v35, v166, v167\n\t"
    "v_permlane32_swap_b32 v32, v34\n\t"
    "s_nop 0\n\t"
    "v_permlane32_swap_b32 v33, v35\n\t"
    "buffer_store_dwordx4 v[32:35], v14, s[88:91], 0 offen offset:0\n\t"
    "v_pk_fma_f32 v[168:169], v[168:169], v[2:3], v[136:137] op_sel_hi:[1,0,1]\n\t"
    "v_pk_fma_f32 v[170:171], v[170:171], v[2:3], v[138:139] op_sel_hi:[1,0,1]\n\t"
    "v_pk_fma_f32 v[172:173], v[172:173], v[2:3], v[140:141] op_sel_hi:[1,0,1]\n\t"
    "v_pk_fma_f32 v[174:175], v[174:175], v[2:3], v[142:143] op_sel_hi:[1,0,1]\n\t"
    "v_cvt_pk_bf16_f32 v36, v168, v169\n\t"
    "v_cvt_pk_bf16_f32 v38, v172, v173\n\t"
    "v_cvt_pk_bf16_f32 v37, v170, v171\n\t"
    "v_cvt_pk_bf16_f32 v39, v174, v175\n\t"
    "v_permlane32_swap_b32 v36, v38\n\t"
    "s_nop 0\n\t"
    "v_permlane32_swap_b32 v37, v39\n\t"
    "buffer_store_dwordx4 v[36:39], v14, s[88:91], 0 offen offset:32\n\t"
    "s_cmp_eq_u32 s94, 0\n\t"
    "s_cbranch_scc1 END_%=\n\t"
    "ds_write_b128 v101, a[180:183] offset:0\n\t"
    "ds_write_b128 v101, a[184:187] offset:1024\n\t"
    "ds_write_b128 v102, a[188:191] offset:2048\n\t"
    "ds_write_b128 v102, a[192:195] offset:3072\n\t"
    "ds_write_b128 v103, a[196:199] offset:0\n\t"
    "ds_write_b128 v103, a[200:203] offset:1024\n\t"
    "ds_write_b128 v104, a[204:207] offset:2048\n\t"
    "ds_write_b128 v104, a[208:211] offset:3072\n\t"
    "ds_write_b128 v105, a[212:215] offset:0\n\t"
    "v_readfirstlane_b32 s92, v234\n\t"
    "v_readfirstlane_b32 s93, v235\n\t"
    "s_waitcnt lgkmcnt(0)\n\t"
    "s_nop 4\n\t"
    "s_branch END_%=\n\t"
    "PFS0_%=:\n\t"
    "s_mov_b32 s95, 0\n\t"
    "PFL0_%=:\n\t"
    "s_sleep 1\n\t"
    "buffer_load_dwordx2 v[232:233], v115, s[80:83], 0 offen sc1\n\t"
    "s_waitcnt vmcnt(0)\n\t"
    "v_cmp_ne_u64 vcc, v[232:233], v[234:235]\n\t"
    "s_nop 1\n\t"
    "s_add_u32 s95, s95, 1\n\t"
    "s_cmp_lt_u32 s95, 0x10000\n\t"
    "s_cselect_b64 vcc, vcc, 0\n\t"
    "s_nop 4\n\t"
    "s_cbranch_vccnz PFL0_%=\n\t"
    "s_branch PFD0_%=\n\t"
    "PFS1_%=:\n\t"
    "s_mov_b32 s95, 0\n\t"
    "PFL1_%=:\n\t"
    "s_sleep 1\n\t"
    "buffer_load_dwordx2 v[232:233], v115, s[80:83], 0 offen sc1\n\t"
    "s_waitcnt vmcnt(0)\n\t"
    "v_cmp_ne_u64 vcc, v[232:233], v[234:235]\n\t"
    "s_nop 1\n\t"
    "s_add_u32 s95, s95, 1\n\t"
    "s_cmp_lt_u32 s95, 0x10000\n\t"
    "s_cselect_b64 vcc, vcc, 0\n\t"
    "s_nop 4\n\t"
    "s_cbranch_vccnz PFL1_%=\n\t"
    "s_branch PFD1_%=\n\t"
    "RXS_%=:\n\t"
    "v_cmp_eq_u32 vcc, v32, v228\n\t"
    "v_cmp_eq_u32 s[46:47], v33, v228\n\t"
    "s_or_b64 vcc, vcc, s[46:47]\n\t"
    "v_cmp_eq_u32 s[46:47], v34, v228\n\t"
    "s_or_b64 vcc, vcc, s[46:47]\n\t"
    "v_cmp_eq_u32 s[46:47], v35, v228\n\t"
    "s_or_b64 vcc, vcc, s[46:47]\n\t"
    "v_cmp_eq_u32 s[46:47], v36, v228\n\t"
    "s_or_b64 vcc, vcc, s[46:47]\n\t"
    "v_cmp_eq_u32 s[46:47], v37, v228\n\t"
    "s_or_b64 vcc, vcc, s[46:47]\n\t"
    "v_cmp_eq_u32 s[46:47], v38, v228\n\t"
    "s_or_b64 vcc, vcc, s[46:47]\n\t"
    "v_cmp_eq_u32 s[46:47], v39, v228\n\t"
    "s_or_b64 vcc, vcc, s[46:47]\n\t"
    "v_cmp_eq_u32 s[46:47], v40, v228\n\t"
    "s_or_b64 vcc, vcc, s[46:47]\n\t"
    "v_cmp_eq_u32 s[46:47], v41, v228\n\t"
    "s_or_b64 vcc, vcc, s[46:47]\n\t"
    "s_add_u32 s95, s95, 1\n\t"
    "s_cmp_lt_u32 s95, 0x10000\n\t"
    "s_cselect_b64 vcc, vcc, 0\n\t"
    "s_nop 4\n\t"
    "s_cbranch_vccz RXD_%=\n\t"
    "buffer_load_dwordx4 v[32:35], v113, s[80:83], s53 offen offset:0 sc1\n\t"
    "buffer_load_dwordx4 v[36:39], v113, s[80:83], s53 offen offset:1024 sc1\n\t"
    "buffer_load_dwordx2 v[40:41], v114, s[80:83], s53 offen sc1\n\t"
    "s_waitcnt vmcnt(0) lgkmcnt(0)\n\t"
    "s_branch RX_%=\n\t"
    "END_%=:\n\t"
    :  "={s[92:95]}"(sr), "={v[128:143]}"(cac), "={v[16:19]}"(csc)
    : "{s[6:7]}"(__builtin_amdgcn_kernarg_segment_ptr()), "{s8}"(bid), "{s60}"(w)
    : "memory", "scc", "vcc", "a0", "a1", "a2", "a3", "a4", "a5", "a6", "a7", "a8", "a9", "a10", "a11", "a12", "a13", "a14", "a15", "a16", "a17", "a18", "a19", "a20", "a21", "a22", "a23", "a24", "a25", "a26", "a27", "a28", "a29", "a30", "a31", "a32", "a33", "a34", "a35", "a36", "a37", "a38", "a39", "a40", "a41", "a42", "a43", "a44", "a45", "a46", "a47", "a48", "a49", "a50", "a51", "a52", "a53", "a54", "a55", "a56", "a57", "a58", "a59", "a60", "a61", "a62", "a63", "a64", "a65", "a66", "a67", "a68", "a69", "a70", "a71", "a72", "a73", "a74", "a75", "a76", "a77", "a78", "a79", "a80", "a81", "a82", "a83", "a84", "a85", "a86", "a87", "a88", "a89", "a90", "a91", "a92", "a93", "a94", "a95", "a96", "a97", "a98", "a99", "a100", "a101", "a102", "a103", "a104", "a105", "a106", "a107", "a108", "a109", "a110", "a111", "a112", "a113", "a114", "a115", "a116", "a117", "a118", "a119", "a120", "a121", "a122", "a123", "a124", "a125", "a126", "a127", "a128", "a129", "a130", "a131", "a132", "a133", "a134", "a135", "a136", "a137", "a138", "a139", "a140", "a141", "a142", "a143", "a144", "a145", "a146", "a147", "a148", "a149", "a150", "a151", "a152", "a153", "a154", "a155", "a156", "a157", "a158", "a159", "a160", "a161", "a162", "a163", "a164", "a165", "a166", "a167", "a168", "a169", "a170", "a171", "a172", "a173", "a174", "a175", "a176", "a177", "a178", "a179", "a180", "a181", "a182", "a183", "a184", "a185", "a186", "a187", "a188", "a189", "a190", "a191", "a192", "a193", "a194", "a195", "a196", "a197", "a198", "a199", "a200", "a201", "a202", "a203", "a204", "a205", "a206", "a207", "a208", "a209", "a210", "a211", "a212", "a213", "a214", "a215", "a216", "a217", "a218", "a219", "a220", "a221", "a222", "a223", "a224", "a225", "a226", "a227", "a228", "a229", "a230", "a231", "a232", "a233", "a234", "a235", "a236", "a237", "a238", "a239", "a240", "a241", "a242", "a243", "a244", "a245", "a246", "a247", "a248", "a249", "a250", "a251", "v0", "v1", "v2", "v3", "v4", "v5", "v6", "v7", "v8", "v9", "v10", "v11", "v12", "v13", "v14", "v15", "v20", "v21", "v22", "v23", "v24", "v25", "v26", "v27", "v28", "v29", "v30", "v31", "v32", "v33", "v34", "v35", "v36", "v37", "v38", "v39", "v40", "v41", "v42", "v43", "v44", "v45", "v46", "v47", "v48", "v49", "v50", "v51", "v52", "v53", "v54", "v55", "v56", "v57", "v58", "v59", "v60", "v61", "v62", "v63", "v64", "v65", "v66", "v67", "v68", "v69", "v70", "v71", "v72", "v73", "v74", "v75", "v76", "v77", "v78", "v79", "v80", "v81", "v82", "v83", "v84", "v85", "v86", "v87", "v88", "v89", "v90", "v91", "v92", "v93", "v94", "v95", "v96", "v97", "v98", "v99", "v100", "v101", "v102", "v103", "v104", "v105", "v106", "v107", "v108", "v109", "v110", "v111", "v112", "v113", "v114", "v115", "v116", "v117", "v118", "v119", "v120", "v121", "v122", "v123", "v124", "v125", "v126", "v127", "v144", "v145", "v146", "v147", "v148", "v149", "v150", "v151", "v152", "v153", "v154", "v155", "v156", "v157", "v158", "v159", "v160", "v161", "v162", "v163", "v164", "v165", "v166", "v167", "v168", "v169", "v170", "v171", "v172", "v173", "v174", "v175", "v176", "v177", "v178", "v179", "v180", "v181", "v182", "v183", "v184", "v185", "v186", "v187", "v188", "v189", "v190", "v191", "v192", "v193", "v194", "v195", "v196", "v197", "v198", "v199", "v200", "v201", "v202", "v203", "v204", "v205", "v206", "v207", "v208", "v209", "v210", "v211", "v212", "v213", "v214", "v215", "v216", "v217", "v218", "v219", "v220", "v221", "v228", "v229", "v230", "v231", "v232", "v233", "v234", "v235", "v236", "v237", "v238", "v239", "v240", "v241", "v242", "v243", "v244", "v245", "v246", "v247", "v248", "v249", "v250", "v251", "v252", "s9", "s10", "s11", "s12", "s13", "s14", "s15", "s16", "s17", "s18", "s19", "s20", "s21", "s22", "s23", "s24", "s25", "s26", "s27", "s28", "s29", "s30", "s31", "s33", "s34", "s35", "s36", "s37", "s38", "s39", "s40", "s41", "s42", "s43", "s44", "s45", "s46", "s47", "s48", "s49", "s50", "s51", "s52", "s53", "s54", "s55", "s56", "s57", "s58", "s59", "s61", "s62", "s63", "s64", "s65", "s66", "s67", "s68", "s69", "s70", "s71", "s72", "s73", "s74", "s75", "s76", "s77", "s78", "s79", "s80", "s81", "s82", "s83", "s84", "s85", "s86", "s87", "s88", "s89", "s90", "s91");
  }
  if (__builtin_expect(sr[2] != 0u && tile < NT, 0)) {
    LANEVARS(fresh_lane());
    const u64 tag = ((u64)sr[1] << 32) | sr[0];
#pragma unroll 1
    for (int k = 0; k < 2; ++k) { const int i = 64 * k + lane; waitflag(ws + WS_FK + 128 * (b * 128 + (i < NT ? i : 0)), tag); }
    const rsrc_t rk = mkrs(ws + WS_KT + (size_t)(b * NT) * TILEB, NT * TILEB);
    u32x4 qf[9];
#pragma unroll
    for (int s = 0; s < 9; ++s) qf[s] = ld16(L + 2048 + 1024 * s + lane * 16);
    float mx = -3.0e38f;
#pragma unroll 1
    for (int t = w; t < NT; t += 4) { f32x16 sv = {};
#pragma unroll
      for (int s = 0; s < 9; ++s) sv = mm32(bld16(rk, t * TILEB + aoff(r, 2 * s + h)), qf[s], sv);
#pragma unroll
      for (int e = 0; e < 16; ++e) mx = __builtin_fmaxf(mx, sv[e]); }
    LAS float* st = (LAS float*)(L + L_U);
    st[64 * w + lane] = mx;
    BAR();
#pragma unroll
    for (int v = 0; v < 8; ++v) mx = __builtin_fmaxf(mx, st[32 * v + r]);
    lptr pv = L + L_SLOT + 4096 * w;
    const int chl = 2 * (g & 1) + (p >> 1);
    f32x16 ob = {}; f32x4 sm = {};
#pragma unroll 1
    for (int t = 0; t < NT; ++t) { f32x16 sv;
#pragma unroll
      for (int e = 0; e < 16; ++e) sv[e] = -mx;
#pragma unroll
      for (int s = 0; s < 9; ++s) sv = mm32(bld16(rk, t * TILEB + aoff(r, 2 * s + h)), qf[s], sv);
      st16(pv + lane * 16, bld16(rk, t * TILEB + 2048 * w + lane * 16)); st16(pv + 1024 + lane * 16, bld16(rk, t * TILEB + 2048 * w + 1024 + lane * 16));
      st16(pv + 2048 + lane * 16, bld16(rk, t * TILEB + 8192 + lane * 16));
#pragma unroll
      for (int e = 0; e < 16; ++e) sv[e] = __builtin_amdgcn_exp2f(sv[e]);
      u32x4 p0 = {cvtpk(sv[0], sv[1]), cvtpk(sv[2], sv[3]), cvtpk(sv[8], sv[9]), cvtpk(sv[10], sv[11])};
      u32x4 p1 = {cvtpk(sv[4], sv[5]), cvtpk(sv[6], sv[7]), cvtpk(sv[12], sv[13]), cvtpk(sv[14], sv[15])};
      asm volatile("s_waitcnt lgkmcnt(0)" ::: "memory");
      ob = mm32(ldtr2(pv + aoff(4 * h + q, chl) + 8 * (p & 1), 256), p0, ob);
      ob = mm32(ldtr2(pv + aoff(8 + 4 * h + q, chl) + 8 * (p & 1), 256), p1, ob);
      u32x4 ps;
#pragma unroll
      for (int i = 0; i < 4; ++i) { auto sw = __builtin_amdgcn_permlane16_swap(p0[i], p1[i], false, false); ps[i] = z ? sw[1] : sw[0]; }
      sm = mm16(ldtr2(pv + 2048 + aoff(8 * (g & 1) + 4 * (g >> 1) + q, p >> 1) + 8 * (p & 1), 256), ps, sm);
      asm volatile("s_waitcnt lgkmcnt(0)" ::: "memory");
    }
    LAS float* rsum = (LAS float*)(L + 9216);
    { float s = 0.f;
#pragma unroll
      for (int e = 0; e < 16; ++e) s += ob[e];
#pragma unroll
      for (int ks = 0; ks < 2; ++ks) { const int o = 4 * ks;
        st16(L + 1024 * (2 * w + ks) + lane * 16, u32x4{cvtpk(ob[o], ob[o + 1]), cvtpk(ob[o + 2], ob[o + 3]), cvtpk(ob[o + 8], ob[o + 9]), cvtpk(ob[o + 10], ob[o + 11])}); }
      st8(L + 512 * (16 + (g >> 1)) + 16 * tk + 8 * (g & 1), pk4(sm[0], sm[1], sm[2], sm[3]));
      rsum[8 * r + w] = xhalf(s); rsum[8 * tk + 4] = xhalf(xrow((sm[0] + sm[1]) + (sm[2] + sm[3]))); }
    BAR();
    auto tot = [&](int t_) { f32x4 a = *(LAS f32x4*)(rsum + 8 * t_); return (a[0] + a[1]) + (a[2] + a[3]) + rsum[8 * t_ + 4]; };
    u32x4 wa[9], wvs[5], obf[9], obs[5];
#pragma unroll
    for (int s = 0; s < 9; ++s) { wa[s] = bld16(rw, WS_WV + TILEB * w + 1024 * s + lane * 16); obf[s] = ld16(L + 1024 * s + lane * 16); }
#pragma unroll
    for (int s = 0; s < 4; ++s) { wvs[s] = bld16(rw, WS_WV + 36864 + 1024 * s + lane * 16); obs[s] = ld16(L + 512 * (4 * s + g) + 16 * tk); }
    { u32x2 t = bld8(rw, WS_WV + 40960 + lane * 8); wvs[4] = u32x4{t[0], t[1], 0u, 0u}; }
    obs[4] = ld8z(L + 512 * (16 + (g >> 1)) + 16 * tk + 8 * (g & 1));
    const float il = (D * BETA) / tot(r), il2 = (D * BETA) / tot(tk);
    f32x16 vac; f32x4 vsc;
    mmul<false>(wa, obf, wvs, obs, vac, vsc);
    const rsrc_t ro = mkrs(out + (size_t)gt * 32 * D, TILEB);
    VMW(0);
    __builtin_amdgcn_raw_buffer_store_b64(pk4(csc[0] + vsc[0] * il2, csc[1] + vsc[1] * il2, csc[2] + vsc[2] * il2, csc[3] + vsc[3] * il2), ro, w < 2 ? 288 * tk + 256 + 8 * g : OOB, 0, 0);
#pragma unroll
    for (int a = 0; a < 4; ++a)
      __builtin_amdgcn_raw_buffer_store_b64(pk4(cac[4 * a] + vac[4 * a] * il, cac[4 * a + 1] + vac[4 * a + 1] * il, cac[4 * a + 2] + vac[4 * a + 2] * il, cac[4 * a + 3] + vac[4 * a + 3] * il), ro, 288 * r + 64 * w + 16 * a + 8 * h, 0, 0);
  }
}

extern "C" void kernel_launch(void* const* d_in, const int* in_sizes, int n_in, void* d_out, int out_size, void* d_ws, size_t ws_size, hipStream_t stream) {
  (void)hipFuncSetAttribute((const void*)branchformer, hipFuncAttributeMaxDynamicSharedMemorySize, 163840);
  branchformer<<<256, 256, 163840, stream>>>((const u16*)d_in[0], (const u16*)d_in[1], (const u16*)d_in[2], (const u16*)d_in[3], (const u16*)d_in[4], (const u16*)d_in[5],
                                             (const u16*)d_in[6], (const u16*)d_in[7], (const u16*)d_in[8], (char*)d_ws, (u16*)d_out);
}
```
